```python
import torch

def _softmax(x, axis=-1):
    return torch.softmax(x, dim=axis)
def _gelu(x):
    return torch.nn.functional.gelu(x, approximate="tanh")
def _silu(x):
    return torch.nn.functional.silu(x)
def _sigmoid(x):
    return torch.sigmoid(x)
def _layernorm(x, axis=-1, eps=1e-5):
    m = x.mean(dim=axis, keepdim=True)
    v = x.var(dim=axis, keepdim=True, unbiased=False)
    return (x - m) / torch.sqrt(v + eps)
def _scatter_add(tgt, idx, src, axis=0):
    return tgt.clone().index_add_(axis, idx, src)
def _segment_sum(src, seg, n):
    out = torch.zeros((n,) + src.shape[1:], dtype=src.dtype)
    return out.index_add_(0, seg, src)
def _pad(x, pw):
    flat = tuple(v for pair in reversed(pw) for v in pair)
    return torch.nn.functional.pad(x, flat)

kernel_name = 'standalone__detection_seg_mask2former__vision'

def setup_inputs():

    _w = (lambda a: (lambda t: t.to(torch.bfloat16) if t.is_floating_point() else t)(torch.as_tensor(a)))
    H = W = 512
    C = 192
    Hc, Wc = H // 2, W // 2
    nh = 4
    Nq = 16
    sc = C ** -0.5
    return {
        "q": _w((lambda *s: torch.randn(*s, dtype=torch.bfloat16) * 0.4)(1, Nq, C)),
        "f_hi": _w((lambda *s: torch.randn(*s, dtype=torch.bfloat16) * 0.4)(1, H, W, C)),
        "f_lo": _w((lambda *s: torch.randn(*s, dtype=torch.bfloat16) * 0.4)(1, Hc, Wc, C)),
        "w_lat": _w((lambda *s: torch.randn(*s, dtype=torch.bfloat16) * 0.4)(C, C) * sc),
        "w_q": _w((lambda *s: torch.randn(*s, dtype=torch.bfloat16) * 3.2)(nh, C, C // nh) * sc),
        "w_k": _w((lambda *s: torch.randn(*s, dtype=torch.bfloat16) * 3.2)(nh, C, C // nh) * sc),
        "w_v": _w((lambda *s: torch.randn(*s, dtype=torch.bfloat16) * 3.2)(nh, C, C // nh) * sc),
        "w_o": _w((lambda *s: torch.randn(*s, dtype=torch.bfloat16) * 3.2)(C, C) * sc),
        "w_m": _w((lambda *s: torch.randn(*s, dtype=torch.bfloat16) * 3.2)(C, C) * sc),
    }

def reference(q, f_hi, f_lo, w_lat, w_q, w_k, w_v, w_o, w_m):
    B, Nq, C = q.shape
    _, H, W, _ = f_hi.shape
    nh = w_q.shape[0]
    dh = C // nh
    up = (lambda x, n, axis: torch.repeat_interleave(x, n, dim=axis))((lambda x, n, axis: torch.repeat_interleave(x, n, dim=axis))(f_lo, 2, axis=1), 2, axis=2)
    feat = f_hi + up @ w_lat
    feat = _layernorm(feat, axis=-1)
    kv = feat.reshape(B, H * W, C)
    m_embed = q @ w_m
    mask_bias = torch.einsum('bqc,bkc->bqk', m_embed, kv) * (C ** -0.5)
    Q = torch.einsum('bqc,hcd->bhqd', q, w_q)
    K = torch.einsum('bkc,hcd->bhkd', kv, w_k)
    V = torch.einsum('bkc,hcd->bhkd', kv, w_v)
    logits = torch.einsum('bhqd,bhkd->bhqk', Q, K) * (dh ** -0.5)
    logits = logits + mask_bias[:, None, :, :]
    attn = _softmax(logits, axis=-1)
    ctx = torch.einsum('bhqk,bhkd->bqhd', attn, V).reshape(B, Nq, C)
    return q + ctx @ w_o
```

```cpp
#include <hip/hip_runtime.h>
#include <stdint.h>

typedef __attribute__((ext_vector_type(8))) __bf16 bf16x8;
typedef __attribute__((ext_vector_type(2))) __bf16 bf16x2;
typedef __attribute__((ext_vector_type(4))) short s16x4;
typedef __attribute__((ext_vector_type(4))) float f32x4;
typedef __attribute__((ext_vector_type(2))) float f32x2;
typedef __attribute__((ext_vector_type(4))) unsigned u32x4;
typedef __attribute__((ext_vector_type(2))) unsigned u32x2;
typedef __attribute__((address_space(3))) char lds_char;
typedef __attribute__((address_space(3))) s16x4 lds_s16x4;
typedef __attribute__((address_space(3))) u32x4 lds_u32x4;
typedef __attribute__((address_space(3))) f32x4 lds_f32x4;
typedef __attribute__((address_space(3))) float lds_f32;

#define DEV __device__ __forceinline__

constexpr int LDS_WL = 0;
constexpr int LDS_X = 73728;
constexpr int LDS_R = 122880;
constexpr int LDS_M = 123392;
constexpr int LDS_TOTAL = 135936;
constexpr float THR = 16.0f;

DEV unsigned cvtpk(float lo, float hi) {
  f32x2 v = {lo, hi};
  return __builtin_bit_cast(unsigned, __builtin_convertvector(v, bf16x2));
}
DEV u32x4 cvt8(f32x4 a, f32x4 b) { return u32x4{cvtpk(a[0], a[1]), cvtpk(a[2], a[3]), cvtpk(b[0], b[1]), cvtpk(b[2], b[3])}; }
DEV f32x4 mfma32(u32x4 a, u32x4 b, f32x4 c) {
  return __builtin_amdgcn_mfma_f32_16x16x32_bf16(__builtin_bit_cast(bf16x8, a), __builtin_bit_cast(bf16x8, b), c, 0, 0, 0);
}
DEV f32x4 mfma16(u32x2 a, u32x2 b, f32x4 c) {
  return __builtin_amdgcn_mfma_f32_16x16x16bf16_1k(__builtin_bit_cast(s16x4, a), __builtin_bit_cast(s16x4, b), c, 0, 0, 0);
}
DEV u32x2 trread(lds_char* p) {
  return __builtin_bit_cast(u32x2, __builtin_amdgcn_ds_read_tr16_b64_v4i16((lds_s16x4*)p));
}
template <int CTRL, int BANK> DEV unsigned dppu(unsigned old, unsigned src) {
  return (unsigned)__builtin_amdgcn_update_dpp((int)old, (int)src, CTRL, 0xf, BANK, false);
}
template <int CTRL, int BANK> DEV float dppf(float old, float src) {
  return __builtin_bit_cast(float, dppu<CTRL, BANK>(__builtin_bit_cast(unsigned, old), __builtin_bit_cast(unsigned, src)));
}
constexpr int DPP_ID = 0xE4, DPP_ROR8 = 0x128;
DEV float xg_sum(float x) {
  auto s = __builtin_amdgcn_permlane16_swap(__float_as_uint(x), __float_as_uint(x), false, false);
  x = __uint_as_float(s[0]) + __uint_as_float(s[1]);
  auto t = __builtin_amdgcn_permlane32_swap(__float_as_uint(x), __float_as_uint(x), false, false);
  return __uint_as_float(t[0]) + __uint_as_float(t[1]);
}
DEV float xg_max(float x) {
  auto s = __builtin_amdgcn_permlane16_swap(__float_as_uint(x), __float_as_uint(x), false, false);
  x = fmaxf(__uint_as_float(s[0]), __uint_as_float(s[1]));
  auto t = __builtin_amdgcn_permlane32_swap(__float_as_uint(x), __float_as_uint(x), false, false);
  return fmaxf(__uint_as_float(t[0]), __uint_as_float(t[1]));
}
DEV void bar() { asm volatile("s_waitcnt lgkmcnt(0)\n\ts_barrier" ::: "memory"); }
typedef __amdgpu_buffer_rsrc_t rsrc_t;
DEV rsrc_t mkrsrc(const void* p, unsigned bytes) { return __builtin_amdgcn_make_buffer_rsrc((void*)p, (short)0, (int)bytes, 0x00020000); }
template <int AUX> DEV u32x4 bld4(rsrc_t r, unsigned voff, unsigned soff) { return __builtin_amdgcn_raw_buffer_load_b128(r, voff, soff, AUX); }
template <int AUX> DEV u32x2 bld2(rsrc_t r, unsigned voff, unsigned soff) { return __builtin_amdgcn_raw_buffer_load_b64(r, voff, soff, AUX); }
constexpr int NT = 2;

struct WSlice { u32x4 w4[12]; u32x2 w2[12]; };
template <int RS> DEV void load_slice(WSlice& w, rsrc_t r, unsigned soff, int lane) {
  const int m = lane & 15, g = lane >> 4;
  const unsigned row = 8 * (m >> 2) + (m & 3);
  const unsigned o4 = row * RS + 16 * g, o2 = row * RS + 64 + 8 * g;
#pragma unroll
  for (int t = 0; t < 12; ++t) {
    const unsigned so = soff + (32 * (t >> 1) + 4 * (t & 1)) * RS;
    w.w4[t] = bld4<0>(r, o4, so);
    w.w2[t] = bld2<0>(r, o2, so);
  }
}
DEV void proj_slice(const WSlice& w, const u32x4 (&qf)[6], u32x2 I16, f32x4 (&R)[3]) {
  const f32x4 z = {0.f, 0.f, 0.f, 0.f};
  R[0] = z; R[1] = z; R[2] = z;
#pragma unroll
  for (int s = 0; s < 6; ++s) {
    f32x4 T[3][2];
#pragma unroll
    for (int u = 0; u < 2; ++u) {
      const u32x4 a = w.w4[2 * s + u];
      T[0][u] = mfma16(u32x2{a[0], a[1]}, I16, z);
      T[1][u] = mfma16(u32x2{a[2], a[3]}, I16, z);
      T[2][u] = mfma16(w.w2[2 * s + u], I16, z);
    }
#pragma unroll
    for (int dt = 0; dt < 3; ++dt) R[dt] = mfma32(cvt8(T[dt][0], T[dt][1]), qf[s], R[dt]);
  }
}

struct Prod {
  u32x4 ring[4][6];
  u32x4 lo[6];
  f32x4 lat[6][2];
};

__global__ void __launch_bounds__(512, 2)
m2f_main(const void* __restrict__ q_, const void* __restrict__ fhi_, const void* __restrict__ flo_, const void* __restrict__ wlat_,
         const void* __restrict__ wq_, const void* __restrict__ wk_, const void* __restrict__ wv_, const void* __restrict__ wm_,
         float* __restrict__ pctx, float* __restrict__ pml) {
  __shared__ __attribute__((aligned(16))) char smem_[LDS_TOTAL];
  lds_char* smem = (lds_char*)smem_;
  const int tid = threadIdx.x, lane = tid & 63;
  const int w = __builtin_amdgcn_readfirstlane(tid >> 6);
  const int i = lane & 15, g = lane >> 4;
  const int b = blockIdx.x;
  const f32x4 z4 = {0.f, 0.f, 0.f, 0.f};
  u32x2 I16;
  {
    const int d = i - 4 * g;
    I16[0] = d == 0 ? 0x00003F80u : (d == 1 ? 0x3F800000u : 0u);
    I16[1] = d == 2 ? 0x00003F80u : (d == 3 ? 0x3F800000u : 0u);
  }

  if (w < 4) {
    const int p = w;
    const rsrc_t rhi = mkrsrc(fhi_, 100663296u), rlo = mkrsrc(flo_, 25165824u), rwl = mkrsrc(wlat_, 73728u), rwv = mkrsrc(wv_, 73728u);
    const unsigned hf = i >> 3, i7 = i & 7;
    const unsigned ca = (4 * hf + g) * 16, cb = (4 * (1 - hf) + g) * 16;
    const unsigned voLa = i7 * 384 + ca, voLb = (8 + i7) * 384 + cb;
    const unsigned voHa = i7 * 768 + ca, voHb = (8 + i7) * 768 + cb;
    const unsigned soL = (unsigned)(b * 256 + p * 64) * 384u;
    const unsigned soH = (unsigned)(b * 1024 + p * 128) * 384u;
    Prod P;
    auto ld_lo = [&](int u) {
#pragma unroll
      for (int L = 0; L < 3; ++L) {
        P.lo[L] = bld4<NT>(rlo, voLa + L * 128, soL + u * 6144);
        P.lo[3 + L] = bld4<NT>(rlo, voLb + L * 128, soL + u * 6144);
      }
    };
    auto ld_hi = [&](int u, int t) {
      const unsigned so = soH + u * 12288 + (t >> 1) * 196608;
#pragma unroll
      for (int L = 0; L < 3; ++L) {
        P.ring[t][L] = bld4<NT>(rhi, voHa + L * 128 + (t & 1) * 384, so);
        P.ring[t][3 + L] = bld4<NT>(rhi, voHb + L * 128 + (t & 1) * 384, so);
      }
    };
    {
      u32x4 wst[18];
#pragma unroll
      for (int n = 0; n < 18; ++n) wst[n] = bld4<0>(rwl, lane * 16 + (n % 3) * 1024, p * 18432 + (n / 3) * 3072);
      ld_lo(0);
#pragma unroll
      for (int t = 0; t < 4; ++t) ld_hi(0, t);
      unsigned wa[3];
#pragma unroll
      for (int j = 0; j < 3; ++j) {
        const unsigned o = j * 1024 + lane * 16, k = o / 384, ch = (o % 384) / 16;
        wa[j] = p * 18432 + k * 384 + ((ch ^ (4 * ((k >> 1) & 1))) * 16);
      }
#pragma unroll
      for (int n = 0; n < 18; ++n) {
        const u32x4 v = wst[n];
        *(lds_u32x4*)(smem + LDS_WL + wa[n % 3] + (n / 3) * 3072) = ((n / 3) & 1) ? u32x4{v[2], v[3], v[0], v[1]} : v;
      }
    }
    unsigned wb[2][2];
    {
      const unsigned rp = i >> 2, cp = i & 3;
#pragma unroll
      for (int sp = 0; sp < 2; ++sp)
#pragma unroll
        for (int u = 0; u < 2; ++u) wb[sp][u] = (8 * g + rp) * 384 + (8 * (sp ^ (rp >> 1)) + 2 * cp + (u ^ (g & 1))) * 8;
    }
    const unsigned xwa = (16 * p + i7) * 128 + (((4 * hf + g) ^ i7) * 16);
    const unsigned xwb = (16 * p + 8 + i7) * 128 + (((4 * (1 - hf) + g) ^ i7) * 16);
    const unsigned one2 = 0x3F803F80u;
    const unsigned oL = hf ? 0u : one2, oH = hf ? one2 : 0u;
    const u32x4 onesLo = {oL, oL, oL, oL}, onesHi = {oH, oH, oH, oH};
    const bool rvalid = ((g & 1) == (int)(i >> 2));
    const unsigned rw = LDS_R + (16 * p + 8 * (g >> 1) + i7) * 4;
    const int i3 = i & 3;
    bar();

    auto burst = [&]() {
      u32x4 B[6];
#pragma unroll
      for (int L = 0; L < 3; ++L)
#pragma unroll
        for (int r = 0; r < 4; ++r) {
          const unsigned A_ = P.lo[L][r], B_ = P.lo[3 + L][r];
          B[2 * L + 1][r] = dppu<DPP_ROR8, 0xC>(dppu<DPP_ROR8, 0x3>(A_, A_), B_);
          B[2 * L][r] = dppu<DPP_ID, 0xC>(A_, B_);
        }
#pragma unroll
      for (int L = 0; L < 3; ++L) {
        f32x4 T[2][2] = {{z4, z4}, {z4, z4}};
#pragma unroll
        for (int ks = 0; ks < 6; ++ks)
#pragma unroll
          for (int sp = 0; sp < 2; ++sp)
#pragma unroll
            for (int u = 0; u < 2; ++u) {
              lds_char* a = smem + LDS_WL + wb[sp][u] + ks * 12288 + L * 128;
              const u32x2 a0 = trread(a), a1 = trread(a + 1536);
              T[sp][u] = mfma32(u32x4{a0[0], a0[1], a1[0], a1[1]}, B[ks], T[sp][u]);
            }
#pragma unroll
        for (int u = 0; u < 2; ++u)
#pragma unroll
          for (int r = 0; r < 4; ++r) {
            P.lat[L][u][r] = dppf<DPP_ROR8, 0xC>(T[0][u][r], T[1][u][r]);
            P.lat[3 + L][u][r] = dppf<DPP_ROR8, 0x3>(T[0][u][r], T[1][u][r]);
          }
      }
    };
    auto tile = [&](int t) {
      lds_char* xb = smem + LDS_X + (t & 1) * 24576;
      f32x4 G[2] = {z4, z4}, S = z4;
#pragma unroll
      for (int n = 0; n < 6; ++n) {
        const int ab = n / 3, L = n % 3;
        const u32x4 F = P.ring[t][n];
        const f32x4 d0 = mfma16(I16, u32x2{F[0], F[1]}, P.lat[n][0]);
        const f32x4 d1 = mfma16(I16, u32x2{F[2], F[3]}, P.lat[n][1]);
        const u32x4 x = cvt8(d0, d1);
        *(lds_u32x4*)(xb + (ab ? xwb : xwa) + L * 8192) = x;
        G[ab] = mfma32(x, x, G[ab]);
        S = mfma32(ab ? onesHi : onesLo, x, S);
      }
      float va = G[0][0], vb = G[1][0];
      va = i3 == 1 ? G[0][1] : va; vb = i3 == 1 ? G[1][1] : vb;
      va = i3 == 2 ? G[0][2] : va; vb = i3 == 2 ? G[1][2] : vb;
      va = i3 == 3 ? G[0][3] : va; vb = i3 == 3 ? G[1][3] : vb;
      auto sw = __builtin_amdgcn_permlane32_swap(__float_as_uint(va), __float_as_uint(vb), false, false);
      const float y1 = __uint_as_float(sw[1]);
      const float ss = __uint_as_float(sw[0]) + dppf<DPP_ROR8, 0xF>(y1, y1);
      const float sm = S[0] + dppf<DPP_ROR8, 0xF>(S[0], S[0]);
      const float mu = sm * (1.0f / 192.0f);
      const float var = fmaxf(ss * (1.0f / 192.0f) - mu * mu, 0.0f);
      const float rs = __builtin_amdgcn_rsqf(var + 1e-5f);
      if (rvalid) *(lds_f32*)(smem + rw + (t & 1) * 256) = rs;
    };

    for (int u = 0; u < 3; ++u) {
      burst();
      ld_lo(u + 1);
#pragma unroll
      for (int t = 0; t < 4; ++t) {
        tile(t);
        ld_hi(u + 1, t);
        bar();
      }
    }
    burst();
    tile(0); bar();
    u32x4 wst[18];
#pragma unroll
    for (int n = 0; n < 12; ++n) wst[n] = bld4<0>(rwv, lane * 16 + (n % 4) * 1024, p * 18432 + (n / 4) * 4096);
    tile(1); bar();
#pragma unroll
    for (int n = 12; n < 18; ++n) wst[n] = bld4<0>(rwv, lane * 16 + (n % 4) * 1024, p * 18432 + (n / 4) * 4096);
    tile(2); bar();
    tile(3); bar();
#pragma unroll
    for (int n = 0; n < 18; ++n) *(lds_u32x4*)(smem + LDS_WL + p * 18432 + lane * 16 + n * 1024) = wst[n];
    bar();
  } else {
    const int h = w - 4;
    const rsrc_t rq = mkrsrc(q_, 6144u), rwq = mkrsrc(wq_, 73728u), rwk = mkrsrc(wk_, 73728u), rwm = mkrsrc(wm_, 73728u);
    u32x4 Ab[6];
    {
      u32x4 qf[6];
#pragma unroll
      for (int s = 0; s < 6; ++s) qf[s] = bld4<0>(rq, i * 384 + g * 16 + s * 64, 0);
      WSlice wm, wq;
      load_slice<384>(wm, rwm, h * 96, lane);
      load_slice<96>(wq, rwq, h * 18432, lane);
      bar();
      f32x4 R[3];
      proj_slice(wm, qf, I16, R);
      lds_char* mw = smem + LDS_M + i * 784 + h * 192;
      *(lds_f32x4*)(mw + g * 32) = R[0];
      *(lds_f32x4*)(mw + g * 32 + 16) = R[1];
      *(lds_f32x4*)(mw + 128 + g * 16) = R[2];
      WSlice wk;
      load_slice<96>(wk, rwk, h * 18432, lane);
      proj_slice(wq, qf, I16, R);
      const u32x4 Bq0 = cvt8(R[0], R[1]);
      const u32x4 Bq1 = {cvtpk(R[2][0], R[2][1]), cvtpk(R[2][2], R[2][3]), 0u, 0u};
      f32x4 AT[12];
#pragma unroll
      for (int t = 0; t < 12; ++t) AT[t] = mfma32(u32x4{wk.w2[t][0], wk.w2[t][1], 0u, 0u}, Bq1, mfma32(wk.w4[t], Bq0, z4));
      bar();
      const float LOG2E = 1.4426950408889634f;
      const float s1 = LOG2E * 0.14433756729740643f, s2 = LOG2E * 0.07216878364870322f;
      float sum = 0.f;
#pragma unroll
      for (int t = 0; t < 12; ++t) {
        const f32x4 mv = *(lds_f32x4*)(smem + LDS_M + i * 784 + g * 32 + (t >> 1) * 128 + (t & 1) * 16);
#pragma unroll
        for (int r = 0; r < 4; ++r) { AT[t][r] = AT[t][r] * s1 + mv[r] * s2; sum += AT[t][r]; }
      }
      const float mean = xg_sum(sum) * (1.0f / 192.0f);
#pragma unroll
      for (int s = 0; s < 6; ++s) Ab[s] = cvt8(AT[2 * s] - mean, AT[2 * s + 1] - mean);
    }
    const unsigned xr0 = i * 128 + ((g ^ (i & 7)) * 16), xr1 = i * 128 + (((4 + g) ^ (i & 7)) * 16);
    unsigned xt[4];
    {
      const unsigned rp = i >> 2, cp = i & 3, row = 4 * g + rp;
#pragma unroll
      for (int c = 0; c < 4; ++c) xt[c] = row * 128 + (((2 * c + (cp >> 1)) ^ (row & 7)) * 16) + (cp & 1) * 8;
    }
    f32x4 U[12];
#pragma unroll
    for (int c = 0; c < 12; ++c) U[c] = z4;
    float mrun = -1e30f, lsum = 0.f;
#pragma unroll 2
    for (int t = 0; t < 16; ++t) {
      lds_char* xb = smem + LDS_X + (t & 1) * 24576;
      f32x4 S[4], rr[4];
#pragma unroll
      for (int pt = 0; pt < 4; ++pt) {
        rr[pt] = *(lds_f32x4*)(smem + LDS_R + (t & 1) * 256 + pt * 64 + g * 16);
        S[pt] = z4;
#pragma unroll
        for (int s = 0; s < 6; ++s)
          S[pt] = mfma32(*(lds_u32x4*)(xb + ((s & 1) ? xr1 : xr0) + (s >> 1) * 8192 + pt * 2048), Ab[s], S[pt]);
      }
      f32x4 V[4];
      float vmax = -3e38f;
#pragma unroll
      for (int pt = 0; pt < 4; ++pt)
#pragma unroll
        for (int r = 0; r < 4; ++r) { V[pt][r] = S[pt][r] * rr[pt][r] - mrun; vmax = fmaxf(vmax, V[pt][r]); }
      if (__builtin_amdgcn_ballot_w64(!(vmax <= THR)) != 0) {
        float tmax = -3e38f;
#pragma unroll
        for (int pt = 0; pt < 4; ++pt)
#pragma unroll
          for (int r = 0; r < 4; ++r) { V[pt][r] = S[pt][r] * rr[pt][r]; tmax = fmaxf(tmax, V[pt][r]); }
        const float mnew = fmaxf(mrun, xg_max(tmax));
        const float al = __builtin_amdgcn_exp2f(mrun - mnew);
        mrun = mnew; lsum *= al;
#pragma unroll
        for (int c = 0; c < 12; ++c) U[c] *= al;
#pragma unroll
        for (int pt = 0; pt < 4; ++pt) V[pt] -= mnew;
      }
      u32x4 Pb[2];
#pragma unroll
      for (int pt = 0; pt < 4; ++pt) {
        f32x4 pp;
#pragma unroll
        for (int r = 0; r < 4; ++r) { const float e = __builtin_amdgcn_exp2f(V[pt][r]); lsum += e; pp[r] = e * rr[pt][r]; }
        Pb[pt >> 1][2 * (pt & 1)] = cvtpk(pp[0], pp[1]);
        Pb[pt >> 1][2 * (pt & 1) + 1] = cvtpk(pp[2], pp[3]);
      }
#pragma unroll
      for (int kp = 0; kp < 2; ++kp)
#pragma unroll
        for (int c = 0; c < 12; ++c) {
          lds_char* a = xb + xt[c & 3] + (c >> 2) * 8192 + kp * 4096;
          const u32x2 a0 = trread(a), a1 = trread(a + 2048);
          U[c] = mfma32(u32x4{a0[0], a0[1], a1[0], a1[1]}, Pb[kp], U[c]);
        }
      bar();
    }
    float us = 0.f;
#pragma unroll
    for (int c = 0; c < 12; ++c) us += (U[c][0] + U[c][1]) + (U[c][2] + U[c][3]);
    const float um = xg_sum(us) * (1.0f / 192.0f);
    const float l = xg_sum(lsum);
    f32x4 cx[3] = {z4, z4, z4};
    lds_char* wv = smem + LDS_WL + h * 18432 + (4 * g + (i >> 2)) * 96 + (i & 3) * 8;
#pragma unroll
    for (int s = 0; s < 6; ++s) {
      const u32x4 ub = cvt8(U[2 * s] - um, U[2 * s + 1] - um);
#pragma unroll
      for (int dt = 0; dt < 3; ++dt) {
        const u32x2 a0 = trread(wv + s * 3072 + dt * 32), a1 = trread(wv + s * 3072 + 1536 + dt * 32);
        cx[dt] = mfma32(u32x4{a0[0], a0[1], a1[0], a1[1]}, ub, cx[dt]);
      }
    }
    float* pc = pctx + ((size_t)i * 256 + b) * 192 + h * 48 + 4 * g;
#pragma unroll
    for (int dt = 0; dt < 3; ++dt) *(f32x4*)(pc + 16 * dt) = cx[dt];
    if (g == 0) *(f32x2*)(pml + (((size_t)i * 256 + b) * 4 + h) * 2) = f32x2{mrun, l};
  }
}

__global__ void __launch_bounds__(384)
m2f_reduce(const float* __restrict__ pctx, const float* __restrict__ pml, const unsigned short* __restrict__ q_,
           const unsigned short* __restrict__ wo, unsigned short* __restrict__ out) {
  __shared__ float wgt[256 * 4];
  __shared__ float red[8][4][2];
  __shared__ f32x4 acc8[8][48];
  __shared__ float ctx[192];
  __shared__ float o2[2][192];
  const int qi = blockIdx.x, tid = threadIdx.x;
  const int col = tid % 48, grp = tid / 48;
  f32x4 v[32];
#pragma unroll
  for (int k = 0; k < 32; ++k) v[k] = *(const f32x4*)(pctx + ((size_t)qi * 256 + grp * 32 + k) * 192 + col * 4);
  f32x4 m0 = {-3e38f, 0.f, -3e38f, 0.f}, m1 = m0;
  if (tid < 256) { m0 = *(const f32x4*)(pml + ((size_t)qi * 256 + tid) * 8); m1 = *(const f32x4*)(pml + ((size_t)qi * 256 + tid) * 8 + 4); }
  float mm[4] = {m0[0], m0[2], m1[0], m1[2]}, ll[4] = {m0[1], m0[3], m1[1], m1[3]};
  float mx[4];
#pragma unroll
  for (int hh = 0; hh < 4; ++hh) {
    float a = mm[hh];
    for (int o = 32; o; o >>= 1) a = fmaxf(a, __shfl_xor(a, o));
    if ((tid & 63) == 0) red[tid >> 6][hh][0] = a;
  }
  __syncthreads();
#pragma unroll
  for (int hh = 0; hh < 4; ++hh) {
    mx[hh] = fmaxf(fmaxf(red[0][hh][0], red[1][hh][0]), fmaxf(red[2][hh][0], red[3][hh][0]));
    const float e = tid < 256 ? __builtin_amdgcn_exp2f(mm[hh] - mx[hh]) : 0.f;
    if (tid < 256) wgt[tid * 4 + hh] = e;
    float a = e * ll[hh];
    for (int o = 32; o; o >>= 1) a += __shfl_xor(a, o);
    if ((tid & 63) == 0) red[tid >> 6][hh][1] = a;
  }
  __syncthreads();
  const int hh = col / 12;
  f32x4 a = {0.f, 0.f, 0.f, 0.f};
#pragma unroll
  for (int k = 0; k < 32; ++k) a += v[k] * wgt[(grp * 32 + k) * 4 + hh];
  acc8[grp][col] = a;
  __syncthreads();
  if (tid < 192) {
    float s = 0.f;
#pragma unroll
    for (int k = 0; k < 8; ++k) s += ((const float*)acc8[k])[tid];
    const int h2 = tid / 48;
    const float L = (red[0][h2][1] + red[1][h2][1]) + (red[2][h2][1] + red[3][h2][1]);
    ctx[tid] = s / L;
  }
  __syncthreads();
  {
    const int c = tid % 192, half = tid / 192;
    float s = 0.f;
#pragma unroll 8
    for (int k = 0; k < 96; ++k) s += ctx[half * 96 + k] * __uint_as_float((unsigned)wo[(half * 96 + k) * 192 + c] << 16);
    o2[half][c] = s;
  }
  __syncthreads();
  if (tid < 192) {
    const float r = __uint_as_float((unsigned)q_[qi * 192 + tid] << 16) + o2[0][tid] + o2[1][tid];
    out[qi * 192 + tid] = (unsigned short)(cvtpk(r, r) & 0xffffu);
  }
}

extern "C" void kernel_launch(void* const* d_in, const int* in_sizes, int n_in, void* d_out, int out_size, void* d_ws,
                              size_t ws_size, hipStream_t stream) {
  float* pctx = (float*)d_ws;
  float* pml = pctx + 16 * 256 * 192;
  m2f_main<<<256, 512, 0, stream>>>(d_in[0], d_in[1], d_in[2], d_in[3], d_in[4], d_in[5], d_in[6], d_in[8], pctx, pml);
  m2f_reduce<<<16, 384, 0, stream>>>(pctx, pml, (const unsigned short*)d_in[0], (const unsigned short*)d_in[7], (unsigned short*)d_out);
}
```

```cpp
#include <hip/hip_runtime.h>
#include <stdint.h>

typedef __attribute__((ext_vector_type(8))) __bf16 bf16x8;
typedef __attribute__((ext_vector_type(2))) __bf16 bf16x2;
typedef __attribute__((ext_vector_type(4))) short s16x4;
typedef __attribute__((ext_vector_type(4))) float f32x4;
typedef __attribute__((ext_vector_type(2))) float f32x2;
typedef __attribute__((ext_vector_type(4))) unsigned u32x4;
typedef __attribute__((ext_vector_type(2))) unsigned u32x2;
typedef __attribute__((address_space(3))) char lds_char;
typedef __attribute__((address_space(3))) s16x4 lds_s16x4;
typedef __attribute__((address_space(3))) u32x4 lds_u32x4;
typedef __attribute__((address_space(3))) f32x4 lds_f32x4;
typedef __attribute__((address_space(3))) float lds_f32;

typedef __attribute__((ext_vector_type(16))) float f32x16;
#if defined(__HIP_DEVICE_COMPILE__)
#define NOPK __attribute__((target("no-packed-fp32-ops")))
#else
#define NOPK
#endif
#define DEV __device__ __forceinline__ NOPK
#define SB() __builtin_amdgcn_sched_barrier(0)
#define OPQ(x) asm volatile("" : "+v"(x))

constexpr int LDS_X = 0;
constexpr int LDS_WL = 24576;
constexpr int LDS_WV = 49152;
constexpr int LDS_LO = 98304;
constexpr int LDS_LAT = 122880;
constexpr int LDS_R = 147456;
constexpr int LDS_TOTAL = 147968;
constexpr float THR = 16.0f;

DEV unsigned cvtpk(float lo, float hi) {
  f32x2 v = {lo, hi};
  return __builtin_bit_cast(unsigned, __builtin_convertvector(v, bf16x2));
}
DEV u32x4 cvt8(f32x4 a, f32x4 b) { return u32x4{cvtpk(a[0], a[1]), cvtpk(a[2], a[3]), cvtpk(b[0], b[1]), cvtpk(b[2], b[3])}; }
DEV f32x4 mfma32(u32x4 a, u32x4 b, f32x4 c) {
  return __builtin_amdgcn_mfma_f32_16x16x32_bf16(__builtin_bit_cast(bf16x8, a), __builtin_bit_cast(bf16x8, b), c, 0, 0, 0);
}
DEV f32x16 mfma3232(u32x4 a, u32x4 b, f32x16 c) {
  return __builtin_amdgcn_mfma_f32_32x32x16_bf16(__builtin_bit_cast(bf16x8, a), __builtin_bit_cast(bf16x8, b), c, 0, 0, 0);
}
DEV unsigned f2u(float x) { return __builtin_bit_cast(unsigned, x); }
DEV float u2f(unsigned x) { return __builtin_bit_cast(float, x); }
DEV f32x4 mfma16(u32x2 a, u32x2 b, f32x4 c) {
  return __builtin_amdgcn_mfma_f32_16x16x16bf16_1k(__builtin_bit_cast(s16x4, a), __builtin_bit_cast(s16x4, b), c, 0, 0, 0);
}
DEV f32x4 mfma4(u32x2 a, u32x2 b, f32x4 c) {
  return __builtin_amdgcn_mfma_f32_4x4x4bf16_1k(__builtin_bit_cast(s16x4, a), __builtin_bit_cast(s16x4, b), c, 0, 0, 0);
}
DEV u32x2 trread(lds_char* p) {
  return __builtin_bit_cast(u32x2, __builtin_amdgcn_ds_read_tr16_b64_v4i16((lds_s16x4*)p));
}
template <int CTRL, int BANK> DEV unsigned dppu(unsigned old, unsigned src) {
  return (unsigned)__builtin_amdgcn_update_dpp((int)old, (int)src, CTRL, 0xf, BANK, false);
}
template <int CTRL, int BANK> DEV float dppf(float old, float src) {
  return __builtin_bit_cast(float, dppu<CTRL, BANK>(__builtin_bit_cast(unsigned, old), __builtin_bit_cast(unsigned, src)));
}
constexpr int DPP_ID = 0xE4, DPP_ROR8 = 0x128;
DEV float xg_sum(float x) {
  auto s = __builtin_amdgcn_permlane16_swap(f2u(x), f2u(x), false, false);
  x = u2f(s[0]) + u2f(s[1]);
  auto t = __builtin_amdgcn_permlane32_swap(f2u(x), f2u(x), false, false);
  return u2f(t[0]) + u2f(t[1]);
}
DEV float xg_max(float x) {
  auto s = __builtin_amdgcn_permlane16_swap(f2u(x), f2u(x), false, false);
  x = __builtin_fmaxf(u2f(s[0]), u2f(s[1]));
  auto t = __builtin_amdgcn_permlane32_swap(f2u(x), f2u(x), false, false);
  return __builtin_fmaxf(u2f(t[0]), u2f(t[1]));
}
DEV void bar() { SB(); __builtin_amdgcn_s_waitcnt(0xC07F); asm volatile("s_barrier" ::: "memory"); SB(); }
typedef __amdgpu_buffer_rsrc_t rsrc_t;
DEV rsrc_t mkrsrc(const void* p, unsigned bytes) { return __builtin_amdgcn_make_buffer_rsrc((void*)p, (short)0, (int)bytes, 0x00020000); }
template <int AUX> DEV u32x4 bld4(rsrc_t r, unsigned voff, unsigned soff) { return __builtin_amdgcn_raw_buffer_load_b128(r, voff, soff, AUX); }
template <int AUX> DEV u32x2 bld2(rsrc_t r, unsigned voff, unsigned soff) { return __builtin_amdgcn_raw_buffer_load_b64(r, voff, soff, AUX); }
constexpr int NT = 2;

DEV void dma16(rsrc_t r, unsigned voff, unsigned soff, unsigned ldsdst) {
  asm volatile("s_mov_b32 m0, %0\n\ts_nop 0\n\tbuffer_load_dwordx4 %1, %2, %3 offen lds" :: "s"(ldsdst), "v"(voff), "s"(r), "s"(soff) : "memory");
}
DEV void dma24(rsrc_t r, unsigned v0, unsigned v1, unsigned soff, unsigned ldsdst) {
  asm volatile("s_mov_b32 m0, %0\n\ts_nop 0\n\tbuffer_load_dwordx4 %1, %3, %4 offen nt lds\n\t"
               "s_mov_b32 exec_hi, 0\n\ts_add_u32 m0, m0, 0x400\n\ts_nop 0\n\tbuffer_load_dwordx4 %2, %3, %4 offen nt lds\n\ts_mov_b32 exec_hi, -1"
               :: "s"(ldsdst), "v"(v0), "v"(v1), "s"(r), "s"(soff) : "memory", "scc");
}
DEV float dot2(unsigned a, unsigned b, float c) {
  return __builtin_amdgcn_fdot2_f32_bf16(__builtin_bit_cast(bf16x2, a), __builtin_bit_cast(bf16x2, b), c, false);
}
template <int CTRL> DEV float dppadd(float x) {
  return x + __builtin_bit_cast(float, __builtin_amdgcn_update_dpp(0, __builtin_bit_cast(int, x), CTRL, 0xf, 0xf, true));
}

#define BAR() bar()
#define TSX(k)
#define BARARGS
#define BARPASS
#define TSINIT()
#define TSFIN()

struct Prod {
  u32x4 W[18];
  u32x4 ring[4][6];
};
struct ProdC {
  lds_char* smem;
  unsigned xw[2], rw, br[2], lw[2], lr[2], vd[2], voH, lds0;
  u32x2 I4;
  bool rvalid, odd;
  rsrc_t rhi, rlo;
};
DEV void lat_read(u32x4 (&B)[6], const ProdC& C, int t, int k0, int k1) {
#pragma unroll
  for (int ks = k0; ks < k1; ++ks) B[ks] = *(lds_u32x4*)(C.smem + C.br[ks & 1] + (t & 3) * 6144 + (ks >> 1) * 128);
}
DEV void lat_mm(const Prod& P, const u32x4 (&B)[6], const ProdC& C, int t) {
  const f32x4 z4 = {0.f, 0.f, 0.f, 0.f};
  f32x4 T[3];
#pragma unroll
  for (int ks = 0; ks < 6; ++ks)
#pragma unroll
    for (int tt = 0; tt < 3; ++tt) {
      T[tt] = mfma32(P.W[tt * 6 + ks], B[ks], ks == 0 ? z4 : T[tt]);
      OPQ(T[tt]);
      SB();
    }
#pragma unroll
  for (int tt = 0; tt < 3; ++tt) *(lds_f32x4*)(C.smem + C.lw[tt & 1] + (t & 1) * 12288 + (tt >> 1) * 128) = T[tt];
}
template <bool LOAD, bool LAT> DEV void pstep(Prod& P, const ProdC& C, int t, unsigned so, unsigned sl) {
  float ss[2] = {0.f, 0.f}, sm[2] = {0.f, 0.f};
  f32x4 d[6][2], lat[3][2];
  u32x4 B[6];
#pragma unroll
  for (int L = 0; L < 2; ++L)
#pragma unroll
    for (int u = 0; u < 2; ++u) lat[L][u] = *(lds_f32x4*)(C.smem + C.lr[u] + (t & 1) * 12288 + L * 256);
  if (LOAD) dma24(C.rlo, C.vd[0], C.vd[1], sl, C.lds0 + (t & 3) * 6144);
  SB();
#pragma unroll
  for (int m = 0; m < 7; ++m) {
    if (m < 6) {
      const u32x4 F = P.ring[t & 3][m];
      d[m][0] = mfma4(C.I4, u32x2{F[0], F[1]}, lat[m >> 1][0]);
      d[m][1] = mfma4(C.I4, u32x2{F[2], F[3]}, lat[m >> 1][1]);
      OPQ(d[m][0]); OPQ(d[m][1]);
      SB();
      if (LOAD) P.ring[t & 3][m] = bld4<NT>(C.rhi, C.voH + (m >> 1) * 128, so + (m & 1) * 196608);
      if (m == 1) {
#pragma unroll
        for (int u = 0; u < 2; ++u) lat[2][u] = *(lds_f32x4*)(C.smem + C.lr[u] + (t & 1) * 12288 + 512);
      }
      if (LAT && m >= 3) lat_read(B, C, t + 1, 2 * (m - 3), 2 * (m - 3) + 2);
    }
    if (m > 0) {
      const int k = m - 1, ab = k & 1, L = k >> 1;
      const u32x4 x = cvt8(d[k][0], d[k][1]);
      *(lds_u32x4*)(C.smem + C.xw[ab] + (t & 1) * 24576 + L * 8192) = x;
#pragma unroll
      for (int r = 0; r < 4; ++r) { ss[ab] = dot2(x[r], x[r], ss[ab]); sm[ab] = dot2(x[r], 0x3F803F80u, sm[ab]); }
    }
    SB();
  }
#pragma unroll
  for (int ab = 0; ab < 2; ++ab) {
    ss[ab] = dppadd<0x141>(dppadd<0x4E>(dppadd<0xB1>(ss[ab])));
    sm[ab] = dppadd<0x141>(dppadd<0x4E>(dppadd<0xB1>(sm[ab])));
  }
  const float mu = (C.odd ? sm[1] : sm[0]) * (1.0f / 192.0f);
  const float var = __builtin_fmaxf((C.odd ? ss[1] : ss[0]) * (1.0f / 192.0f) - mu * mu, 0.0f);
  const float rs = __builtin_amdgcn_rsqf(var + 1e-5f);
  if (C.rvalid) *(lds_f32*)(C.smem + C.rw + (t & 1) * 256) = rs;
  SB();
  if (LAT) lat_mm(P, B, C, t + 1);
}
DEV void ld_hi(u32x4 (&dst)[6], const ProdC& C, unsigned so) {
#pragma unroll
  for (int n = 0; n < 6; ++n) dst[n] = bld4<NT>(C.rhi, C.voH + (n >> 1) * 128, so + (n & 1) * 196608);
}
template <int N> DEV void vmwait() { asm volatile("s_waitcnt vmcnt(%0)" :: "n"(N) : "memory"); }

struct Cons {
  f32x16 U[6];
  u32x4 Ab[12];
  u32x4 X[12];
  f32x4 rr[4];
  float mrun, lsum;
};
struct ConsC { lds_char* smem; unsigned xr[4], xt[2][2], ra; };
DEV u32x4 row_read(const ConsC& C, int s) { return *(lds_u32x4*)(C.smem + C.xr[s & 3] + (s >> 2) * 8192); }
DEV u32x4 col_read(const ConsC& C, int o) {
  const int ks = o / 6, ct = o % 6;
  const u32x2 a0 = trread(C.smem + C.xt[ct & 1][0] + (ct >> 1) * 8192 + ks * 2048);
  const u32x2 a1 = trread(C.smem + C.xt[ct & 1][1] + (ct >> 1) * 8192 + ks * 2048);
  return u32x4{a0[0], a0[1], a1[0], a1[1]};
}
DEV void cstep(Cons& K, ConsC& C, int dx, int dr, bool first BARARGS) {
  f32x16 S = {0.f, 0.f, 0.f, 0.f, 0.f, 0.f, 0.f, 0.f, 0.f, 0.f, 0.f, 0.f, 0.f, 0.f, 0.f, 0.f};
#pragma unroll
  for (int s = 0; s < 12; ++s) {
    S = mfma3232(K.X[s], K.Ab[s], S);
    OPQ(S);
    SB();
    K.X[s] = col_read(C, s);
    SB();
  }
#pragma unroll
  for (int s = 0; s < 4; ++s) { C.xr[s] += dx; C.xt[s >> 1][s & 1] += dx; OPQ(C.xr[s]); OPQ(C.xt[s >> 1][s & 1]); }
  C.ra += dr; OPQ(C.ra);
  SB();
  float V[16];
  float vmax = -3e38f;
#pragma unroll
  for (int e = 0; e < 16; ++e) { V[e] = S[e] * K.rr[e >> 2][e & 3] - K.mrun; vmax = __builtin_fmaxf(vmax, V[e]); }
  BAR();
  if (first || __builtin_amdgcn_ballot_w64(!(vmax <= THR)) != 0) {
    auto t = __builtin_amdgcn_permlane32_swap(f2u(vmax), f2u(vmax), false, false);
    float dl = __builtin_fmaxf(u2f(t[0]), u2f(t[1]));
    if (!first) dl = __builtin_fmaxf(dl, 0.f);
    const float al = __builtin_amdgcn_exp2f(__builtin_fminf(-dl, 0.f));
    K.mrun += dl; K.lsum *= al;
#pragma unroll
    for (int c = 0; c < 6; ++c) K.U[c] *= al;
#pragma unroll
    for (int e = 0; e < 16; ++e) V[e] -= dl;
  }
  u32x4 Pb[2];
  float pe[16];
#pragma unroll
  for (int e = 0; e < 8; ++e) pe[e] = __builtin_amdgcn_exp2f(V[e]);
#pragma unroll
  for (int e = 0; e < 8; e += 2) Pb[0][e >> 1] = cvtpk(pe[e] * K.rr[e >> 2][e & 3], pe[e + 1] * K.rr[e >> 2][(e & 3) + 1]);
  SB();
#pragma unroll
  for (int ct = 0; ct < 6; ++ct) {
    K.U[ct] = mfma3232(K.X[ct], Pb[0], K.U[ct]);
    OPQ(K.U[ct]);
    SB();
    K.X[ct] = row_read(C, ct);
    if (ct < 4) {
      const int e = 8 + 2 * ct;
      pe[e] = __builtin_amdgcn_exp2f(V[e]); pe[e + 1] = __builtin_amdgcn_exp2f(V[e + 1]);
      Pb[1][ct] = cvtpk(pe[e] * K.rr[e >> 2][e & 3], pe[e + 1] * K.rr[e >> 2][(e & 3) + 1]);
    } else {
      const int e = 4 * (ct - 4);
      K.lsum += (pe[e] + pe[e + 1]) + (pe[e + 2] + pe[e + 3]);
    }
    SB();
  }
#pragma unroll
  for (int ct = 0; ct < 6; ++ct) {
    K.U[ct] = mfma3232(K.X[6 + ct], Pb[1], K.U[ct]);
    OPQ(K.U[ct]);
    SB();
    K.X[6 + ct] = row_read(C, 6 + ct);
    if (ct < 4) K.rr[ct] = *(lds_f32x4*)(C.smem + C.ra + ct * 32);
    if (ct < 2) { const int e = 8 + 4 * ct; K.lsum += (pe[e] + pe[e + 1]) + (pe[e + 2] + pe[e + 3]); }
    SB();
  }
}

__global__ void __launch_bounds__(192)
m2f_prep(const unsigned short* __restrict__ q_, const unsigned short* __restrict__ wq, const unsigned short* __restrict__ wk,
         const unsigned short* __restrict__ wm, unsigned short* __restrict__ aout) {
  __shared__ float sq[192], sQ[48], red[4];
  const int h = blockIdx.x >> 4, qi = blockIdx.x & 15, c = threadIdx.x;
  auto bf = [](unsigned short v) { return __uint_as_float((unsigned)v << 16); };
  sq[c] = bf(q_[qi * 192 + c]);
  __syncthreads();
  if (c < 48) { float s = 0.f; for (int k = 0; k < 192; ++k) s += sq[k] * bf(wq[(h * 192 + k) * 48 + c]); sQ[c] = s; }
  float m = 0.f;
  for (int k = 0; k < 192; ++k) m += sq[k] * bf(wm[k * 192 + c]);
  __syncthreads();
  float s = 0.f;
  for (int d = 0; d < 48; ++d) s += sQ[d] * bf(wk[(h * 192 + c) * 48 + d]);
  const float LOG2E = 1.4426950408889634f;
  const float a = s * (LOG2E * 0.14433756729740643f) + m * (LOG2E * 0.07216878364870322f);
  float t = a;
  for (int o = 32; o; o >>= 1) t += __shfl_xor(t, o);
  if ((c & 63) == 0) red[c >> 6] = t;
  __syncthreads();
  const float v = a - (red[0] + red[1] + red[2]) * (1.0f / 192.0f);
  aout[(((h >> 1) * 12 + (c >> 4)) * 64 + ((c >> 3) & 1) * 32 + (h & 1) * 16 + qi) * 8 + (c & 7)] = (unsigned short)(cvtpk(v, v) & 0xffffu);
}

__global__ void __launch_bounds__(512, 2) NOPK
m2f_main(const void* __restrict__ fhi_, const void* __restrict__ flo_, const void* __restrict__ wlat_,
         const void* __restrict__ wv_, const void* __restrict__ a_, float* __restrict__ pctx, float* __restrict__ pml) {
  __shared__ __attribute__((aligned(16))) char smem_[LDS_TOTAL];
  lds_char* smem = (lds_char*)smem_;
  const int tid = __builtin_amdgcn_workitem_id_x(), lane = tid & 63;
  const int w = __builtin_amdgcn_readfirstlane(tid >> 6);
  const int i = lane & 15, g = lane >> 4;
  const int b = __builtin_amdgcn_workgroup_id_x();
  const f32x4 z4 = {0.f, 0.f, 0.f, 0.f};
  TSINIT();

  if (w < 4) {
    const int p = w;
    const rsrc_t rwv = mkrsrc(wv_, 73728u), rwl = mkrsrc(wlat_, 73728u);
    ProdC C;
    C.smem = smem;
    C.rhi = mkrsrc(fhi_, 100663296u);
    C.rlo = mkrsrc(flo_, 25165824u);
    const unsigned j = lane >> 3, c7 = lane & 7;
    const unsigned lbase = (unsigned)(uintptr_t)smem;
    C.voH = j * 384 + c7 * 16;
    C.lds0 = lbase + LDS_LO + p * 1536;
#pragma unroll
    for (int k = 0; k < 2; ++k) {
      const unsigned o = k * 1024 + (lane & (k ? 31 : 63)) * 16, px = o / 384, pc = (o % 384) / 16;
      C.vd[k] = px * 384 + ((pc ^ (2 * p + (px >> 1))) * 16);
    }
    const unsigned soL = (unsigned)(b * 256 + p * 4) * 384u;
    const unsigned soH = (unsigned)(b * 1024 + p * 8) * 384u;
    Prod P;
    dma24(C.rlo, C.vd[0], C.vd[1], soL, C.lds0);
    dma24(C.rlo, C.vd[0], C.vd[1], soL + 6144, C.lds0 + 6144);
    ld_hi(P.ring[0], C, soH);
    SB();
    {
      unsigned vo[6];
#pragma unroll
      for (int m = 0; m < 6; ++m) {
        const unsigned o = m * 1024 + lane * 16, k = o / 384, pc = (o % 384) / 16;
        vo[m] = k * 384 + ((pc ^ (2 * (((k >> 1) & 1) | ((k >> 3) << 1)))) * 16);
      }
#pragma unroll
      for (int n = 0; n < 18; ++n) dma16(rwl, vo[n % 6], p * 18432 + (n / 6) * 6144, lbase + LDS_WL + p * 18432 + n * 1024);
    }
    SB();
    asm volatile("s_barrier" ::: "memory");
    SB();
    ld_hi(P.ring[1], C, soH + 12288);
    SB();
    dma24(C.rlo, C.vd[0], C.vd[1], soL + 2 * 6144, C.lds0 + 2 * 6144);
    ld_hi(P.ring[2], C, soH + 2 * 12288);
    SB();
    dma24(C.rlo, C.vd[0], C.vd[1], soL + 3 * 6144, C.lds0 + 3 * 6144);
    ld_hi(P.ring[3], C, soH + 3 * 12288);
    SB();
    {
      const int d = lane & 3;
      C.I4[0] = d == 0 ? 0x00003F80u : (d == 1 ? 0x3F800000u : 0u);
      C.I4[1] = d == 2 ? 0x00003F80u : (d == 3 ? 0x3F800000u : 0u);
#pragma unroll
      for (int ab = 0; ab < 2; ++ab) {
        C.xw[ab] = LDS_X + (16 * p + 8 * ab + j) * 128 + ((c7 ^ (((j >> 1) & 1) * 4 + 2 * ab + (j >> 2))) * 16);
        C.br[ab] = LDS_LO + i * 384 + (((4 * ab + g) ^ (i >> 1)) * 16);
        C.lw[ab] = LDS_LAT + i * 768 + (((12 * p + 4 * ab + g) ^ (i & 7)) * 16);
        C.lr[ab] = LDS_LAT + (4 * p + (j >> 1)) * 768 + (((2 * c7 + ab) ^ ((4 * p + (j >> 1)) & 7)) * 16);
      }
      C.rvalid = c7 < 2; C.odd = lane & 1;
      C.rw = LDS_R + (16 * p + 8 * (lane & 1) + j) * 4;
#pragma unroll
      for (int ab = 0; ab < 2; ++ab) { OPQ(C.xw[ab]); OPQ(C.br[ab]); OPQ(C.lw[ab]); OPQ(C.lr[ab]); OPQ(C.vd[ab]); }
      OPQ(C.rw); OPQ(C.voH);
    }
    vmwait<22>();
    BAR();
    {
      const unsigned rp = i >> 2, cp = i & 3, s = (rp >> 1) | ((g & 1) << 1);
      u32x4 B[6];
      lat_read(B, C, 0, 0, 6);
#pragma unroll
      for (int tt = 0; tt < 3; ++tt) {
        const unsigned ct = 3 * p + tt;
        lds_char* a = smem + LDS_WL + (8 * g + rp) * 384 + (((ct & 3) ^ s) * 32) + cp * 8 + (ct >> 2) * 128;
#pragma unroll
        for (int ks = 0; ks < 6; ++ks) {
          const u32x2 a0 = trread(a + ks * 12288), a1 = trread(a + ks * 12288 + 1536);
          P.W[tt * 6 + ks] = u32x4{a0[0], a0[1], a1[0], a1[1]};
        }
      }
      SB();
      lat_mm(P, B, C, 0);
    }
    BAR();
#pragma unroll 1
    for (int u = 0; u < 3; ++u) {
      const unsigned so = soH + (u + 1) * 49152, sl = soL + (u + 1) * 24576;
      pstep<true, true>(P, C, 0, so, sl); vmwait<22>(); BAR();
      pstep<true, true>(P, C, 1, so + 12288, sl + 6144); vmwait<22>(); BAR();
      pstep<true, true>(P, C, 2, so + 2 * 12288, sl + 2 * 6144); vmwait<22>(); BAR();
      pstep<true, true>(P, C, 3, so + 3 * 12288, sl + 3 * 6144); vmwait<22>(); BAR();
    }
    const unsigned vw = lane * 16, lw = lbase + LDS_WV + p * 1024;
    pstep<false, true>(P, C, 0, 0, 0); vmwait<14>();
#pragma unroll
    for (int n = 0; n < 13; ++n) dma16(rwv, vw, p * 1024 + n * 4096, lw + n * 4096);
    BAR();
    pstep<false, true>(P, C, 1, 0, 0); vmwait<19>();
#pragma unroll
    for (int n = 13; n < 15; ++n) dma16(rwv, vw, p * 1024 + n * 4096, lw + n * 4096);
    BAR();
    pstep<false, true>(P, C, 2, 0, 0);
    dma16(rwv, vw, p * 1024 + 15 * 4096, lw + 15 * 4096);
    BAR();
    pstep<false, false>(P, C, 3, 0, 0);
#pragma unroll
    for (int n = 16; n < 18; ++n) dma16(rwv, vw, p * 1024 + n * 4096, lw + n * 4096);
    BAR();
    BAR();
    vmwait<0>();
    BAR();
  } else {
    const int h = w - 4, hp = h >> 1, ph = h & 1;
    const rsrc_t ra = mkrsrc(a_, 24576u);
    Cons K;
    __builtin_amdgcn_s_setprio(2);
    SB();
    asm volatile("s_barrier" ::: "memory");
    SB();
#pragma unroll
    for (int s = 0; s < 12; ++s) K.Ab[s] = bld4<0>(ra, lane * 16 + (s & 3) * 1024, hp * 12288 + (s >> 2) * 4096);
    BAR();
    BAR();
    ConsC C;
    C.smem = smem;
    const unsigned hl = lane >> 5;
    {
      const unsigned m = lane & 31, row = 32 * ph + m, f = ((m >> 1) & 1) * 4 + ((m >> 2) & 3);
#pragma unroll
      for (int s = 0; s < 4; ++s) C.xr[s] = LDS_X + row * 128 + (((2 * s + hl) ^ f) * 16);
      const unsigned cb = g & 1, rp = i >> 2, cp = i & 3;
#pragma unroll
      for (int c = 0; c < 2; ++c)
#pragma unroll
        for (int sec = 0; sec < 2; ++sec)
          C.xt[c][sec] = LDS_X + (32 * ph + 8 * sec + 4 * hl + rp) * 128 + (((4 * c + 2 * cb + (cp >> 1)) ^ ((rp >> 1) * 4 + hl + 2 * sec)) * 16) + (cp & 1) * 8;
      C.ra = LDS_R + (32 * ph + 4 * hl) * 4;
#pragma unroll
      for (int s = 0; s < 4; ++s) { OPQ(C.xr[s]); OPQ(C.xt[s >> 1][s & 1]); }
      OPQ(C.ra);
    }
#pragma unroll
    for (int c = 0; c < 6; ++c)
#pragma unroll
      for (int e = 0; e < 16; ++e) K.U[c][e] = 0.f;
    K.mrun = 0.f; K.lsum = 0.f;
    BAR();
#pragma unroll
    for (int s = 0; s < 12; ++s) K.X[s] = row_read(C, s);
#pragma unroll
    for (int a = 0; a < 4; ++a) K.rr[a] = *(lds_f32x4*)(smem + C.ra + a * 32);
    SB();
#pragma unroll 1
    for (int t = 0; t < 16; ++t) cstep(K, C, (t & 1) ? -24576 : 24576, (t & 1) ? -256 : 256, t == 0 BARPASS);
    TSX(50);
    {
    int lane;
    asm volatile("v_mbcnt_lo_u32_b32 %0, -1, 0\n\tv_mbcnt_hi_u32_b32 %0, -1, %0" : "=v"(lane));
    const int i = lane & 15, g = lane >> 4;
    f32x4 Uo[12];
    lds_char* ex = smem + LDS_X + h * 12288 + lane * 16;
#pragma unroll
    for (int ct = 0; ct < 6; ++ct)
#pragma unroll
      for (int a = 0; a < 2; ++a) {
        f32x4 k0, k1;
#pragma unroll
        for (int r = 0; r < 4; ++r) {
          auto sw = __builtin_amdgcn_permlane16_swap(f2u(K.U[ct][8 * a + r]), f2u(K.U[ct][8 * a + 4 + r]), false, false);
          k0[r] = u2f(sw[0]); k1[r] = u2f(sw[1]);
        }
        Uo[2 * ct + a] = ph ? k1 : k0;
        *(lds_f32x4*)(ex + (2 * ct + a) * 1024) = ph ? k0 : k1;
      }
    TSX(51);
    float mo, lo_;
    {
      auto t = __builtin_amdgcn_permlane32_swap(f2u(K.lsum), f2u(K.lsum), false, false);
      const float l = u2f(t[0]) + u2f(t[1]);
      auto sl = __builtin_amdgcn_permlane16_swap(f2u(l), f2u(l), false, false);
      auto sm = __builtin_amdgcn_permlane16_swap(f2u(K.mrun), f2u(K.mrun), false, false);
      mo = u2f(ph ? sm[1] : sm[0]); lo_ = u2f(ph ? sl[1] : sl[0]);
      *(__attribute__((address_space(3))) f32x2*)(smem + LDS_LAT + h * 512 + lane * 8) = f32x2{u2f(ph ? sm[0] : sm[1]), u2f(ph ? sl[0] : sl[1])};
    }
    BAR();
    const f32x2 ml = *(__attribute__((address_space(3))) f32x2*)(smem + LDS_LAT + (h ^ 1) * 512 + lane * 8);
    const float mn = __builtin_fmaxf(mo, ml[0]);
    const float fa = __builtin_amdgcn_exp2f(mo - mn), fb = __builtin_amdgcn_exp2f(ml[0] - mn);
    const float l = lo_ * fa + ml[1] * fb;
    float us = 0.f;
    lds_char* ey = smem + LDS_X + (h ^ 1) * 12288 + lane * 16;
#pragma unroll
    for (int c = 0; c < 12; ++c) {
      const f32x4 o = *(lds_f32x4*)(ey + c * 1024);
      Uo[c] = Uo[c] * fa + o * fb;
      us += (Uo[c][0] + Uo[c][1]) + (Uo[c][2] + Uo[c][3]);
    }
    TSX(52);
    const float um = xg_sum(us) * (1.0f / 192.0f);
    f32x4 cx[3] = {z4, z4, z4};
    lds_char* wv = smem + LDS_WV + h * 18432 + (8 * (g & 1) + 4 * (g >> 1) + (i >> 2)) * 96 + (i & 3) * 8;
#pragma unroll
    for (int s = 0; s < 6; ++s) {
      const u32x4 ub = cvt8(Uo[2 * s] - um, Uo[2 * s + 1] - um);
#pragma unroll
      for (int dt = 0; dt < 3; ++dt) {
        const u32x2 a0 = trread(wv + s * 3072 + dt * 32), a1 = trread(wv + s * 3072 + 1536 + dt * 32);
        cx[dt] = mfma32(u32x4{a0[0], a0[1], a1[0], a1[1]}, ub, cx[dt]);
      }
    }
    TSX(53);
    float* pc = pctx + ((size_t)i * 256 + b) * 192 + h * 48 + 4 * g;
#pragma unroll
    for (int dt = 0; dt < 3; ++dt) *(f32x4*)(pc + 16 * dt) = cx[dt];
    if (g == 0) *(f32x2*)(pml + (((size_t)i * 256 + b) * 4 + h) * 2) = f32x2{mn, l};
    }
  }
  TSFIN();
}

__global__ void __launch_bounds__(384)
m2f_reduce(const float* __restrict__ pctx, const float* __restrict__ pml, const unsigned short* __restrict__ q_,
           const unsigned short* __restrict__ wo, unsigned short* __restrict__ out) {
  __shared__ float wgt[256 * 4];
  __shared__ float red[8][4][2];
  __shared__ f32x4 acc8[8][48];
  __shared__ float ctx[192];
  __shared__ float o2[2][192];
  const int qi = blockIdx.x, tid = threadIdx.x;
  const int col = tid % 48, grp = tid / 48;
  f32x4 v[32];
#pragma unroll
  for (int k = 0; k < 32; ++k) v[k] = *(const f32x4*)(pctx + ((size_t)qi * 256 + grp * 32 + k) * 192 + col * 4);
  f32x4 m0 = {-3e38f, 0.f, -3e38f, 0.f}, m1 = m0;
  if (tid < 256) { m0 = *(const f32x4*)(pml + ((size_t)qi * 256 + tid) * 8); m1 = *(const f32x4*)(pml + ((size_t)qi * 256 + tid) * 8 + 4); }
  float mm[4] = {m0[0], m0[2], m1[0], m1[2]}, ll[4] = {m0[1], m0[3], m1[1], m1[3]};
  float mx[4];
#pragma unroll
  for (int hh = 0; hh < 4; ++hh) {
    float a = mm[hh];
    for (int o = 32; o; o >>= 1) a = fmaxf(a, __shfl_xor(a, o));
    if ((tid & 63) == 0) red[tid >> 6][hh][0] = a;
  }
  __syncthreads();
#pragma unroll
  for (int hh = 0; hh < 4; ++hh) {
    mx[hh] = fmaxf(fmaxf(red[0][hh][0], red[1][hh][0]), fmaxf(red[2][hh][0], red[3][hh][0]));
    const float e = tid < 256 ? __builtin_amdgcn_exp2f(mm[hh] - mx[hh]) : 0.f;
    if (tid < 256) wgt[tid * 4 + hh] = e;
    float a = e * ll[hh];
    for (int o = 32; o; o >>= 1) a += __shfl_xor(a, o);
    if ((tid & 63) == 0) red[tid >> 6][hh][1] = a;
  }
  __syncthreads();
  const int hh = col / 12;
  f32x4 a = {0.f, 0.f, 0.f, 0.f};
#pragma unroll
  for (int k = 0; k < 32; ++k) a += v[k] * wgt[(grp * 32 + k) * 4 + hh];
  acc8[grp][col] = a;
  __syncthreads();
  if (tid < 192) {
    float s = 0.f;
#pragma unroll
    for (int k = 0; k < 8; ++k) s += ((const float*)acc8[k])[tid];
    const int h2 = tid / 48;
    const float L = (red[0][h2][1] + red[1][h2][1]) + (red[2][h2][1] + red[3][h2][1]);
    ctx[tid] = s / L;
  }
  __syncthreads();
  {
    const int c = tid % 192, half = tid / 192;
    float s = 0.f;
#pragma unroll 8
    for (int k = 0; k < 96; ++k) s += ctx[half * 96 + k] * __uint_as_float((unsigned)wo[(half * 96 + k) * 192 + c] << 16);
    o2[half][c] = s;
  }
  __syncthreads();
  if (tid < 192) {
    const float r = __uint_as_float((unsigned)q_[qi * 192 + tid] << 16) + o2[0][tid] + o2[1][tid];
    out[qi * 192 + tid] = (unsigned short)(cvtpk(r, r) & 0xffffu);
  }
}

extern "C" void kernel_launch(void* const* d_in, const int* in_sizes, int n_in, void* d_out, int out_size, void* d_ws,
                              size_t ws_size, hipStream_t stream) {
  float* pctx = (float*)d_ws;
  float* pml = pctx + 16 * 256 * 192;
  unsigned short* abuf = (unsigned short*)(pml + 16 * 256 * 8);
  m2f_prep<<<64, 192, 0, stream>>>((const unsigned short*)d_in[0], (const unsigned short*)d_in[4], (const unsigned short*)d_in[5], (const unsigned short*)d_in[8], abuf);
  m2f_main<<<256, 512, 0, stream>>>(d_in[1], d_in[2], d_in[3], d_in[6], abuf, pctx, pml);
  m2f_reduce<<<16, 384, 0, stream>>>(pctx, pml, (const unsigned short*)d_in[0], (const unsigned short*)d_in[7], (unsigned short*)d_out);
}
```

```cpp
#include <hip/hip_runtime.h>
#include <stdint.h>

typedef __attribute__((ext_vector_type(8))) __bf16 bf16x8;
typedef __attribute__((ext_vector_type(2))) __bf16 bf16x2;
typedef __attribute__((ext_vector_type(4))) short s16x4;
typedef __attribute__((ext_vector_type(4))) float f32x4;
typedef __attribute__((ext_vector_type(2))) float f32x2;
typedef __attribute__((ext_vector_type(4))) unsigned u32x4;
typedef __attribute__((ext_vector_type(2))) unsigned u32x2;
typedef __attribute__((address_space(3))) char lds_char;
typedef __attribute__((address_space(3))) s16x4 lds_s16x4;
typedef __attribute__((address_space(3))) u32x4 lds_u32x4;
typedef __attribute__((address_space(3))) f32x4 lds_f32x4;
typedef __attribute__((address_space(3))) float lds_f32;

typedef __attribute__((ext_vector_type(16))) float f32x16;
#define DEV __device__ __forceinline__
#define SB() __builtin_amdgcn_sched_barrier(0)
#define OPQ(x) asm volatile("" : "+v"(x))

constexpr int LDS_X = 0;
constexpr int LDS_WL = 24576;
constexpr int LDS_WV = 49152;
constexpr int LDS_LO = 110592;
constexpr int LDS_LAT = 135168;
constexpr int LDS_R = 159744;
constexpr int LDS_ML = 160256;
constexpr int LDS_TOTAL = 162304;
constexpr int LDS_AF = 86016;
constexpr int LDS_AWQ = 0, LDS_AWM = LDS_LAT, LDS_AWK = LDS_LAT + 18432;
constexpr int LDS_WO = LDS_LO + 2 * 6144, LDS_RED = LDS_LO + 3 * 6144;
constexpr float THR = 16.0f;
constexpr unsigned WS_A = 4096, WS_REC = 65536, WS_CTX = WS_REC + 64 * 32768, WS_BYTES = WS_CTX + 8192;
constexpr unsigned KCHK = 0x9E3779B9u;
constexpr int SC1 = 16, SPIN_MAX = 1 << 15;
constexpr int L1_B0 = 64, L2_B0 = 128;

DEV float cut(float x) { asm("" : "+v"(x)); return x; }
DEV unsigned cvtpk(float lo, float hi) {
  f32x2 v = {lo, hi};
  return __builtin_bit_cast(unsigned, __builtin_convertvector(v, bf16x2));
}
DEV u32x4 cvt8(f32x4 a, f32x4 b) { return u32x4{cvtpk(a[0], a[1]), cvtpk(a[2], a[3]), cvtpk(b[0], b[1]), cvtpk(b[2], b[3])}; }
DEV f32x4 mfma32(u32x4 a, u32x4 b, f32x4 c) {
  return __builtin_amdgcn_mfma_f32_16x16x32_bf16(__builtin_bit_cast(bf16x8, a), __builtin_bit_cast(bf16x8, b), c, 0, 0, 0);
}
DEV f32x16 mfma3232(u32x4 a, u32x4 b, f32x16 c) {
  return __builtin_amdgcn_mfma_f32_32x32x16_bf16(__builtin_bit_cast(bf16x8, a), __builtin_bit_cast(bf16x8, b), c, 0, 0, 0);
}
DEV unsigned f2u(float x) { return __builtin_bit_cast(unsigned, x); }
DEV float u2f(unsigned x) { return __builtin_bit_cast(float, x); }
DEV f32x4 mfma16(u32x2 a, u32x2 b, f32x4 c) {
  return __builtin_amdgcn_mfma_f32_16x16x16bf16_1k(__builtin_bit_cast(s16x4, a), __builtin_bit_cast(s16x4, b), c, 0, 0, 0);
}
DEV f32x4 mfma4(u32x2 a, u32x2 b, f32x4 c) {
  return __builtin_amdgcn_mfma_f32_4x4x4bf16_1k(__builtin_bit_cast(s16x4, a), __builtin_bit_cast(s16x4, b), c, 0, 0, 0);
}
DEV u32x2 trread(lds_char* p) {
  return __builtin_bit_cast(u32x2, __builtin_amdgcn_ds_read_tr16_b64_v4i16((lds_s16x4*)p));
}
template <int CTRL, int BANK> DEV unsigned dppu(unsigned old, unsigned src) {
  return (unsigned)__builtin_amdgcn_update_dpp((int)old, (int)src, CTRL, 0xf, BANK, false);
}
template <int CTRL, int BANK> DEV float dppf(float old, float src) {
  return __builtin_bit_cast(float, dppu<CTRL, BANK>(__builtin_bit_cast(unsigned, old), __builtin_bit_cast(unsigned, src)));
}
constexpr int DPP_ID = 0xE4, DPP_ROR8 = 0x128;
DEV float xg_sum(float x) {
  auto s = __builtin_amdgcn_permlane16_swap(f2u(x), f2u(x), false, false);
  x = u2f(s[0]) + u2f(s[1]);
  auto t = __builtin_amdgcn_permlane32_swap(f2u(x), f2u(x), false, false);
  return u2f(t[0]) + u2f(t[1]);
}
DEV float xg_max(float x) {
  auto s = __builtin_amdgcn_permlane16_swap(f2u(x), f2u(x), false, false);
  x = __builtin_fmaxf(u2f(s[0]), u2f(s[1]));
  auto t = __builtin_amdgcn_permlane32_swap(f2u(x), f2u(x), false, false);
  return __builtin_fmaxf(u2f(t[0]), u2f(t[1]));
}
DEV int laneid() {
  int l;
  asm volatile("v_mbcnt_lo_u32_b32 %0, -1, 0\n\tv_mbcnt_hi_u32_b32 %0, -1, %0" : "=v"(l));
  __builtin_assume(l >= 0 && l < 64);
  return l;
}
DEV void bar() { SB(); __builtin_amdgcn_s_waitcnt(0xC07F); asm volatile("s_barrier" ::: "memory"); SB(); }
typedef __amdgpu_buffer_rsrc_t rsrc_t;
DEV rsrc_t mkrsrc(const void* p, unsigned bytes) { return __builtin_amdgcn_make_buffer_rsrc((void*)p, (short)0, (int)bytes, 0x00020000); }
template <int AUX> DEV u32x4 bld4(rsrc_t r, unsigned voff, unsigned soff) { return __builtin_amdgcn_raw_buffer_load_b128(r, voff, soff, AUX); }
template <int AUX> DEV u32x2 bld2(rsrc_t r, unsigned voff, unsigned soff) { return __builtin_amdgcn_raw_buffer_load_b64(r, voff, soff, AUX); }
constexpr int NT = 2;

DEV void dma16(rsrc_t r, unsigned voff, unsigned soff, unsigned ldsdst) {
  asm volatile("s_mov_b32 m0, %0\n\ts_nop 0\n\tbuffer_load_dwordx4 %1, %2, %3 offen lds" :: "s"(ldsdst), "v"(voff), "s"(r), "s"(soff) : "memory");
}
DEV void dma24(rsrc_t r, unsigned v0, unsigned v1, unsigned soff, unsigned ldsdst) {
  asm volatile("s_mov_b32 m0, %0\n\ts_nop 0\n\tbuffer_load_dwordx4 %1, %3, %4 offen nt lds\n\t"
               "s_mov_b32 exec_hi, 0\n\ts_add_u32 m0, m0, 0x400\n\ts_nop 0\n\tbuffer_load_dwordx4 %2, %3, %4 offen nt lds\n\ts_mov_b32 exec_hi, -1"
               :: "s"(ldsdst), "v"(v0), "v"(v1), "s"(r), "s"(soff) : "memory", "scc");
}
template <int OFF> DEV void ldnt(u32x4& d, rsrc_t r, unsigned voff, unsigned soff) {
  asm volatile("buffer_load_dwordx4 %0, %1, %2, %3 offen offset:%4 nt" : "=v"(d) : "v"(voff), "s"(r), "s"(soff), "n"(OFF) : "memory");
}
template <int OFF> DEV void ldpl(u32x4& d, rsrc_t r, unsigned voff) {
  asm volatile("buffer_load_dwordx4 %0, %1, %2, 0 offen offset:%3" : "=v"(d) : "v"(voff), "s"(r), "n"(OFF) : "memory");
}
template <int AUX> DEV void bst4(u32x4 v, rsrc_t r, unsigned voff, unsigned soff) { __builtin_amdgcn_raw_buffer_store_b128(v, r, voff, soff, AUX); }
template <int N> DEV void vmwait() { asm volatile("s_waitcnt vmcnt(%0)" :: "n"(N) : "memory"); }
DEV unsigned epoch(const void* ws) {
  asm volatile("" : "+s"(ws));
  return *(const __attribute__((address_space(4))) unsigned*)ws + KCHK;
}
DEV f32x2 pkmul(f32x2 a, f32x2 b) { return a * b; }
DEV f32x2 pkadd(f32x2 a, f32x2 b) { return a + b; }
DEV f32x2 pkfma(f32x2 a, f32x2 b, f32x2 c) { return __builtin_elementwise_fma(a, b, c); }
DEV float dot2(unsigned a, unsigned b, float c) {
  return __builtin_amdgcn_fdot2_f32_bf16(__builtin_bit_cast(bf16x2, a), __builtin_bit_cast(bf16x2, b), c, false);
}
template <int CTRL> DEV float dppadd(float x) {
  return cut(x + __builtin_bit_cast(float, __builtin_amdgcn_update_dpp(0, __builtin_bit_cast(int, x), CTRL, 0xf, 0xf, true)));
}

#define BAR() bar()
#define TSX(k)
#define TSC(k)
#define TSSAVE()
#define BARARGS
#define BARPASS
#define TSINIT()
#define TSFIN()

struct Prod {
  u32x4 W[18];
  u32x4 ring[4][6];
};
struct ProdC {
  lds_char* smem;
  unsigned xw[2], rw, br[2], lw[2], lr[2], vd[2], voH, vw, lds0, ldsv;
  u32x2 I4;
  bool rvalid, odd;
  rsrc_t rhi, rlo, rwv;
};
DEV void lat_read(u32x4 (&B)[6], const ProdC& C, int t, int k0, int k1) {
#pragma unroll
  for (int ks = k0; ks < k1; ++ks) B[ks] = *(lds_u32x4*)(C.smem + C.br[ks & 1] + (t & 3) * 6144 + (ks >> 1) * 128);
}
DEV void lat_mm(const Prod& P, const u32x4 (&B)[6], const ProdC& C, int t) {
  const f32x4 z4 = {0.f, 0.f, 0.f, 0.f};
  f32x4 T[3];
#pragma unroll
  for (int ks = 0; ks < 6; ++ks)
#pragma unroll
    for (int tt = 0; tt < 3; ++tt) {
      T[tt] = mfma32(P.W[tt * 6 + ks], B[ks], ks == 0 ? z4 : T[tt]);
      OPQ(T[tt]);
      SB();
    }
#pragma unroll
  for (int tt = 0; tt < 3; ++tt) *(lds_f32x4*)(C.smem + C.lw[tt & 1] + (t & 1) * 12288 + (tt >> 1) * 128) = T[tt];
}
template <int M> DEV void ld_hi1(u32x4& d, const ProdC& C, unsigned so0, unsigned so1) { ldnt<(M >> 1) * 128>(d, C.rhi, C.voH, (M & 1) ? so1 : so0); }
DEV void ld_hi(u32x4 (&dst)[6], const ProdC& C, unsigned so) {
  const unsigned so1 = so + 196608;
  ld_hi1<0>(dst[0], C, so, so1); ld_hi1<1>(dst[1], C, so, so1); ld_hi1<2>(dst[2], C, so, so1);
  ld_hi1<3>(dst[3], C, so, so1); ld_hi1<4>(dst[4], C, so, so1); ld_hi1<5>(dst[5], C, so, so1);
}
template <int M, bool LOAD, bool LAT, int WAIT>
DEV void psub(Prod& P, const ProdC& C, int t, unsigned so, unsigned so1, f32x4 (&d)[6][2], f32x4 (&lat)[3][2], u32x4 (&B)[6],
              float (&ss)[2], float (&sm)[2]) {
  if constexpr (M < 6) {
    if (WAIT >= 0) vmwait<(WAIT >= 0 ? WAIT - M : 0)>();
    OPQ(P.ring[t & 3][M]);
    const u32x4 F = P.ring[t & 3][M];
    d[M][0] = mfma4(C.I4, u32x2{F[0], F[1]}, lat[M >> 1][0]);
    d[M][1] = mfma4(C.I4, u32x2{F[2], F[3]}, lat[M >> 1][1]);
    OPQ(d[M][0]); OPQ(d[M][1]);
    SB();
    if (LOAD) ld_hi1<M>(P.ring[t & 3][M], C, so, so1);
    if (M == 1) {
#pragma unroll
      for (int u = 0; u < 2; ++u) lat[2][u] = *(lds_f32x4*)(C.smem + C.lr[u] + (t & 1) * 12288 + 512);
    }
    if (LAT && M >= 3) lat_read(B, C, t + 1, 2 * (M - 3), 2 * (M - 3) + 2);
  }
  if constexpr (M > 0) {
    constexpr int k = M - 1, ab = k & 1, L = k >> 1;
    const u32x4 x = cvt8(d[k][0], d[k][1]);
    *(lds_u32x4*)(C.smem + C.xw[ab] + (t & 1) * 24576 + L * 8192) = x;
#pragma unroll
    for (int r = 0; r < 4; ++r) { ss[ab] = dot2(x[r], x[r], ss[ab]); sm[ab] = dot2(x[r], 0x3F803F80u, sm[ab]); }
  }
  SB();
}
template <bool LOAD, bool LAT, int NWV, int WAIT> DEV void pstep(Prod& P, const ProdC& C, int t, unsigned so, unsigned sl, unsigned wv BARARGS) {
  TSC(0);
  float ss[2] = {0.f, 0.f}, sm[2] = {0.f, 0.f};
  f32x4 d[6][2], lat[3][2];
  u32x4 B[6];
#pragma unroll
  for (int L = 0; L < 2; ++L)
#pragma unroll
    for (int u = 0; u < 2; ++u) lat[L][u] = *(lds_f32x4*)(C.smem + C.lr[u] + (t & 1) * 12288 + L * 256);
#pragma unroll
  for (int n = 0; n < NWV; ++n) dma16(C.rwv, C.vw, wv + n * 4096, C.ldsv + wv + n * 4096);
  if (LOAD) dma24(C.rlo, C.vd[0], C.vd[1], sl, C.lds0 + (t & 3) * 6144);
  const unsigned so1 = so + 196608;
  SB();
  TSC(1);
  psub<0, LOAD, LAT, WAIT>(P, C, t, so, so1, d, lat, B, ss, sm);
  psub<1, LOAD, LAT, WAIT>(P, C, t, so, so1, d, lat, B, ss, sm);
  TSC(2);
  psub<2, LOAD, LAT, WAIT>(P, C, t, so, so1, d, lat, B, ss, sm);
  psub<3, LOAD, LAT, WAIT>(P, C, t, so, so1, d, lat, B, ss, sm);
  TSC(3);
  psub<4, LOAD, LAT, WAIT>(P, C, t, so, so1, d, lat, B, ss, sm);
  psub<5, LOAD, LAT, WAIT>(P, C, t, so, so1, d, lat, B, ss, sm);
  psub<6, LOAD, LAT, WAIT>(P, C, t, so, so1, d, lat, B, ss, sm);
  TSC(4);
#pragma unroll
  for (int ab = 0; ab < 2; ++ab) {
    ss[ab] = dppadd<0x141>(dppadd<0x4E>(dppadd<0xB1>(ss[ab])));
    sm[ab] = dppadd<0x141>(dppadd<0x4E>(dppadd<0xB1>(sm[ab])));
  }
  const float mu = cut((C.odd ? sm[1] : sm[0]) * (1.0f / 192.0f));
  const float var = __builtin_fmaxf((C.odd ? ss[1] : ss[0]) * (1.0f / 192.0f) - mu * mu, 0.0f);
  const float rs = __builtin_amdgcn_rsqf(var + 1e-5f);
  if (C.rvalid) *(lds_f32*)(C.smem + C.rw + (t & 1) * 256) = rs;
  SB();
  TSC(5);
  if (LAT) lat_mm(P, B, C, t + 1);
  TSC(6);
}

struct Cons {
  f32x16 U[6];
  u32x4 Ab[12];
  float mrun, lsum;
};
struct ConsC { lds_char* smem; unsigned xr[4], xt[2][2], ra; };
DEV u32x4 row_read(const ConsC& C, int s) { return *(lds_u32x4*)(C.smem + C.xr[s & 3] + (s >> 2) * 8192); }
DEV u32x4 col_read(const ConsC& C, int o) {
  const int ks = o / 6, ct = o % 6;
  const u32x2 a0 = trread(C.smem + C.xt[ct & 1][0] + (ct >> 1) * 8192 + ks * 2048);
  const u32x2 a1 = trread(C.smem + C.xt[ct & 1][1] + (ct >> 1) * 8192 + ks * 2048);
  return u32x4{a0[0], a0[1], a1[0], a1[1]};
}
DEV void cstep(Cons& K, ConsC& C, int dx, int dr, bool first BARARGS) {
  TSC(0);
  u32x4 X[12];
  f32x4 rr[4];
#pragma unroll
  for (int s = 0; s < 12; ++s) X[s] = row_read(C, s);
#pragma unroll
  for (int a = 0; a < 4; ++a) rr[a] = *(lds_f32x4*)(C.smem + C.ra + a * 32);
  SB();
  f32x16 S = {0.f, 0.f, 0.f, 0.f, 0.f, 0.f, 0.f, 0.f, 0.f, 0.f, 0.f, 0.f, 0.f, 0.f, 0.f, 0.f};
#pragma unroll
  for (int s = 0; s < 12; ++s) {
    S = mfma3232(X[s], K.Ab[s], S);
    OPQ(S);
    SB();
    if (s == 0) TSC(1);
    if (s == 11) TSC(2);
    X[s] = col_read(C, s);
    SB();
  }
#pragma unroll
  for (int s = 0; s < 4; ++s) { C.xr[s] += dx; C.xt[s >> 1][s & 1] += dx; OPQ(C.xr[s]); OPQ(C.xt[s >> 1][s & 1]); }
  C.ra += dr; OPQ(C.ra);
  SB();
  float V[16];
  float vmax = -3e38f;
#pragma unroll
  for (int e = 0; e < 16; ++e) { V[e] = cut(S[e] * rr[e >> 2][e & 3] - K.mrun); vmax = __builtin_fmaxf(vmax, V[e]); }
  if (first || __builtin_amdgcn_ballot_w64(!(vmax <= THR)) != 0) {
    auto t = __builtin_amdgcn_permlane32_swap(f2u(vmax), f2u(vmax), false, false);
    float dl = __builtin_fmaxf(u2f(t[0]), u2f(t[1]));
    if (!first) dl = __builtin_fmaxf(dl, 0.f);
    const float al = __builtin_amdgcn_exp2f(__builtin_fminf(-dl, 0.f));
    K.mrun += dl; K.lsum *= al;
#pragma unroll
    for (int c = 0; c < 6; ++c) K.U[c] *= al;
#pragma unroll
    for (int e = 0; e < 16; ++e) V[e] -= dl;
  }
  TSC(3);
  u32x4 Pb[2];
  float pe[16];
#pragma unroll
  for (int e = 0; e < 8; ++e) pe[e] = __builtin_amdgcn_exp2f(V[e]);
#pragma unroll
  for (int e = 0; e < 8; e += 2) Pb[0][e >> 1] = cvtpk(cut(pe[e] * rr[e >> 2][e & 3]), cut(pe[e + 1] * rr[e >> 2][(e & 3) + 1]));
  SB();
  TSC(4);
#pragma unroll
  for (int ct = 0; ct < 6; ++ct) {
    K.U[ct] = mfma3232(X[ct], Pb[0], K.U[ct]);
    OPQ(K.U[ct]);
    SB();
    if (ct < 4) {
      const int e = 8 + 2 * ct;
      pe[e] = __builtin_amdgcn_exp2f(V[e]); pe[e + 1] = __builtin_amdgcn_exp2f(V[e + 1]);
      Pb[1][ct] = cvtpk(cut(pe[e] * rr[e >> 2][e & 3]), cut(pe[e + 1] * rr[e >> 2][(e & 3) + 1]));
    } else {
      const int e = 4 * (ct - 4);
      K.lsum += cut(pe[e] + pe[e + 1]) + cut(pe[e + 2] + pe[e + 3]);
    }
    SB();
  }
  TSC(5);
#pragma unroll
  for (int ct = 0; ct < 6; ++ct) {
    K.U[ct] = mfma3232(X[6 + ct], Pb[1], K.U[ct]);
    OPQ(K.U[ct]);
    SB();
    if (ct < 2) { const int e = 8 + 4 * ct; K.lsum += cut(pe[e] + pe[e + 1]) + cut(pe[e + 2] + pe[e + 3]); }
    SB();
  }
  TSC(6);
}
DEV void cfinish(Cons& K, lds_char* smem, int h, int b, rsrc_t rws, unsigned ek BARARGS) {
  const int lane = laneid(), i = lane & 15, g = lane >> 4;
  const int xo = (h >> 1) ? LDS_LAT : LDS_X, PH = h & 1;
  f32x4 Uo[12];
  lds_char* ex = smem + xo + PH * 12288 + lane * 16;
#pragma unroll
  for (int ct = 0; ct < 6; ++ct)
#pragma unroll
    for (int a = 0; a < 2; ++a) {
      f32x4 k0, k1;
#pragma unroll
      for (int r = 0; r < 4; ++r) {
        auto sw = __builtin_amdgcn_permlane16_swap(f2u(K.U[ct][8 * a + r]), f2u(K.U[ct][8 * a + 4 + r]), false, false);
        k0[r] = u2f(sw[0]); k1[r] = u2f(sw[1]);
      }
      Uo[2 * ct + a] = k0;
      *(lds_f32x4*)(ex + (2 * ct + a) * 1024) = k1;
      SB();
    }
  TSX(51);
  float mo, lo_;
  {
    auto t = __builtin_amdgcn_permlane32_swap(f2u(K.lsum), f2u(K.lsum), false, false);
    const float l = u2f(t[0]) + u2f(t[1]);
    auto sl = __builtin_amdgcn_permlane16_swap(f2u(l), f2u(l), false, false);
    auto sm = __builtin_amdgcn_permlane16_swap(f2u(K.mrun), f2u(K.mrun), false, false);
    mo = u2f(sm[0]); lo_ = u2f(sl[0]);
    *(__attribute__((address_space(3))) f32x2*)(smem + LDS_ML + h * 512 + lane * 8) = f32x2{u2f(sm[1]), u2f(sl[1])};
  }
  BAR();
  const f32x2 ml = *(__attribute__((address_space(3))) f32x2*)(smem + LDS_ML + (h ^ 1) * 512 + lane * 8);
  lds_char* ey = smem + xo + (1 - PH) * 12288 + lane * 16;
  f32x4 o[12];
#pragma unroll
  for (int c = 0; c < 12; ++c) o[c] = *(lds_f32x4*)(ey + c * 1024);
  lds_char* wv = smem + LDS_WV + h * 18432 + (8 * (g & 1) + 4 * (g >> 1) + (i >> 2)) * 96 + (i & 3) * 8;
  u32x4 Wv[18];
#pragma unroll
  for (int n = 0; n < 18; ++n) {
    const u32x2 a0 = trread(wv + (n / 3) * 3072 + (n % 3) * 32), a1 = trread(wv + (n / 3) * 3072 + 1536 + (n % 3) * 32);
    Wv[n] = u32x4{a0[0], a0[1], a1[0], a1[1]};
  }
  SB();
  const float mn = __builtin_fmaxf(mo, ml[0]);
  const float fa = __builtin_amdgcn_exp2f(mo - mn), fb = __builtin_amdgcn_exp2f(ml[0] - mn);
  const float l = lo_ * fa + ml[1] * fb, il = __builtin_amdgcn_rcpf(l);
  const unsigned lse = (unsigned)__builtin_amdgcn_ds_bpermute((4 * g + (i & 3)) * 4, (int)f2u(mn + __builtin_amdgcn_logf(l)));
  const f32x2 fa2 = {fa * il, fa * il}, fb2 = {fb * il, fb * il};
  f32x2 u2[24], us[2];
#pragma unroll
  for (int c = 0; c < 24; ++c) {
    const f32x2 own = {Uo[c >> 1][(c & 1) * 2], Uo[c >> 1][(c & 1) * 2 + 1]}, oth = {o[c >> 1][(c & 1) * 2], o[c >> 1][(c & 1) * 2 + 1]};
    u2[c] = pkfma(oth, fb2, pkmul(own, fa2));
    us[c & 1] = c < 2 ? u2[c] : pkadd(us[c & 1], u2[c]);
  }
  TSX(52);
  const float um = xg_sum((us[0][0] + us[0][1]) + (us[1][0] + us[1][1])) * (-1.0f / 192.0f);
  const f32x2 um2 = {um, um};
  const f32x4 z4 = {0.f, 0.f, 0.f, 0.f};
  f32x4 cx[3] = {z4, z4, z4};
#pragma unroll
  for (int s = 0; s < 6; ++s) {
    u32x4 ub;
#pragma unroll
    for (int r = 0; r < 4; ++r) { const f32x2 v = pkadd(u2[4 * s + r], um2); ub[r] = cvtpk(v[0], v[1]); }
#pragma unroll
    for (int dt = 0; dt < 3; ++dt) cx[dt] = mfma32(ub, Wv[3 * s + dt], cx[dt]);
  }
  TSX(53);
  const u32x4 ra = cvt8(cx[0], cx[1]);
  u32x4 rb = {cvtpk(cx[2][0], cx[2][1]), cvtpk(cx[2][2], cx[2][3]), lse, 0u};
  rb[3] = (ra[0] + ra[1] + ra[2]) + (ra[3] + rb[0] + rb[1]) + lse + ek;
  const unsigned vo = (lane >> 2) * 32768 + (lane & 3) * 16, so = WS_REC + h * 524288 + b * 64;
  bst4<SC1>(ra, rws, vo, so);
  bst4<SC1>(rb, rws, vo, so + 16384);
}

struct AIn { u32x4 q[6]; };
DEV void a_issue(AIn& I, unsigned lbase, int h, int cs, const void* q_, const void* wq_, const void* wk_, const void* wm_) {
  const int lane = laneid();
  const rsrc_t rq = mkrsrc(q_, 6144u), rwq = mkrsrc(wq_, 73728u), rwk = mkrsrc(wk_, 73728u), rwm = mkrsrc(wm_, 73728u);
  const unsigned vl = lane * 16;
#pragma unroll
  for (int n = 0; n < 18; ++n) dma16(rwq, vl, h * 18432 + n * 1024, lbase + LDS_AWQ + n * 1024);
  unsigned vo[3];
#pragma unroll
  for (int m = 0; m < 3; ++m) { const unsigned o = m * 1024 + vl; vo[m] = (o / 96) * 384 + o % 96; }
#pragma unroll
  for (int n = 0; n < 18; ++n) dma16(rwm, vo[n % 3], (n / 3) * 12288 + cs * 96, lbase + LDS_AWM + n * 1024);
#pragma unroll
  for (int n = 0; n < 5; ++n) dma16(rwk, vl, h * 18432 + cs * 4608 + n * 1024, lbase + LDS_AWK + n * 1024);
  const unsigned vq = (lane & 15) * 384 + (lane >> 4) * 16;
  ldpl<0>(I.q[0], rq, vq); ldpl<64>(I.q[1], rq, vq); ldpl<128>(I.q[2], rq, vq);
  ldpl<192>(I.q[3], rq, vq); ldpl<256>(I.q[4], rq, vq); ldpl<320>(I.q[5], rq, vq);
}
DEV void a_compute(AIn& I, lds_char* smem, int h, int cs, rsrc_t rws, unsigned ek) {
  const int lane = laneid(), i = lane & 15, g = lane >> 4;
  vmwait<0>();
#pragma unroll
  for (int s = 0; s < 6; ++s) OPQ(I.q[s]);
  SB();
  lds_char* a = smem + (8 * g + (i >> 2)) * 96 + (i & 3) * 8;
  const f32x4 z4 = {0.f, 0.f, 0.f, 0.f};
  f32x4 Q[3], M[3], T[3];
  u32x4 W[18], V[18];
  u32x2 Kf[9];
#pragma unroll
  for (int n = 0; n < 18; ++n) {
    const u32x2 a0 = trread(a + LDS_AWQ + (n / 3) * 3072 + (n % 3) * 32), a1 = trread(a + LDS_AWQ + (n / 3) * 3072 + 384 + (n % 3) * 32);
    W[n] = u32x4{a0[0], a0[1], a1[0], a1[1]};
  }
#pragma unroll
  for (int n = 0; n < 18; ++n) {
    const u32x2 a0 = trread(a + LDS_AWM + (n / 3) * 3072 + (n % 3) * 32), a1 = trread(a + LDS_AWM + (n / 3) * 3072 + 384 + (n % 3) * 32);
    V[n] = u32x4{a0[0], a0[1], a1[0], a1[1]};
  }
#pragma unroll
  for (int n = 0; n < 9; ++n) Kf[n] = *(__attribute__((address_space(3))) u32x2*)(smem + LDS_AWK + i * 96 + g * 8 + (n / 3) * 1536 + (n % 3) * 32);
  SB();
#pragma unroll
  for (int n = 0; n < 18; ++n) Q[n % 3] = mfma32(W[n], I.q[n / 3], n < 3 ? z4 : Q[n % 3]);
#pragma unroll
  for (int n = 0; n < 18; ++n) M[n % 3] = mfma32(V[n], I.q[n / 3], n < 3 ? z4 : M[n % 3]);
#pragma unroll
  for (int dt = 0; dt < 3; ++dt) {
    const u32x2 qb = {cvtpk(Q[dt][0], Q[dt][1]), cvtpk(Q[dt][2], Q[dt][3])};
#pragma unroll
    for (int ct = 0; ct < 3; ++ct) T[ct] = mfma16(Kf[3 * ct + dt], qb, dt == 0 ? z4 : T[ct]);
  }
  const float s1 = 1.4426950408889634f * 0.14433756729740643f;
  unsigned P[6];
#pragma unroll
  for (int n = 0; n < 6; ++n)
    P[n] = cvtpk((T[n >> 1][(n & 1) * 2] + 0.5f * M[n >> 1][(n & 1) * 2]) * s1, (T[n >> 1][(n & 1) * 2 + 1] + 0.5f * M[n >> 1][(n & 1) * 2 + 1]) * s1);
  const unsigned so = WS_A + h * 8192 + cs * 2048;
  bst4<SC1>(u32x4{P[0], P[1], P[2], P[0] + P[1] + P[2] + ek}, rws, lane * 16, so);
  bst4<SC1>(u32x4{P[4], P[5], P[3], P[4] + P[5] + P[3] + ek}, rws, lane * 16 + 1024, so);
}
DEV void a_poll(u32x4 (&G)[8], int h, rsrc_t rws, unsigned ek) {
  const int lane = laneid();
  for (int spin = 0;; ++spin) {
    asm volatile("" ::: "memory");
#pragma unroll
    for (int n = 0; n < 8; ++n) G[n] = bld4<SC1>(rws, lane * 16 + (n & 3) * 1024, WS_A + h * 8192 + (n >> 2) * 4096);
    bool bad = false;
#pragma unroll
    for (int n = 0; n < 8; ++n) bad |= G[n][0] + G[n][1] + G[n][2] + ek != G[n][3];
    if (__builtin_amdgcn_ballot_w64(bad) == 0 || spin >= SPIN_MAX) break;
  }
}
DEV void a_centre(const u32x4 (&G)[8], lds_char* smem, int h) {
  const int lane = laneid(), i = lane & 15, g = lane >> 4;
  float sm[2] = {0.f, 0.f};
#pragma unroll
  for (int n = 0; n < 24; ++n) sm[n & 1] = dot2(G[n / 3][n % 3], 0x3F803F80u, sm[n & 1]);
  const float mu = xg_sum(sm[0] + sm[1]) * (-1.0f / 192.0f);
  const f32x4 mu4 = {mu, mu, mu, mu};
  const int d = lane & 3;
  const u32x2 I4 = {d == 0 ? 0x00003F80u : (d == 1 ? 0x3F800000u : 0u), d == 2 ? 0x00003F80u : (d == 3 ? 0x3F800000u : 0u)};
  lds_char* dst = smem + LDS_AF + (h >> 1) * 12288 + (g >> 1) * 512 + ((h & 1) * 16 + i) * 16 + (g & 1) * 8;
#pragma unroll
  for (int s = 0; s < 12; ++s) {
    const int cs = s / 3, ct = s % 3;
    const u32x2 v = ct == 0 ? u32x2{G[2 * cs][0], G[2 * cs][1]} : (ct == 1 ? u32x2{G[2 * cs][2], G[2 * cs + 1][2]} : u32x2{G[2 * cs + 1][0], G[2 * cs + 1][1]});
    const f32x4 c = mfma4(I4, v, mu4);
    *(__attribute__((address_space(3))) u32x2*)(dst + s * 1024) = u32x2{cvtpk(c[0], c[1]), cvtpk(c[2], c[3])};
  }
}
template <int CTRL> DEV float dppmov(float x) { return __builtin_bit_cast(float, __builtin_amdgcn_update_dpp(0, __builtin_bit_cast(int, x), CTRL, 0xf, 0xf, true)); }
DEV float quads_sum(float x) { x += dppmov<0x124>(x); return x + dppmov<0x128>(x); }
DEV float quads_max(float x) { x = __builtin_fmaxf(x, dppmov<0x124>(x)); return __builtin_fmaxf(x, dppmov<0x128>(x)); }
DEV void reduce1(lds_char* smem, int r, int p, rsrc_t rws, unsigned ek) {
  const int lane = laneid(), i = lane & 15, g = lane >> 4, d = lane & 3;
  const unsigned m0 = d == 0 ? 0x0000FFFFu : (d == 1 ? 0xFFFF0000u : 0u), m1 = d == 2 ? 0x0000FFFFu : (d == 3 ? 0xFFFF0000u : 0u);
  u32x4 RA[4], RB[4];
  const unsigned so = WS_REC + r * 32768 + p * 4096;
  for (int spin = 0;; ++spin) {
    asm volatile("" ::: "memory");
#pragma unroll
    for (int j = 0; j < 4; ++j) { RB[j] = bld4<SC1>(rws, lane * 16 + j * 1024, so + 16384); RA[j] = bld4<SC1>(rws, lane * 16 + j * 1024, so); }
    bool bad = false;
#pragma unroll
    for (int j = 0; j < 4; ++j) bad |= (RA[j][0] + RA[j][1] + RA[j][2]) + (RA[j][3] + RB[j][0] + RB[j][1]) + RB[j][2] + ek != RB[j][3];
    if (__builtin_amdgcn_ballot_w64(bad) == 0 || spin >= SPIN_MAX) break;
  }
  const float M = xg_max(quads_max(__builtin_fmaxf(__builtin_fmaxf(u2f(RB[0][2]), u2f(RB[1][2])), __builtin_fmaxf(u2f(RB[2][2]), u2f(RB[3][2])))));
  const f32x4 z4 = {0.f, 0.f, 0.f, 0.f};
  f32x4 acc[3] = {z4, z4, z4};
  float L = 0.f;
#pragma unroll
  for (int j = 0; j < 4; ++j) {
    const float wf = __builtin_amdgcn_exp2f(u2f(RB[j][2]) - M);
    const unsigned wb = cvtpk(wf, wf);
    L += u2f(wb & 0xFFFF0000u);
    const u32x2 dg = {wb & m0, wb & m1};
    acc[0] = mfma4(dg, u32x2{RA[j][0], RA[j][1]}, acc[0]);
    acc[1] = mfma4(dg, u32x2{RA[j][2], RA[j][3]}, acc[1]);
    acc[2] = mfma4(dg, u32x2{RB[j][0], RB[j][1]}, acc[2]);
  }
  L = xg_sum(quads_sum(L));
  float u[6];
  f32x4 v;
#pragma unroll
  for (int k = 0; k < 6; ++k) {
    auto t = __builtin_amdgcn_permlane32_swap(f2u(acc[k % 3][k / 3]), f2u(acc[k % 3][2 + k / 3]), false, false);
    u[k] = u2f(t[0]) + u2f(t[1]);
  }
#pragma unroll
  for (int k = 0; k < 3; ++k) {
    auto t = __builtin_amdgcn_permlane16_swap(f2u(u[k]), f2u(u[k + 3]), false, false);
    v[k] = quads_sum(u2f(t[0]) + u2f(t[1]));
  }
  v[3] = 0.f;
  if (i < 4) *(lds_f32x4*)(smem + LDS_RED + p * 256 + g * 64 + d * 16) = v;
  if (lane < 4) *(__attribute__((address_space(3))) f32x2*)(smem + LDS_RED + 1024 + p * 32 + d * 8) = f32x2{M, L};
  bar();
  if (p == 0) {
    const f32x4 x = *(lds_f32x4*)(smem + LDS_RED + (i >> 2) * 256 + g * 64 + d * 16);
    const f32x2 ml = *(__attribute__((address_space(3))) f32x2*)(smem + LDS_RED + 1024 + (i >> 2) * 32 + g * 8);
    const float f = __builtin_amdgcn_exp2f(ml[0] - quads_max(ml[0]));
    const float il = __builtin_amdgcn_rcpf(quads_sum(ml[1] * f));
    u32x4 o;
#pragma unroll
    for (int k = 0; k < 3; ++k) { const float c = quads_sum(x[k] * f) * il; o[k] = cvtpk(c, dppmov<0xB1>(c)); }
    o[3] = o[0] + o[1] + o[2] + ek;
    if (i == 0 || i == 2) bst4<SC1>(o, rws, (d >> 1) * 64 + g * 16, WS_CTX + r * 128);
  }
}
DEV void reduce2_issue(u32x2& qr, unsigned lbase, int nt, const void* q_, const void* wo_) {
  const int lane = laneid();
  const rsrc_t rq = mkrsrc(q_, 6144u), rwo = mkrsrc(wo_, 73728u);
  const unsigned vo = (lane >> 1) * 384 + (lane & 1) * 16, vq = (lane & 15) * 384 + (lane >> 4) * 8;
#pragma unroll
  for (int n = 0; n < 6; ++n) dma16(rwo, vo, n * 12288 + nt * 32, lbase + LDS_WO + n * 1024);
  asm volatile("buffer_load_dwordx2 %0, %1, %2, %3 offen" : "=v"(qr) : "v"(vq), "s"(rq), "s"(nt * 32) : "memory");
}
DEV void reduce2(u32x2& qr, lds_char* smem, int nt, rsrc_t rws, unsigned ek, void* out_) {
  const int lane = laneid(), i = lane & 15, g = lane >> 4, rho = i >> 2;
  vmwait<0>();
  OPQ(qr);
  SB();
  lds_char* a0 = smem + LDS_WO + (16 * (rho >> 1) + 4 * g + (rho & 1)) * 32 + (i & 3) * 8;
  lds_char* a1 = smem + LDS_WO + (32 + 4 * g + (rho & 1)) * 32 + (i & 3) * 8;
  u32x4 W[8], G[8];
#pragma unroll
  for (int m = 0; m < 8; ++m) {
    const u32x2 x0 = trread(a0 + ((m >> 1) * 48 + 2 * (m & 1)) * 32), x1 = trread(a1 + ((m >> 1) * 48 + 2 * (m & 1)) * 32);
    W[m] = u32x4{x0[0], x0[1], x1[0], x1[1]};
    OPQ(W[m]);
  }
  const unsigned vo = (i >> 2) * 512 + g * 128 + (i & 3) * 16;
  for (int spin = 0;; ++spin) {
    asm volatile("" ::: "memory");
#pragma unroll
    for (int m = 0; m < 8; ++m) G[m] = bld4<SC1>(rws, vo + (m & 1) * 64 + ((m >> 1) & 1) * 2048, WS_CTX + (m >> 2) * 4096);
    bool bad = false;
#pragma unroll
    for (int m = 0; m < 8; ++m) bad |= G[m][0] + G[m][1] + G[m][2] + ek != G[m][3];
    if (__builtin_amdgcn_ballot_w64(bad) == 0 || spin >= SPIN_MAX) break;
  }
  f32x4 c[2] = {{u2f(qr[0] << 16), u2f(qr[0] & 0xFFFF0000u), u2f(qr[1] << 16), u2f(qr[1] & 0xFFFF0000u)}, {0.f, 0.f, 0.f, 0.f}};
#pragma unroll
  for (int m = 0; m < 8; ++m) { G[m][3] = 0u; c[m & 1] = mfma32(W[m], G[m], c[m & 1]); }
  const f32x4 o = c[0] + c[1];
  __builtin_amdgcn_raw_buffer_store_b64(u32x2{cvtpk(o[0], o[1]), cvtpk(o[2], o[3])}, mkrsrc(out_, 6144u), i * 384 + g * 8, nt * 32, 0);
  if (nt == 0 && lane == 0) __builtin_amdgcn_raw_buffer_store_b32(ek - KCHK + 1u, rws, 0, 0, 0);
}

__global__ void __launch_bounds__(64)
m2f_prep(const void* __restrict__ q_, const void* __restrict__ wq_, const void* __restrict__ wk_, const void* __restrict__ wm_, void* __restrict__ ws_) {
  __shared__ __attribute__((aligned(16))) char smem_[LDS_TOTAL];
  lds_char* smem = (lds_char*)smem_;
  const int b = __builtin_amdgcn_workgroup_id_x();
  AIn I;
  a_issue(I, (unsigned)(uintptr_t)smem, b >> 2, b & 3, q_, wq_, wk_, wm_);
  const unsigned ek = epoch(ws_);
  a_compute(I, smem, b >> 2, b & 3, mkrsrc(ws_, WS_BYTES), ek);
}

__global__ void __launch_bounds__(512, 2)
m2f_main(const void* __restrict__ fhi_, const void* __restrict__ flo_, const void* __restrict__ wlat_,
         const void* __restrict__ wv_, const void* __restrict__ q_, const void* __restrict__ wq_, const void* __restrict__ wk_,
         const void* __restrict__ wm_, const void* __restrict__ wo_, void* __restrict__ ws_, void* __restrict__ out_) {
  __shared__ __attribute__((aligned(16))) char smem_[LDS_TOTAL];
  lds_char* smem = (lds_char*)smem_;
  const int w = __builtin_amdgcn_readfirstlane(__builtin_amdgcn_workitem_id_x() >> 6);
  const int b = __builtin_amdgcn_workgroup_id_x();
  TSINIT();

  if (w < 4) {
    const int p = w, lane = laneid(), i = lane & 15, g = lane >> 4;
#define PRIO_P 2
    __builtin_amdgcn_s_setprio(PRIO_P);
    const rsrc_t rwl = mkrsrc(wlat_, 73728u);
    ProdC C;
    C.smem = smem;
    C.rhi = mkrsrc(fhi_, 100663296u);
    C.rlo = mkrsrc(flo_, 25165824u);
    C.rwv = mkrsrc(wv_, 73728u);
    const unsigned j = lane >> 3, c7 = lane & 7;
    const unsigned lbase = (unsigned)(uintptr_t)smem;
    C.voH = j * 384 + c7 * 16;
    C.vw = lane * 16;
    C.lds0 = lbase + LDS_LO + p * 1536;
    C.ldsv = lbase + LDS_WV;
#pragma unroll
    for (int k = 0; k < 2; ++k) {
      const unsigned o = k * 1024 + (lane & (k ? 31 : 63)) * 16, px = o / 384, pc = (o % 384) / 16;
      C.vd[k] = px * 384 + ((pc ^ (2 * p + (px >> 1))) * 16);
    }
    const unsigned soL = (unsigned)(b * 256 + p * 4) * 384u;
    const unsigned soH = (unsigned)(b * 1024 + p * 8) * 384u;
    Prod P;
    dma24(C.rlo, C.vd[0], C.vd[1], soL, C.lds0);
    dma24(C.rlo, C.vd[0], C.vd[1], soL + 6144, C.lds0 + 6144);
    SB();
    {
      unsigned vo[6];
#pragma unroll
      for (int m = 0; m < 6; ++m) {
        const unsigned o = m * 1024 + lane * 16, k = o / 384, pc = (o % 384) / 16;
        vo[m] = k * 384 + ((pc ^ (2 * (((k >> 1) & 1) | ((k >> 3) << 1)))) * 16);
      }
      const unsigned so1 = soH + 196608, wo = p * 18432, wl = lbase + LDS_WL + p * 18432;
#define WGRP(M) ld_hi1<M>(P.ring[0][M], C, soH, so1); \
      for (int n = 3 * M; n < 3 * M + 3; ++n) dma16(rwl, vo[n % 6], wo + (n / 6) * 6144, wl + n * 1024);
      WGRP(0) WGRP(1) WGRP(2) WGRP(3) WGRP(4) WGRP(5)
#undef WGRP
    }
    SB();
    ld_hi(P.ring[1], C, soH + 12288);
    dma24(C.rlo, C.vd[0], C.vd[1], soL + 2 * 6144, C.lds0 + 2 * 6144);
    SB();
    {
      const int d = lane & 3;
      C.I4[0] = d == 0 ? 0x00003F80u : (d == 1 ? 0x3F800000u : 0u);
      C.I4[1] = d == 2 ? 0x00003F80u : (d == 3 ? 0x3F800000u : 0u);
#pragma unroll
      for (int ab = 0; ab < 2; ++ab) {
        C.xw[ab] = LDS_X + (16 * p + 8 * ab + j) * 128 + ((c7 ^ (((j >> 1) & 1) * 4 + 2 * ab + (j >> 2))) * 16);
        C.br[ab] = LDS_LO + i * 384 + (((4 * ab + g) ^ (i >> 1)) * 16);
        C.lw[ab] = LDS_LAT + i * 768 + (((12 * p + 4 * ab + g) ^ (i & 7)) * 16);
        C.lr[ab] = LDS_LAT + (4 * p + (j >> 1)) * 768 + (((2 * c7 + ab) ^ ((4 * p + (j >> 1)) & 7)) * 16);
      }
      C.rvalid = c7 < 2; C.odd = lane & 1;
      C.rw = LDS_R + (16 * p + 8 * (lane & 1) + j) * 4;
#pragma unroll
      for (int ab = 0; ab < 2; ++ab) { OPQ(C.xw[ab]); OPQ(C.br[ab]); OPQ(C.lw[ab]); OPQ(C.lr[ab]); OPQ(C.vd[ab]); }
      OPQ(C.rw); OPQ(C.vw);
    }
    SB();
    unsigned ek = epoch(ws_);
    SB();
    vmwait<8>();
    asm volatile("" : "+s"(ek));
    BAR();
    ld_hi(P.ring[2], C, soH + 2 * 12288);
    SB();
    {
      const unsigned rp = i >> 2, cp = i & 3, s = (rp >> 1) | ((g & 1) << 1);
      u32x4 B[6];
      lat_read(B, C, 0, 0, 6);
#pragma unroll
      for (int tt = 0; tt < 3; ++tt) {
        const unsigned ct = 3 * p + tt;
        lds_char* a = smem + LDS_WL + (8 * g + rp) * 384 + (((ct & 3) ^ s) * 32) + cp * 8 + (ct >> 2) * 128;
#pragma unroll
        for (int ks = 0; ks < 6; ++ks) {
          const u32x2 a0 = trread(a + ks * 12288), a1 = trread(a + ks * 12288 + 1536);
          P.W[tt * 6 + ks] = u32x4{a0[0], a0[1], a1[0], a1[1]};
        }
      }
      SB();
      lat_mm(P, B, C, 0);
    }
    BAR();
    dma24(C.rlo, C.vd[0], C.vd[1], soL + 3 * 6144, C.lds0 + 3 * 6144);
    ld_hi(P.ring[3], C, soH + 3 * 12288);
    SB();
#pragma unroll 1
    for (int u = 0; u < 3; ++u) {
      const unsigned so = soH + (u + 1) * 49152, sl = soL + (u + 1) * 24576, wv = p * 1024 + u * 20480;
      pstep<true, true, 1, -1>(P, C, 0, so, sl, wv BARPASS); vmwait<22>(); BAR();
      pstep<true, true, 1, -1>(P, C, 1, so + 12288, sl + 6144, wv + 4096 BARPASS); vmwait<22>(); BAR();
      pstep<true, true, 1, -1>(P, C, 2, so + 2 * 12288, sl + 2 * 6144, wv + 2 * 4096 BARPASS); vmwait<22>(); BAR();
      pstep<true, true, 2, -1>(P, C, 3, so + 3 * 12288, sl + 3 * 6144, wv + 3 * 4096 BARPASS); vmwait<22>(); BAR();
    }
    pstep<false, true, 0, -1>(P, C, 0, 0, 0, 0 BARPASS); vmwait<14>(); BAR();
    pstep<false, true, 0, -1>(P, C, 1, 0, 0, 0 BARPASS); vmwait<6>(); TSSAVE(); BAR();
    pstep<false, true, 3, -1>(P, C, 2, 0, 0, p * 1024 + 61440 BARPASS); BAR();
    pstep<false, false, 0, 8>(P, C, 3, 0, 0, 0 BARPASS); BAR();
    const bool l2 = p == 0 && b >= L2_B0 && b < L2_B0 + 12;
    u32x2 qr;
    if (l2) { reduce2_issue(qr, lbase, b - L2_B0, q_, wo_); vmwait<7>(); } else vmwait<0>();
    BAR();
    if (b >= L1_B0 && b < L1_B0 + 64) reduce1(smem, b - L1_B0, p, mkrsrc(ws_, WS_BYTES), ek);
    if (l2) reduce2(qr, smem, b - L2_B0, mkrsrc(ws_, WS_BYTES), ek, out_);
  } else {
    const int h = w - 4, hp = h >> 1, ph = h & 1, lane = laneid(), i = lane & 15, g = lane >> 4;
    const rsrc_t rws = mkrsrc(ws_, WS_BYTES);
    Cons K;
#define PRIO_C 0
    __builtin_amdgcn_s_setprio(PRIO_C);
    unsigned ek = epoch(ws_);
    asm volatile("" : "+s"(ek));
    u32x4 G[8];
    a_poll(G, h, rws, ek);
    BAR();
    BAR();
    a_centre(G, smem, h);
    ConsC C;
    C.smem = smem;
    const unsigned hl = lane >> 5;
    {
      const unsigned m = lane & 31, row = 32 * ph + m, f = ((m >> 1) & 1) * 4 + ((m >> 2) & 3);
#pragma unroll
      for (int s = 0; s < 4; ++s) C.xr[s] = LDS_X + row * 128 + (((2 * s + hl) ^ f) * 16);
      const unsigned cb = g & 1, rp = i >> 2, cp = i & 3;
#pragma unroll
      for (int c = 0; c < 2; ++c)
#pragma unroll
        for (int sec = 0; sec < 2; ++sec)
          C.xt[c][sec] = LDS_X + (32 * ph + 8 * sec + 4 * hl + rp) * 128 + (((4 * c + 2 * cb + (cp >> 1)) ^ ((rp >> 1) * 4 + hl + 2 * sec)) * 16) + (cp & 1) * 8;
      C.ra = LDS_R + (32 * ph + 4 * hl) * 4;
#pragma unroll
      for (int s = 0; s < 4; ++s) { OPQ(C.xr[s]); OPQ(C.xt[s >> 1][s & 1]); }
      OPQ(C.ra);
    }
#pragma unroll
    for (int c = 0; c < 6; ++c)
#pragma unroll
      for (int e = 0; e < 16; ++e) K.U[c][e] = 0.f;
    K.mrun = 0.f; K.lsum = 0.f;
#pragma unroll 1
    for (int t = 0; t < 16; ++t) {
      BAR();
      if (t == 0) {
        lds_char* af = smem + LDS_AF + hp * 12288 + (laneid() ^ (16 * ph)) * 16;
#pragma unroll
        for (int s = 0; s < 12; ++s) K.Ab[s] = *(lds_u32x4*)(af + s * 1024);
      }
      cstep(K, C, (t & 1) ? -24576 : 24576, (t & 1) ? -256 : 256, t == 0 BARPASS);
    }
    TSX(50);
    cfinish(K, smem, h, b, rws, ek BARPASS);
  }
  TSFIN();
}

extern "C" void kernel_launch(void* const* d_in, const int* in_sizes, int n_in, void* d_out, int out_size, void* d_ws,
                              size_t ws_size, hipStream_t stream) {
  m2f_prep<<<16, 64, 0, stream>>>(d_in[0], d_in[4], d_in[5], d_in[8], d_ws);
  m2f_main<<<256, 512, 0, stream>>>(d_in[1], d_in[2], d_in[3], d_in[6], d_in[0], d_in[4], d_in[5], d_in[8], d_in[7], d_ws, d_out);
}
```

```cpp
#define PROBE 0
#define PROBE_B 128
#include <hip/hip_runtime.h>
#include <stdint.h>

typedef __attribute__((ext_vector_type(8))) __bf16 bf16x8;
typedef __attribute__((ext_vector_type(2))) __bf16 bf16x2;
typedef __attribute__((ext_vector_type(4))) short s16x4;
typedef __attribute__((ext_vector_type(4))) float f32x4;
typedef __attribute__((ext_vector_type(2))) float f32x2;
typedef __attribute__((ext_vector_type(4))) unsigned u32x4;
typedef __attribute__((ext_vector_type(2))) unsigned u32x2;
typedef __attribute__((address_space(3))) char lds_char;
typedef __attribute__((address_space(3))) s16x4 lds_s16x4;
typedef __attribute__((address_space(3))) u32x4 lds_u32x4;
typedef __attribute__((address_space(3))) f32x4 lds_f32x4;
typedef __attribute__((address_space(3))) float lds_f32;

typedef __attribute__((ext_vector_type(16))) float f32x16;
#define DEV __device__ __forceinline__
#define SB() __builtin_amdgcn_sched_barrier(0)
#define OPQ(x) asm volatile("" : "+v"(x))

constexpr int LDS_X = 0;
constexpr int LDS_WL = 24576;
constexpr int LDS_WV = 49152;
constexpr int LDS_LO = 110592;
constexpr int LDS_LAT = 135168;
constexpr int LDS_R = 159744;
constexpr int LDS_ML = 160256;
constexpr int LDS_TOTAL = 162304;
constexpr int LDS_AF = 86016;
constexpr int LDS_AWQ = 0, LDS_AWM = LDS_LAT, LDS_AWK = LDS_LAT + 18432;
constexpr int LDS_WO = LDS_LO + 2 * 6144, LDS_RED = LDS_LO + 3 * 6144;
constexpr float THR = 16.0f;
constexpr unsigned WS_A = 4096, WS_REC = 65536, WS_CTX = WS_REC + 64 * 32768, WS_BYTES = WS_CTX + 8192;
constexpr unsigned KCHK = 0x9E3779B9u;
constexpr int SC1 = 16, SPIN_MAX = 1 << 15;
constexpr int L1_B0 = 64, L2_B0 = 128;
#define APOLL 0

DEV float cut(float x) { asm("" : "+v"(x)); return x; }
DEV unsigned cvtpk(float lo, float hi) {
  f32x2 v = {lo, hi};
  return __builtin_bit_cast(unsigned, __builtin_convertvector(v, bf16x2));
}
DEV u32x4 cvt8(f32x4 a, f32x4 b) { return u32x4{cvtpk(a[0], a[1]), cvtpk(a[2], a[3]), cvtpk(b[0], b[1]), cvtpk(b[2], b[3])}; }
DEV f32x4 mfma32(u32x4 a, u32x4 b, f32x4 c) {
  return __builtin_amdgcn_mfma_f32_16x16x32_bf16(__builtin_bit_cast(bf16x8, a), __builtin_bit_cast(bf16x8, b), c, 0, 0, 0);
}
DEV f32x16 mfma3232(u32x4 a, u32x4 b, f32x16 c) {
  return __builtin_amdgcn_mfma_f32_32x32x16_bf16(__builtin_bit_cast(bf16x8, a), __builtin_bit_cast(bf16x8, b), c, 0, 0, 0);
}
DEV unsigned f2u(float x) { return __builtin_bit_cast(unsigned, x); }
DEV float u2f(unsigned x) { return __builtin_bit_cast(float, x); }
DEV f32x4 mfma16(u32x2 a, u32x2 b, f32x4 c) {
  return __builtin_amdgcn_mfma_f32_16x16x16bf16_1k(__builtin_bit_cast(s16x4, a), __builtin_bit_cast(s16x4, b), c, 0, 0, 0);
}
DEV f32x4 mfma4(u32x2 a, u32x2 b, f32x4 c) {
  return __builtin_amdgcn_mfma_f32_4x4x4bf16_1k(__builtin_bit_cast(s16x4, a), __builtin_bit_cast(s16x4, b), c, 0, 0, 0);
}
DEV u32x2 trread(lds_char* p) {
  return __builtin_bit_cast(u32x2, __builtin_amdgcn_ds_read_tr16_b64_v4i16((lds_s16x4*)p));
}
template <int CTRL, int BANK> DEV unsigned dppu(unsigned old, unsigned src) {
  return (unsigned)__builtin_amdgcn_update_dpp((int)old, (int)src, CTRL, 0xf, BANK, false);
}
template <int CTRL, int BANK> DEV float dppf(float old, float src) {
  return __builtin_bit_cast(float, dppu<CTRL, BANK>(__builtin_bit_cast(unsigned, old), __builtin_bit_cast(unsigned, src)));
}
constexpr int DPP_ID = 0xE4, DPP_ROR8 = 0x128;
DEV float xg_sum(float x) {
  auto s = __builtin_amdgcn_permlane16_swap(f2u(x), f2u(x), false, false);
  x = u2f(s[0]) + u2f(s[1]);
  auto t = __builtin_amdgcn_permlane32_swap(f2u(x), f2u(x), false, false);
  return u2f(t[0]) + u2f(t[1]);
}
DEV float xg_max(float x) {
  auto s = __builtin_amdgcn_permlane16_swap(f2u(x), f2u(x), false, false);
  x = __builtin_fmaxf(u2f(s[0]), u2f(s[1]));
  auto t = __builtin_amdgcn_permlane32_swap(f2u(x), f2u(x), false, false);
  return __builtin_fmaxf(u2f(t[0]), u2f(t[1]));
}
DEV int laneid() {
  int l;
  asm volatile("v_mbcnt_lo_u32_b32 %0, -1, 0\n\tv_mbcnt_hi_u32_b32 %0, -1, %0" : "=v"(l));
  __builtin_assume(l >= 0 && l < 64);
  return l;
}
DEV void bar() { SB(); __builtin_amdgcn_s_waitcnt(0xC07F); asm volatile("s_barrier" ::: "memory"); SB(); }
typedef __amdgpu_buffer_rsrc_t rsrc_t;
DEV rsrc_t mkrsrc(const void* p, unsigned bytes) { return __builtin_amdgcn_make_buffer_rsrc((void*)p, (short)0, (int)bytes, 0x00020000); }
template <int AUX> DEV u32x4 bld4(rsrc_t r, unsigned voff, unsigned soff) { return __builtin_amdgcn_raw_buffer_load_b128(r, voff, soff, AUX); }
template <int AUX> DEV u32x2 bld2(rsrc_t r, unsigned voff, unsigned soff) { return __builtin_amdgcn_raw_buffer_load_b64(r, voff, soff, AUX); }
constexpr int NT = 2;

DEV void dma16(rsrc_t r, unsigned voff, unsigned soff, unsigned ldsdst) {
  asm volatile("s_mov_b32 m0, %0\n\ts_nop 0\n\tbuffer_load_dwordx4 %1, %2, %3 offen lds" :: "s"(ldsdst), "v"(voff), "s"(r), "s"(soff) : "memory");
}
DEV void dma24(rsrc_t r, unsigned v0, unsigned v1, unsigned soff, unsigned ldsdst) {
  asm volatile("s_mov_b32 m0, %0\n\ts_nop 0\n\tbuffer_load_dwordx4 %1, %3, %4 offen nt lds\n\t"
               "s_mov_b32 exec_hi, 0\n\ts_add_u32 m0, m0, 0x400\n\ts_nop 0\n\tbuffer_load_dwordx4 %2, %3, %4 offen nt lds\n\ts_mov_b32 exec_hi, -1"
               :: "s"(ldsdst), "v"(v0), "v"(v1), "s"(r), "s"(soff) : "memory", "scc");
}
template <int OFF> DEV void ldnt(u32x4& d, rsrc_t r, unsigned voff, unsigned soff) {
  asm volatile("buffer_load_dwordx4 %0, %1, %2, %3 offen offset:%4 nt" : "=v"(d) : "v"(voff), "s"(r), "s"(soff), "n"(OFF) : "memory");
}
template <int OFF> DEV void ldpl(u32x4& d, rsrc_t r, unsigned voff) {
  asm volatile("buffer_load_dwordx4 %0, %1, %2, 0 offen offset:%3" : "=v"(d) : "v"(voff), "s"(r), "n"(OFF) : "memory");
}
template <int AUX> DEV void bst4(u32x4 v, rsrc_t r, unsigned voff, unsigned soff) { __builtin_amdgcn_raw_buffer_store_b128(v, r, voff, soff, AUX); }
template <int N> DEV void vmwait() { asm volatile("s_waitcnt vmcnt(%0)" :: "n"(N) : "memory"); }
DEV unsigned epoch(const void* ws) {
  asm volatile("" : "+s"(ws));
  return *(const __attribute__((address_space(4))) unsigned*)ws + KCHK;
}
DEV f32x2 pkmul(f32x2 a, f32x2 b) { return a * b; }
DEV f32x2 pkadd(f32x2 a, f32x2 b) { return a + b; }
DEV f32x2 pkfma(f32x2 a, f32x2 b, f32x2 c) { return __builtin_elementwise_fma(a, b, c); }
DEV float dot2(unsigned a, unsigned b, float c) {
  return __builtin_amdgcn_fdot2_f32_bf16(__builtin_bit_cast(bf16x2, a), __builtin_bit_cast(bf16x2, b), c, false);
}
template <int CTRL> DEV float dppadd(float x) {
  return cut(x + __builtin_bit_cast(float, __builtin_amdgcn_update_dpp(0, __builtin_bit_cast(int, x), CTRL, 0xf, 0xf, true)));
}

#define BAR() bar()
#define TSX(k)
#define RTX(k)
#define VALX(k, x)
#define TSC(k)
#define TSSAVE()
#define BARARGS
#define BARPASS
#define TSINIT()
#define TSFIN()

#define PRB0() unsigned long long prb0_, prbk_; asm volatile("s_memrealtime %0\n\ts_waitcnt lgkmcnt(0)" : "=s"(prb0_) :: "memory"); prbk_ = prb0_
#define PRB(k) do { if (PROBE == k) { SB(); asm volatile("s_memrealtime %0\n\ts_waitcnt lgkmcnt(0)" : "=s"(prbk_) :: "memory"); SB(); } } while (0)
#define PRBEND() do { if (b == PROBE_B && w == 0) { const unsigned lim_ = 6000u + 2u * (unsigned)(prbk_ - prb0_); for (int it_ = 0; it_ < 100000; ++it_) { unsigned long long t_; asm volatile("s_sleep 1\n\ts_memrealtime %0\n\ts_waitcnt lgkmcnt(0)" : "=s"(t_) :: "memory"); if ((unsigned)(t_ - prb0_) >= lim_) break; } } } while (0)

struct Prod {
  u32x4 W[18];
  u32x4 ring[4][6];
};
struct ProdC {
  lds_char* smem;
  unsigned xw[2], rw, br[2], lw[2], lr[2], vd[2], voH, vw, lds0, ldsv;
  u32x2 I4;
  bool rvalid, odd;
  rsrc_t rhi, rlo, rwv;
};
DEV void lat_read(u32x4 (&B)[6], const ProdC& C, int t, int k0, int k1) {
#pragma unroll
  for (int ks = k0; ks < k1; ++ks) B[ks] = *(lds_u32x4*)(C.smem + C.br[ks & 1] + (t & 3) * 6144 + (ks >> 1) * 128);
}
DEV void lat_mm(const Prod& P, const u32x4 (&B)[6], const ProdC& C, int t) {
  const f32x4 z4 = {0.f, 0.f, 0.f, 0.f};
  f32x4 T[3];
#pragma unroll
  for (int ks = 0; ks < 6; ++ks)
#pragma unroll
    for (int tt = 0; tt < 3; ++tt) {
      T[tt] = mfma32(P.W[tt * 6 + ks], B[ks], ks == 0 ? z4 : T[tt]);
      OPQ(T[tt]);
      SB();
    }
#pragma unroll
  for (int tt = 0; tt < 3; ++tt) *(lds_f32x4*)(C.smem + C.lw[tt & 1] + (t & 1) * 12288 + (tt >> 1) * 128) = T[tt];
}
template <int M> DEV void ld_hi1(u32x4& d, const ProdC& C, unsigned so0, unsigned so1) { ldnt<(M >> 1) * 128>(d, C.rhi, C.voH, (M & 1) ? so1 : so0); }
DEV void ld_hi(u32x4 (&dst)[6], const ProdC& C, unsigned so) {
  const unsigned so1 = so + 196608;
  ld_hi1<0>(dst[0], C, so, so1); ld_hi1<1>(dst[1], C, so, so1); ld_hi1<2>(dst[2], C, so, so1);
  ld_hi1<3>(dst[3], C, so, so1); ld_hi1<4>(dst[4], C, so, so1); ld_hi1<5>(dst[5], C, so, so1);
}
template <int M, bool LOAD, bool LAT, int WAIT>
DEV void psub(Prod& P, const ProdC& C, int t, unsigned so, unsigned so1, f32x4 (&d)[6][2], f32x4 (&lat)[3][2], u32x4 (&B)[6],
              float (&ss)[2], float (&sm)[2]) {
  if constexpr (M < 6) {
    if (WAIT >= 0) vmwait<(WAIT >= 0 ? WAIT - M : 0)>();
    OPQ(P.ring[t & 3][M]);
    const u32x4 F = P.ring[t & 3][M];
    d[M][0] = mfma4(C.I4, u32x2{F[0], F[1]}, lat[M >> 1][0]);
    d[M][1] = mfma4(C.I4, u32x2{F[2], F[3]}, lat[M >> 1][1]);
    OPQ(d[M][0]); OPQ(d[M][1]);
    SB();
    if (LOAD) ld_hi1<M>(P.ring[t & 3][M], C, so, so1);
    if (M == 1) {
#pragma unroll
      for (int u = 0; u < 2; ++u) lat[2][u] = *(lds_f32x4*)(C.smem + C.lr[u] + (t & 1) * 12288 + 512);
    }
    if (LAT && M >= 3) lat_read(B, C, t + 1, 2 * (M - 3), 2 * (M - 3) + 2);
  }
  if constexpr (M > 0) {
    constexpr int k = M - 1, ab = k & 1, L = k >> 1;
    const u32x4 x = cvt8(d[k][0], d[k][1]);
    *(lds_u32x4*)(C.smem + C.xw[ab] + (t & 1) * 24576 + L * 8192) = x;
#pragma unroll
    for (int r = 0; r < 4; ++r) { ss[ab] = dot2(x[r], x[r], ss[ab]); sm[ab] = dot2(x[r], 0x3F803F80u, sm[ab]); }
  }
  SB();
}
template <bool LOAD, bool LAT, int NWV, int WAIT> DEV void pstep(Prod& P, const ProdC& C, int t, unsigned so, unsigned sl, unsigned wv BARARGS) {
  TSC(0);
  float ss[2] = {0.f, 0.f}, sm[2] = {0.f, 0.f};
  f32x4 d[6][2], lat[3][2];
  u32x4 B[6];
#pragma unroll
  for (int L = 0; L < 2; ++L)
#pragma unroll
    for (int u = 0; u < 2; ++u) lat[L][u] = *(lds_f32x4*)(C.smem + C.lr[u] + (t & 1) * 12288 + L * 256);
#pragma unroll
  for (int n = 0; n < NWV; ++n) dma16(C.rwv, C.vw, wv + n * 4096, C.ldsv + wv + n * 4096);
  if (LOAD) dma24(C.rlo, C.vd[0], C.vd[1], sl, C.lds0 + (t & 3) * 6144);
  const unsigned so1 = so + 196608;
  SB();
  TSC(1);
  psub<0, LOAD, LAT, WAIT>(P, C, t, so, so1, d, lat, B, ss, sm);
  psub<1, LOAD, LAT, WAIT>(P, C, t, so, so1, d, lat, B, ss, sm);
  TSC(2);
  psub<2, LOAD, LAT, WAIT>(P, C, t, so, so1, d, lat, B, ss, sm);
  psub<3, LOAD, LAT, WAIT>(P, C, t, so, so1, d, lat, B, ss, sm);
  TSC(3);
  psub<4, LOAD, LAT, WAIT>(P, C, t, so, so1, d, lat, B, ss, sm);
  psub<5, LOAD, LAT, WAIT>(P, C, t, so, so1, d, lat, B, ss, sm);
  psub<6, LOAD, LAT, WAIT>(P, C, t, so, so1, d, lat, B, ss, sm);
  TSC(4);
#pragma unroll
  for (int ab = 0; ab < 2; ++ab) {
    ss[ab] = dppadd<0x141>(dppadd<0x4E>(dppadd<0xB1>(ss[ab])));
    sm[ab] = dppadd<0x141>(dppadd<0x4E>(dppadd<0xB1>(sm[ab])));
  }
  const float mu = cut((C.odd ? sm[1] : sm[0]) * (1.0f / 192.0f));
  const float var = __builtin_fmaxf((C.odd ? ss[1] : ss[0]) * (1.0f / 192.0f) - mu * mu, 0.0f);
  const float rs = __builtin_amdgcn_rsqf(var + 1e-5f);
  if (C.rvalid) *(lds_f32*)(C.smem + C.rw + (t & 1) * 256) = rs;
  SB();
  TSC(5);
  if (LAT) lat_mm(P, B, C, t + 1);
  TSC(6);
}

struct Cons {
  f32x16 U[6];
  u32x4 Ab[12];
  float mrun, lsum;
};
struct ConsC { lds_char* smem; unsigned xr[4], xt[2][2], ra; };
DEV u32x4 row_read(const ConsC& C, int s) { return *(lds_u32x4*)(C.smem + C.xr[s & 3] + (s >> 2) * 8192); }
DEV u32x4 col_read(const ConsC& C, int o) {
  const int ks = o / 6, ct = o % 6;
  const u32x2 a0 = trread(C.smem + C.xt[ct & 1][0] + (ct >> 1) * 8192 + ks * 2048);
  const u32x2 a1 = trread(C.smem + C.xt[ct & 1][1] + (ct >> 1) * 8192 + ks * 2048);
  return u32x4{a0[0], a0[1], a1[0], a1[1]};
}
DEV void cstep(Cons& K, ConsC& C, int dx, int dr, bool first BARARGS) {
  TSC(0);
  u32x4 X[12];
  f32x4 rr[4];
#pragma unroll
  for (int s = 0; s < 12; ++s) X[s] = row_read(C, s);
#pragma unroll
  for (int a = 0; a < 4; ++a) rr[a] = *(lds_f32x4*)(C.smem + C.ra + a * 32);
  SB();
  f32x16 S = {0.f, 0.f, 0.f, 0.f, 0.f, 0.f, 0.f, 0.f, 0.f, 0.f, 0.f, 0.f, 0.f, 0.f, 0.f, 0.f};
#pragma unroll
  for (int s = 0; s < 12; ++s) {
    S = mfma3232(X[s], K.Ab[s], S);
    OPQ(S);
    SB();
    if (s == 0) TSC(1);
    if (s == 11) TSC(2);
    X[s] = col_read(C, s);
    SB();
  }
#pragma unroll
  for (int s = 0; s < 4; ++s) { C.xr[s] += dx; C.xt[s >> 1][s & 1] += dx; OPQ(C.xr[s]); OPQ(C.xt[s >> 1][s & 1]); }
  C.ra += dr; OPQ(C.ra);
  SB();
  float V[16];
  float vmax = -3e38f;
#pragma unroll
  for (int e = 0; e < 16; ++e) { V[e] = cut(S[e] * rr[e >> 2][e & 3] - K.mrun); vmax = __builtin_fmaxf(vmax, V[e]); }
  if (first || __builtin_amdgcn_ballot_w64(!(vmax <= THR)) != 0) {
    auto t = __builtin_amdgcn_permlane32_swap(f2u(vmax), f2u(vmax), false, false);
    float dl = __builtin_fmaxf(u2f(t[0]), u2f(t[1]));
    if (!first) dl = __builtin_fmaxf(dl, 0.f);
    const float al = __builtin_amdgcn_exp2f(__builtin_fminf(-dl, 0.f));
    K.mrun += dl; K.lsum *= al;
#pragma unroll
    for (int c = 0; c < 6; ++c) K.U[c] *= al;
#pragma unroll
    for (int e = 0; e < 16; ++e) V[e] -= dl;
  }
  TSC(3);
  u32x4 Pb[2];
  float pe[16];
#pragma unroll
  for (int e = 0; e < 8; ++e) pe[e] = __builtin_amdgcn_exp2f(V[e]);
#pragma unroll
  for (int e = 0; e < 8; e += 2) Pb[0][e >> 1] = cvtpk(cut(pe[e] * rr[e >> 2][e & 3]), cut(pe[e + 1] * rr[e >> 2][(e & 3) + 1]));
  SB();
  TSC(4);
#pragma unroll
  for (int ct = 0; ct < 6; ++ct) {
    K.U[ct] = mfma3232(X[ct], Pb[0], K.U[ct]);
    OPQ(K.U[ct]);
    SB();
    if (ct < 4) {
      const int e = 8 + 2 * ct;
      pe[e] = __builtin_amdgcn_exp2f(V[e]); pe[e + 1] = __builtin_amdgcn_exp2f(V[e + 1]);
      Pb[1][ct] = cvtpk(cut(pe[e] * rr[e >> 2][e & 3]), cut(pe[e + 1] * rr[e >> 2][(e & 3) + 1]));
    } else {
      const int e = 4 * (ct - 4);
      K.lsum += cut(pe[e] + pe[e + 1]) + cut(pe[e + 2] + pe[e + 3]);
    }
    SB();
  }
  TSC(5);
#pragma unroll
  for (int ct = 0; ct < 6; ++ct) {
    K.U[ct] = mfma3232(X[6 + ct], Pb[1], K.U[ct]);
    OPQ(K.U[ct]);
    SB();
    if (ct < 2) { const int e = 8 + 4 * ct; K.lsum += cut(pe[e] + pe[e + 1]) + cut(pe[e + 2] + pe[e + 3]); }
    SB();
  }
  TSC(6);
}
DEV void cfinish(Cons& K, lds_char* smem, int h, int b, rsrc_t rws, unsigned ek BARARGS) {
  const int lane = laneid(), i = lane & 15, g = lane >> 4;
  const int xo = (h >> 1) ? LDS_LAT : LDS_X, PH = h & 1;
  f32x4 Uo[12];
  lds_char* ex = smem + xo + PH * 12288 + lane * 16;
#pragma unroll
  for (int ct = 0; ct < 6; ++ct)
#pragma unroll
    for (int a = 0; a < 2; ++a) {
      f32x4 k0, k1;
#pragma unroll
      for (int r = 0; r < 4; ++r) {
        auto sw = __builtin_amdgcn_permlane16_swap(f2u(K.U[ct][8 * a + r]), f2u(K.U[ct][8 * a + 4 + r]), false, false);
        k0[r] = u2f(sw[0]); k1[r] = u2f(sw[1]);
      }
      Uo[2 * ct + a] = k0;
      *(lds_f32x4*)(ex + (2 * ct + a) * 1024) = k1;
      SB();
    }
  TSX(51);
  float mo, lo_;
  {
    auto t = __builtin_amdgcn_permlane32_swap(f2u(K.lsum), f2u(K.lsum), false, false);
    const float l = u2f(t[0]) + u2f(t[1]);
    auto sl = __builtin_amdgcn_permlane16_swap(f2u(l), f2u(l), false, false);
    auto sm = __builtin_amdgcn_permlane16_swap(f2u(K.mrun), f2u(K.mrun), false, false);
    mo = u2f(sm[0]); lo_ = u2f(sl[0]);
    *(__attribute__((address_space(3))) f32x2*)(smem + LDS_ML + h * 512 + lane * 8) = f32x2{u2f(sm[1]), u2f(sl[1])};
  }
  BAR();
  const f32x2 ml = *(__attribute__((address_space(3))) f32x2*)(smem + LDS_ML + (h ^ 1) * 512 + lane * 8);
  lds_char* ey = smem + xo + (1 - PH) * 12288 + lane * 16;
  f32x4 o[12];
#pragma unroll
  for (int c = 0; c < 12; ++c) o[c] = *(lds_f32x4*)(ey + c * 1024);
  lds_char* wv = smem + LDS_WV + h * 18432 + (8 * (g & 1) + 4 * (g >> 1) + (i >> 2)) * 96 + (i & 3) * 8;
  u32x4 Wv[18];
#pragma unroll
  for (int n = 0; n < 18; ++n) {
    const u32x2 a0 = trread(wv + (n / 3) * 3072 + (n % 3) * 32), a1 = trread(wv + (n / 3) * 3072 + 1536 + (n % 3) * 32);
    Wv[n] = u32x4{a0[0], a0[1], a1[0], a1[1]};
  }
  SB();
  const float mn = __builtin_fmaxf(mo, ml[0]);
  const float fa = __builtin_amdgcn_exp2f(mo - mn), fb = __builtin_amdgcn_exp2f(ml[0] - mn);
  const float l = lo_ * fa + ml[1] * fb, il = __builtin_amdgcn_rcpf(l);
  const unsigned lse = (unsigned)__builtin_amdgcn_ds_bpermute((4 * g + (i & 3)) * 4, (int)f2u(mn + __builtin_amdgcn_logf(l)));
  const f32x2 fa2 = {fa * il, fa * il}, fb2 = {fb * il, fb * il};
  f32x2 u2[24], us[2];
#pragma unroll
  for (int c = 0; c < 24; ++c) {
    const f32x2 own = {Uo[c >> 1][(c & 1) * 2], Uo[c >> 1][(c & 1) * 2 + 1]}, oth = {o[c >> 1][(c & 1) * 2], o[c >> 1][(c & 1) * 2 + 1]};
    u2[c] = pkfma(oth, fb2, pkmul(own, fa2));
    us[c & 1] = c < 2 ? u2[c] : pkadd(us[c & 1], u2[c]);
  }
  TSX(52);
  const float um = xg_sum((us[0][0] + us[0][1]) + (us[1][0] + us[1][1])) * (-1.0f / 192.0f);
  const f32x2 um2 = {um, um};
  const f32x4 z4 = {0.f, 0.f, 0.f, 0.f};
  f32x4 cx[3] = {z4, z4, z4};
#pragma unroll
  for (int s = 0; s < 6; ++s) {
    u32x4 ub;
#pragma unroll
    for (int r = 0; r < 4; ++r) { const f32x2 v = pkadd(u2[4 * s + r], um2); ub[r] = cvtpk(v[0], v[1]); }
#pragma unroll
    for (int dt = 0; dt < 3; ++dt) cx[dt] = mfma32(ub, Wv[3 * s + dt], cx[dt]);
  }
  TSX(53);
  const u32x4 ra = cvt8(cx[0], cx[1]);
  u32x4 rb = {cvtpk(cx[2][0], cx[2][1]), cvtpk(cx[2][2], cx[2][3]), lse, 0u};
  rb[3] = (ra[0] + ra[1] + ra[2]) + (ra[3] + rb[0] + rb[1]) + lse + ek;
  const unsigned vo = (lane >> 2) * 32768 + (lane & 3) * 16, so = WS_REC + h * 524288 + b * 64;
  bst4<SC1>(ra, rws, vo, so);
  bst4<SC1>(rb, rws, vo, so + 16384);
}

struct AIn { u32x4 q[6]; };
DEV void a_issue(AIn& I, unsigned lbase, int h, int cs, const void* q_, const void* wq_, const void* wk_, const void* wm_) {
  const int lane = laneid();
  const rsrc_t rq = mkrsrc(q_, 6144u), rwq = mkrsrc(wq_, 73728u), rwk = mkrsrc(wk_, 73728u), rwm = mkrsrc(wm_, 73728u);
  const unsigned vl = lane * 16;
#pragma unroll
  for (int n = 0; n < 18; ++n) dma16(rwq, vl, h * 18432 + n * 1024, lbase + LDS_AWQ + n * 1024);
  unsigned vo[3];
#pragma unroll
  for (int m = 0; m < 3; ++m) { const unsigned o = m * 1024 + vl; vo[m] = (o / 96) * 384 + o % 96; }
#pragma unroll
  for (int n = 0; n < 18; ++n) dma16(rwm, vo[n % 3], (n / 3) * 12288 + cs * 96, lbase + LDS_AWM + n * 1024);
#pragma unroll
  for (int n = 0; n < 5; ++n) dma16(rwk, vl, h * 18432 + cs * 4608 + n * 1024, lbase + LDS_AWK + n * 1024);
  const unsigned vq = (lane & 15) * 384 + (lane >> 4) * 16;
  ldpl<0>(I.q[0], rq, vq); ldpl<64>(I.q[1], rq, vq); ldpl<128>(I.q[2], rq, vq);
  ldpl<192>(I.q[3], rq, vq); ldpl<256>(I.q[4], rq, vq); ldpl<320>(I.q[5], rq, vq);
}
DEV void a_compute(AIn& I, lds_char* smem, int h, int cs, rsrc_t rws, unsigned ek) {
  const int lane = laneid(), i = lane & 15, g = lane >> 4;
  vmwait<0>();
#pragma unroll
  for (int s = 0; s < 6; ++s) OPQ(I.q[s]);
  SB();
  lds_char* a = smem + (8 * g + (i >> 2)) * 96 + (i & 3) * 8;
  const f32x4 z4 = {0.f, 0.f, 0.f, 0.f};
  f32x4 Q[3], M[3], T[3];
  u32x4 W[18], V[18];
  u32x2 Kf[9];
#pragma unroll
  for (int n = 0; n < 18; ++n) {
    const u32x2 a0 = trread(a + LDS_AWQ + (n / 3) * 3072 + (n % 3) * 32), a1 = trread(a + LDS_AWQ + (n / 3) * 3072 + 384 + (n % 3) * 32);
    W[n] = u32x4{a0[0], a0[1], a1[0], a1[1]};
  }
#pragma unroll
  for (int n = 0; n < 18; ++n) {
    const u32x2 a0 = trread(a + LDS_AWM + (n / 3) * 3072 + (n % 3) * 32), a1 = trread(a + LDS_AWM + (n / 3) * 3072 + 384 + (n % 3) * 32);
    V[n] = u32x4{a0[0], a0[1], a1[0], a1[1]};
  }
#pragma unroll
  for (int n = 0; n < 9; ++n) Kf[n] = *(__attribute__((address_space(3))) u32x2*)(smem + LDS_AWK + i * 96 + g * 8 + (n / 3) * 1536 + (n % 3) * 32);
  SB();
#pragma unroll
  for (int n = 0; n < 18; ++n) Q[n % 3] = mfma32(W[n], I.q[n / 3], n < 3 ? z4 : Q[n % 3]);
#pragma unroll
  for (int n = 0; n < 18; ++n) M[n % 3] = mfma32(V[n], I.q[n / 3], n < 3 ? z4 : M[n % 3]);
#pragma unroll
  for (int dt = 0; dt < 3; ++dt) {
    const u32x2 qb = {cvtpk(Q[dt][0], Q[dt][1]), cvtpk(Q[dt][2], Q[dt][3])};
#pragma unroll
    for (int ct = 0; ct < 3; ++ct) T[ct] = mfma16(Kf[3 * ct + dt], qb, dt == 0 ? z4 : T[ct]);
  }
  const float s1 = 1.4426950408889634f * 0.14433756729740643f;
  unsigned P[6];
#pragma unroll
  for (int n = 0; n < 6; ++n)
    P[n] = cvtpk((T[n >> 1][(n & 1) * 2] + 0.5f * M[n >> 1][(n & 1) * 2]) * s1, (T[n >> 1][(n & 1) * 2 + 1] + 0.5f * M[n >> 1][(n & 1) * 2 + 1]) * s1);
  const unsigned so = WS_A + h * 8192 + cs * 2048;
  bst4<SC1>(u32x4{P[0], P[1], P[2], P[0] + P[1] + P[2] + ek}, rws, lane * 16, so);
  bst4<SC1>(u32x4{P[4], P[5], P[3], P[4] + P[5] + P[3] + ek}, rws, lane * 16 + 1024, so);
}
DEV void a_poll(u32x4 (&G)[8], int h, rsrc_t rws, unsigned ek BARARGS) {
  const int lane = laneid();
  RTX(44);
  for (int spin = 0;; ++spin) {
    VALX(46, spin);
    asm volatile("" ::: "memory");
#pragma unroll
    for (int n = 0; n < 8; ++n) G[n] = bld4<SC1>(rws, lane * 16 + (n & 3) * 1024, WS_A + h * 8192 + (n >> 2) * 4096);
    bool bad = false;
#pragma unroll
    for (int n = 0; n < 8; ++n) bad |= G[n][0] + G[n][1] + G[n][2] + ek != G[n][3];
    if (__builtin_amdgcn_ballot_w64(bad) == 0 || spin >= SPIN_MAX) break;
  }
  RTX(45);
}
DEV void a_centre(const u32x4 (&G)[8], lds_char* smem, int h) {
  const int lane = laneid(), i = lane & 15, g = lane >> 4;
  float sm[2] = {0.f, 0.f};
#pragma unroll
  for (int n = 0; n < 24; ++n) sm[n & 1] = dot2(G[n / 3][n % 3], 0x3F803F80u, sm[n & 1]);
  const float mu = xg_sum(sm[0] + sm[1]) * (-1.0f / 192.0f);
  const f32x4 mu4 = {mu, mu, mu, mu};
  const int d = lane & 3;
  const u32x2 I4 = {d == 0 ? 0x00003F80u : (d == 1 ? 0x3F800000u : 0u), d == 2 ? 0x00003F80u : (d == 3 ? 0x3F800000u : 0u)};
  lds_char* dst = smem + LDS_AF + (h >> 1) * 12288 + (g >> 1) * 512 + ((h & 1) * 16 + i) * 16 + (g & 1) * 8;
#pragma unroll
  for (int s = 0; s < 12; ++s) {
    const int cs = s / 3, ct = s % 3;
    const u32x2 v = ct == 0 ? u32x2{G[2 * cs][0], G[2 * cs][1]} : (ct == 1 ? u32x2{G[2 * cs][2], G[2 * cs + 1][2]} : u32x2{G[2 * cs + 1][0], G[2 * cs + 1][1]});
    const f32x4 c = mfma4(I4, v, mu4);
    *(__attribute__((address_space(3))) u32x2*)(dst + s * 1024) = u32x2{cvtpk(c[0], c[1]), cvtpk(c[2], c[3])};
  }
}
template <int CTRL> DEV float dppmov(float x) { return __builtin_bit_cast(float, __builtin_amdgcn_update_dpp(0, __builtin_bit_cast(int, x), CTRL, 0xf, 0xf, true)); }
DEV float quads_sum(float x) { x += dppmov<0x124>(x); return x + dppmov<0x128>(x); }
DEV float quads_max(float x) { x = __builtin_fmaxf(x, dppmov<0x124>(x)); return __builtin_fmaxf(x, dppmov<0x128>(x)); }
DEV void reduce1(lds_char* smem, int r, int p, rsrc_t rws, unsigned ek BARARGS) {
  const int lane = laneid(), i = lane & 15, g = lane >> 4, d = lane & 3;
  const unsigned m0 = d == 0 ? 0x0000FFFFu : (d == 1 ? 0xFFFF0000u : 0u), m1 = d == 2 ? 0x0000FFFFu : (d == 3 ? 0xFFFF0000u : 0u);
  u32x4 RA[4], RB[4];
  const unsigned so = WS_REC + r * 32768 + p * 4096;
  RTX(44);
  for (int spin = 0;; ++spin) {
    VALX(46, spin);
    asm volatile("" ::: "memory");
#pragma unroll
    for (int j = 0; j < 4; ++j) { RB[j] = bld4<SC1>(rws, lane * 16 + j * 1024, so + 16384); RA[j] = bld4<SC1>(rws, lane * 16 + j * 1024, so); }
    bool bad = false;
#pragma unroll
    for (int j = 0; j < 4; ++j) bad |= (RA[j][0] + RA[j][1] + RA[j][2]) + (RA[j][3] + RB[j][0] + RB[j][1]) + RB[j][2] + ek != RB[j][3];
    if (__builtin_amdgcn_ballot_w64(bad) == 0 || spin >= SPIN_MAX) break;
  }
  RTX(45);
  const float M = xg_max(quads_max(__builtin_fmaxf(__builtin_fmaxf(u2f(RB[0][2]), u2f(RB[1][2])), __builtin_fmaxf(u2f(RB[2][2]), u2f(RB[3][2])))));
  const f32x4 z4 = {0.f, 0.f, 0.f, 0.f};
  f32x4 acc[3] = {z4, z4, z4};
  float L = 0.f;
#pragma unroll
  for (int j = 0; j < 4; ++j) {
    const float wf = __builtin_amdgcn_exp2f(u2f(RB[j][2]) - M);
    const unsigned wb = cvtpk(wf, wf);
    L += u2f(wb & 0xFFFF0000u);
    const u32x2 dg = {wb & m0, wb & m1};
    acc[0] = mfma4(dg, u32x2{RA[j][0], RA[j][1]}, acc[0]);
    acc[1] = mfma4(dg, u32x2{RA[j][2], RA[j][3]}, acc[1]);
    acc[2] = mfma4(dg, u32x2{RB[j][0], RB[j][1]}, acc[2]);
  }
  L = xg_sum(quads_sum(L));
  float u[6];
  f32x4 v;
#pragma unroll
  for (int k = 0; k < 6; ++k) {
    auto t = __builtin_amdgcn_permlane32_swap(f2u(acc[k % 3][k / 3]), f2u(acc[k % 3][2 + k / 3]), false, false);
    u[k] = u2f(t[0]) + u2f(t[1]);
  }
#pragma unroll
  for (int k = 0; k < 3; ++k) {
    auto t = __builtin_amdgcn_permlane16_swap(f2u(u[k]), f2u(u[k + 3]), false, false);
    v[k] = quads_sum(u2f(t[0]) + u2f(t[1]));
  }
  v[3] = 0.f;
  if (i < 4) *(lds_f32x4*)(smem + LDS_RED + p * 256 + g * 64 + d * 16) = v;
  if (lane < 4) *(__attribute__((address_space(3))) f32x2*)(smem + LDS_RED + 1024 + p * 32 + d * 8) = f32x2{M, L};
  bar();
  if (p == 0) {
    const f32x4 x = *(lds_f32x4*)(smem + LDS_RED + (i >> 2) * 256 + g * 64 + d * 16);
    const f32x2 ml = *(__attribute__((address_space(3))) f32x2*)(smem + LDS_RED + 1024 + (i >> 2) * 32 + g * 8);
    const float f = __builtin_amdgcn_exp2f(ml[0] - quads_max(ml[0]));
    const float il = __builtin_amdgcn_rcpf(quads_sum(ml[1] * f));
    u32x4 o;
#pragma unroll
    for (int k = 0; k < 3; ++k) { const float c = quads_sum(x[k] * f) * il; o[k] = cvtpk(c, dppmov<0xB1>(c)); }
    o[3] = o[0] + o[1] + o[2] + ek;
    if (i == 0 || i == 2) bst4<SC1>(o, rws, (d >> 1) * 64 + g * 16, WS_CTX + r * 128);
  }
}
DEV void reduce2_issue(u32x2& qr, unsigned lbase, int nt, const void* q_, const void* wo_) {
  const int lane = laneid();
  const rsrc_t rq = mkrsrc(q_, 6144u), rwo = mkrsrc(wo_, 73728u);
  const unsigned vo = (lane >> 1) * 384 + (lane & 1) * 16, vq = (lane & 15) * 384 + (lane >> 4) * 8;
#pragma unroll
  for (int n = 0; n < 6; ++n) dma16(rwo, vo, n * 12288 + nt * 32, lbase + LDS_WO + n * 1024);
  asm volatile("buffer_load_dwordx2 %0, %1, %2, %3 offen" : "=v"(qr) : "v"(vq), "s"(rq), "s"(nt * 32) : "memory");
}
DEV void reduce2(u32x2& qr, lds_char* smem, int nt, rsrc_t rws, unsigned ek, void* out_ BARARGS) {
  const int lane = laneid(), i = lane & 15, g = lane >> 4, rho = i >> 2;
  vmwait<0>();
  OPQ(qr);
  SB();
  lds_char* a0 = smem + LDS_WO + (16 * (rho >> 1) + 4 * g + (rho & 1)) * 32 + (i & 3) * 8;
  lds_char* a1 = smem + LDS_WO + (32 + 4 * g + (rho & 1)) * 32 + (i & 3) * 8;
  u32x4 W[8], G[8];
#pragma unroll
  for (int m = 0; m < 8; ++m) {
    const u32x2 x0 = trread(a0 + ((m >> 1) * 48 + 2 * (m & 1)) * 32), x1 = trread(a1 + ((m >> 1) * 48 + 2 * (m & 1)) * 32);
    W[m] = u32x4{x0[0], x0[1], x1[0], x1[1]};
    OPQ(W[m]);
  }
  const unsigned vo = (i >> 2) * 512 + g * 128 + (i & 3) * 16;
  RTX(44);
  for (int spin = 0;; ++spin) {
    VALX(46, spin);
    asm volatile("" ::: "memory");
#pragma unroll
    for (int m = 0; m < 8; ++m) G[m] = bld4<SC1>(rws, vo + (m & 1) * 64 + ((m >> 1) & 1) * 2048, WS_CTX + (m >> 2) * 4096);
    bool bad = false;
#pragma unroll
    for (int m = 0; m < 8; ++m) bad |= G[m][0] + G[m][1] + G[m][2] + ek != G[m][3];
    if (__builtin_amdgcn_ballot_w64(bad) == 0 || spin >= SPIN_MAX) break;
  }
  RTX(45);
  f32x4 c[2] = {{u2f(qr[0] << 16), u2f(qr[0] & 0xFFFF0000u), u2f(qr[1] << 16), u2f(qr[1] & 0xFFFF0000u)}, {0.f, 0.f, 0.f, 0.f}};
#pragma unroll
  for (int m = 0; m < 8; ++m) { G[m][3] = 0u; c[m & 1] = mfma32(W[m], G[m], c[m & 1]); }
  const f32x4 o = c[0] + c[1];
  __builtin_amdgcn_raw_buffer_store_b64(u32x2{cvtpk(o[0], o[1]), cvtpk(o[2], o[3])}, mkrsrc(out_, 6144u), i * 384 + g * 8, nt * 32, 0);
  if (nt == 0 && lane == 0) __builtin_amdgcn_raw_buffer_store_b32(ek - KCHK + 1u, rws, 0, 0, 0);
}

__global__ void __launch_bounds__(512, 2)
m2f_main(const void* __restrict__ fhi_, const void* __restrict__ flo_, const void* __restrict__ wlat_,
         const void* __restrict__ wv_, const void* __restrict__ q_, const void* __restrict__ wq_, const void* __restrict__ wk_,
         const void* __restrict__ wm_, const void* __restrict__ wo_, void* __restrict__ ws_, void* __restrict__ out_) {
  __shared__ __attribute__((aligned(16))) char smem_[LDS_TOTAL];
  lds_char* smem = (lds_char*)smem_;
  const int w = __builtin_amdgcn_readfirstlane(__builtin_amdgcn_workitem_id_x() >> 6);
  const int b = __builtin_amdgcn_workgroup_id_x();
  TSINIT();
  PRB0();

  if (w < 4) {
    const int p = w, lane = laneid(), i = lane & 15, g = lane >> 4;
#define PRIO_P 2
    __builtin_amdgcn_s_setprio(PRIO_P);
    if (b < 16) bar();
    const rsrc_t rwl = mkrsrc(wlat_, 73728u);
    ProdC C;
    C.smem = smem;
    C.rhi = mkrsrc(fhi_, 100663296u);
    C.rlo = mkrsrc(flo_, 25165824u);
    C.rwv = mkrsrc(wv_, 73728u);
    const unsigned j = lane >> 3, c7 = lane & 7;
    const unsigned lbase = (unsigned)(uintptr_t)smem;
    C.voH = j * 384 + c7 * 16;
    C.vw = lane * 16;
    C.lds0 = lbase + LDS_LO + p * 1536;
    C.ldsv = lbase + LDS_WV;
#pragma unroll
    for (int k = 0; k < 2; ++k) {
      const unsigned o = k * 1024 + (lane & (k ? 31 : 63)) * 16, px = o / 384, pc = (o % 384) / 16;
      C.vd[k] = px * 384 + ((pc ^ (2 * p + (px >> 1))) * 16);
    }
    const unsigned soL = (unsigned)(b * 256 + p * 4) * 384u;
    const unsigned soH = (unsigned)(b * 1024 + p * 8) * 384u;
    Prod P;
    dma24(C.rlo, C.vd[0], C.vd[1], soL, C.lds0);
    dma24(C.rlo, C.vd[0], C.vd[1], soL + 6144, C.lds0 + 6144);
    SB();
    {
      unsigned vo[6];
#pragma unroll
      for (int m = 0; m < 6; ++m) {
        const unsigned o = m * 1024 + lane * 16, k = o / 384, pc = (o % 384) / 16;
        vo[m] = k * 384 + ((pc ^ (2 * (((k >> 1) & 1) | ((k >> 3) << 1)))) * 16);
      }
      const unsigned so1 = soH + 196608, wo = p * 18432, wl = lbase + LDS_WL + p * 18432;
#define WGRP(M) ld_hi1<M>(P.ring[0][M], C, soH, so1); \
      for (int n = 3 * M; n < 3 * M + 3; ++n) dma16(rwl, vo[n % 6], wo + (n / 6) * 6144, wl + n * 1024);
      WGRP(0) WGRP(1) WGRP(2) WGRP(3) WGRP(4) WGRP(5)
#undef WGRP
    }
    SB();
    ld_hi(P.ring[1], C, soH + 12288);
    dma24(C.rlo, C.vd[0], C.vd[1], soL + 2 * 6144, C.lds0 + 2 * 6144);
    SB();
    {
      const int d = lane & 3;
      C.I4[0] = d == 0 ? 0x00003F80u : (d == 1 ? 0x3F800000u : 0u);
      C.I4[1] = d == 2 ? 0x00003F80u : (d == 3 ? 0x3F800000u : 0u);
#pragma unroll
      for (int ab = 0; ab < 2; ++ab) {
        C.xw[ab] = LDS_X + (16 * p + 8 * ab + j) * 128 + ((c7 ^ (((j >> 1) & 1) * 4 + 2 * ab + (j >> 2))) * 16);
        C.br[ab] = LDS_LO + i * 384 + (((4 * ab + g) ^ (i >> 1)) * 16);
        C.lw[ab] = LDS_LAT + i * 768 + (((12 * p + 4 * ab + g) ^ (i & 7)) * 16);
        C.lr[ab] = LDS_LAT + (4 * p + (j >> 1)) * 768 + (((2 * c7 + ab) ^ ((4 * p + (j >> 1)) & 7)) * 16);
      }
      C.rvalid = c7 < 2; C.odd = lane & 1;
      C.rw = LDS_R + (16 * p + 8 * (lane & 1) + j) * 4;
#pragma unroll
      for (int ab = 0; ab < 2; ++ab) { OPQ(C.xw[ab]); OPQ(C.br[ab]); OPQ(C.lw[ab]); OPQ(C.lr[ab]); OPQ(C.vd[ab]); }
      OPQ(C.rw); OPQ(C.vw);
    }
    SB();
    unsigned ek = epoch(ws_);
    SB();
    vmwait<8>();
    asm volatile("" : "+s"(ek));
    BAR();
    PRB(1);
    ld_hi(P.ring[2], C, soH + 2 * 12288);
    SB();
    {
      const unsigned rp = i >> 2, cp = i & 3, s = (rp >> 1) | ((g & 1) << 1);
      u32x4 B[6];
      lat_read(B, C, 0, 0, 6);
#pragma unroll
      for (int tt = 0; tt < 3; ++tt) {
        const unsigned ct = 3 * p + tt;
        lds_char* a = smem + LDS_WL + (8 * g + rp) * 384 + (((ct & 3) ^ s) * 32) + cp * 8 + (ct >> 2) * 128;
#pragma unroll
        for (int ks = 0; ks < 6; ++ks) {
          const u32x2 a0 = trread(a + ks * 12288), a1 = trread(a + ks * 12288 + 1536);
          P.W[tt * 6 + ks] = u32x4{a0[0], a0[1], a1[0], a1[1]};
        }
      }
      SB();
      lat_mm(P, B, C, 0);
    }
    BAR();
    PRB(2);
    dma24(C.rlo, C.vd[0], C.vd[1], soL + 3 * 6144, C.lds0 + 3 * 6144);
    ld_hi(P.ring[3], C, soH + 3 * 12288);
    SB();
#pragma unroll 1
    for (int u = 0; u < 3; ++u) {
      const unsigned so = soH + (u + 1) * 49152, sl = soL + (u + 1) * 24576, wv = p * 1024 + u * 20480;
      pstep<true, true, 1, -1>(P, C, 0, so, sl, wv BARPASS); vmwait<22>(); BAR(); if (u == 0) PRB(3);
      pstep<true, true, 1, -1>(P, C, 1, so + 12288, sl + 6144, wv + 4096 BARPASS); vmwait<22>(); BAR(); if (u == 0) PRB(4);
      pstep<true, true, 1, -1>(P, C, 2, so + 2 * 12288, sl + 2 * 6144, wv + 2 * 4096 BARPASS); vmwait<22>(); BAR();
      pstep<true, true, 2, -1>(P, C, 3, so + 3 * 12288, sl + 3 * 6144, wv + 3 * 4096 BARPASS); vmwait<22>(); BAR();
    }
    pstep<false, true, 0, -1>(P, C, 0, 0, 0, 0 BARPASS); vmwait<14>(); BAR();
    pstep<false, true, 0, -1>(P, C, 1, 0, 0, 0 BARPASS); vmwait<6>(); TSSAVE(); BAR();
    pstep<false, true, 3, -1>(P, C, 2, 0, 0, p * 1024 + 61440 BARPASS); BAR();
    pstep<false, false, 0, 8>(P, C, 3, 0, 0, 0 BARPASS); BAR();
    PRB(6);
    const bool l2 = p == 0 && b >= L2_B0 && b < L2_B0 + 12;
    u32x2 qr;
    if (l2) { reduce2_issue(qr, lbase, b - L2_B0, q_, wo_); vmwait<7>(); } else vmwait<0>();
    BAR();
    PRB(7);
    if (b >= L1_B0 && b < L1_B0 + 64) reduce1(smem, b - L1_B0, p, mkrsrc(ws_, WS_BYTES), ek BARPASS);
    if (l2) reduce2(qr, smem, b - L2_B0, mkrsrc(ws_, WS_BYTES), ek, out_ BARPASS);
    PRB(9);
    PRBEND();
  } else {
    const int h = w - 4, hp = h >> 1, ph = h & 1, lane = laneid(), i = lane & 15, g = lane >> 4;
    const rsrc_t rws = mkrsrc(ws_, WS_BYTES);
    Cons K;
#define PRIO_C 0
    __builtin_amdgcn_s_setprio(PRIO_C);
    unsigned ek;
    if (b < 16 && h == 0) {
      AIn I;
      a_issue(I, (unsigned)(uintptr_t)smem, b >> 2, b & 3, q_, wq_, wk_, wm_);
      ek = epoch(ws_);
      asm volatile("s_barrier" ::: "memory");
      RTX(47);
      a_compute(I, smem, b >> 2, b & 3, rws, ek);
      RTX(48);
    } else {
      if (b < 16) bar();
      ek = epoch(ws_);
    }
    asm volatile("" : "+s"(ek));
    u32x4 G[8];
    BAR();
    a_poll(G, h, rws, ek BARPASS);
    BAR();
    a_centre(G, smem, h);
    ConsC C;
    C.smem = smem;
    const unsigned hl = lane >> 5;
    {
      const unsigned m = lane & 31, row = 32 * ph + m, f = ((m >> 1) & 1) * 4 + ((m >> 2) & 3);
#pragma unroll
      for (int s = 0; s < 4; ++s) C.xr[s] = LDS_X + row * 128 + (((2 * s + hl) ^ f) * 16);
      const unsigned cb = g & 1, rp = i >> 2, cp = i & 3;
#pragma unroll
      for (int c = 0; c < 2; ++c)
#pragma unroll
        for (int sec = 0; sec < 2; ++sec)
          C.xt[c][sec] = LDS_X + (32 * ph + 8 * sec + 4 * hl + rp) * 128 + (((4 * c + 2 * cb + (cp >> 1)) ^ ((rp >> 1) * 4 + hl + 2 * sec)) * 16) + (cp & 1) * 8;
      C.ra = LDS_R + (32 * ph + 4 * hl) * 4;
#pragma unroll
      for (int s = 0; s < 4; ++s) { OPQ(C.xr[s]); OPQ(C.xt[s >> 1][s & 1]); }
      OPQ(C.ra);
    }
#pragma unroll
    for (int c = 0; c < 6; ++c)
#pragma unroll
      for (int e = 0; e < 16; ++e) K.U[c][e] = 0.f;
    K.mrun = 0.f; K.lsum = 0.f;
#pragma unroll 1
    for (int t = 0; t < 16; ++t) {
      BAR();
      if (t == 0) {
        lds_char* af = smem + LDS_AF + hp * 12288 + (laneid() ^ (16 * ph)) * 16;
#pragma unroll
        for (int s = 0; s < 12; ++s) K.Ab[s] = *(lds_u32x4*)(af + s * 1024);
      }
      cstep(K, C, (t & 1) ? -24576 : 24576, (t & 1) ? -256 : 256, t == 0 BARPASS);
    }
    TSX(50);
    cfinish(K, smem, h, b, rws, ek BARPASS);
  }
  TSFIN();
}

extern "C" void kernel_launch(void* const* d_in, const int* in_sizes, int n_in, void* d_out, int out_size, void* d_ws,
                              size_t ws_size, hipStream_t stream) {
  m2f_main<<<256, 512, 0, stream>>>(d_in[1], d_in[2], d_in[3], d_in[6], d_in[0], d_in[4], d_in[5], d_in[8], d_in[7], d_ws, d_out);
}
```

```cpp
#include <hip/hip_runtime.h>
#include <stdint.h>

typedef __attribute__((ext_vector_type(8))) __bf16 bf16x8;
typedef __attribute__((ext_vector_type(2))) __bf16 bf16x2;
typedef __attribute__((ext_vector_type(4))) short s16x4;
typedef __attribute__((ext_vector_type(4))) float f32x4;
typedef __attribute__((ext_vector_type(2))) float f32x2;
typedef __attribute__((ext_vector_type(4))) unsigned u32x4;
typedef __attribute__((ext_vector_type(2))) unsigned u32x2;
typedef __attribute__((address_space(3))) char lds_char;
typedef __attribute__((address_space(3))) s16x4 lds_s16x4;
typedef __attribute__((address_space(3))) u32x4 lds_u32x4;
typedef __attribute__((address_space(3))) f32x4 lds_f32x4;
typedef __attribute__((address_space(3))) float lds_f32;

typedef __attribute__((ext_vector_type(16))) float f32x16;
#define DEV __device__ __forceinline__
#define SB() __builtin_amdgcn_sched_barrier(0)
#define OPQ(x) asm volatile("" : "+v"(x))

constexpr int LDS_X = 0;
constexpr int LDS_WL = 24576;
constexpr int LDS_WV = 49152;
constexpr int LDS_LO = 110592;
constexpr int LDS_LAT = 135168;
constexpr int LDS_R = 159744;
constexpr int LDS_ML = 160256;
constexpr int LDS_TOTAL = 162304;
constexpr int LDS_AF = 86016;
constexpr int LDS_AWQ = 0, LDS_AWM = LDS_LAT, LDS_AWK = LDS_LAT + 18432;
constexpr int LDS_WO = LDS_LO + 2 * 6144, LDS_RED = LDS_LO + 3 * 6144;
constexpr float THR = 16.0f;
constexpr unsigned WS_A = 4096, WS_REC = 65536, WS_CTX = WS_REC + 64 * 32768, WS_BYTES = WS_CTX + 8192;
constexpr unsigned KCHK = 0x9E3779B9u;
constexpr int SC1 = 16, SPIN_MAX = 1 << 15;
constexpr int L1_B0 = 64, L2_B0 = 128;
#define APOLL 0

DEV float cut(float x) { asm("" : "+v"(x)); return x; }
DEV unsigned cvtpk(float lo, float hi) {
  f32x2 v = {lo, hi};
  return __builtin_bit_cast(unsigned, __builtin_convertvector(v, bf16x2));
}
DEV u32x4 cvt8(f32x4 a, f32x4 b) { return u32x4{cvtpk(a[0], a[1]), cvtpk(a[2], a[3]), cvtpk(b[0], b[1]), cvtpk(b[2], b[3])}; }
DEV f32x4 mfma32(u32x4 a, u32x4 b, f32x4 c) {
  return __builtin_amdgcn_mfma_f32_16x16x32_bf16(__builtin_bit_cast(bf16x8, a), __builtin_bit_cast(bf16x8, b), c, 0, 0, 0);
}
DEV f32x16 mfma3232(u32x4 a, u32x4 b, f32x16 c) {
  return __builtin_amdgcn_mfma_f32_32x32x16_bf16(__builtin_bit_cast(bf16x8, a), __builtin_bit_cast(bf16x8, b), c, 0, 0, 0);
}
DEV unsigned f2u(float x) { return __builtin_bit_cast(unsigned, x); }
DEV float u2f(unsigned x) { return __builtin_bit_cast(float, x); }
DEV f32x4 mfma16(u32x2 a, u32x2 b, f32x4 c) {
  return __builtin_amdgcn_mfma_f32_16x16x16bf16_1k(__builtin_bit_cast(s16x4, a), __builtin_bit_cast(s16x4, b), c, 0, 0, 0);
}
DEV f32x4 mfma4(u32x2 a, u32x2 b, f32x4 c) {
  return __builtin_amdgcn_mfma_f32_4x4x4bf16_1k(__builtin_bit_cast(s16x4, a), __builtin_bit_cast(s16x4, b), c, 0, 0, 0);
}
DEV u32x2 trread(lds_char* p) {
  return __builtin_bit_cast(u32x2, __builtin_amdgcn_ds_read_tr16_b64_v4i16((lds_s16x4*)p));
}
template <int CTRL, int BANK> DEV unsigned dppu(unsigned old, unsigned src) {
  return (unsigned)__builtin_amdgcn_update_dpp((int)old, (int)src, CTRL, 0xf, BANK, false);
}
template <int CTRL, int BANK> DEV float dppf(float old, float src) {
  return __builtin_bit_cast(float, dppu<CTRL, BANK>(__builtin_bit_cast(unsigned, old), __builtin_bit_cast(unsigned, src)));
}
constexpr int DPP_ID = 0xE4, DPP_ROR8 = 0x128;
DEV float xg_sum(float x) {
  auto s = __builtin_amdgcn_permlane16_swap(f2u(x), f2u(x), false, false);
  x = u2f(s[0]) + u2f(s[1]);
  auto t = __builtin_amdgcn_permlane32_swap(f2u(x), f2u(x), false, false);
  return u2f(t[0]) + u2f(t[1]);
}
DEV float xg_max(float x) {
  auto s = __builtin_amdgcn_permlane16_swap(f2u(x), f2u(x), false, false);
  x = __builtin_fmaxf(u2f(s[0]), u2f(s[1]));
  auto t = __builtin_amdgcn_permlane32_swap(f2u(x), f2u(x), false, false);
  return __builtin_fmaxf(u2f(t[0]), u2f(t[1]));
}
DEV int laneid() {
  int l;
  asm volatile("v_mbcnt_lo_u32_b32 %0, -1, 0\n\tv_mbcnt_hi_u32_b32 %0, -1, %0" : "=v"(l));
  __builtin_assume(l >= 0 && l < 64);
  return l;
}
DEV void bar() { SB(); __builtin_amdgcn_s_waitcnt(0xC07F); asm volatile("s_barrier" ::: "memory"); SB(); }
typedef __amdgpu_buffer_rsrc_t rsrc_t;
DEV rsrc_t mkrsrc(const void* p, unsigned bytes) { return __builtin_amdgcn_make_buffer_rsrc((void*)p, (short)0, (int)bytes, 0x00020000); }
template <int AUX> DEV u32x4 bld4(rsrc_t r, unsigned voff, unsigned soff) { return __builtin_amdgcn_raw_buffer_load_b128(r, voff, soff, AUX); }
template <int AUX> DEV u32x2 bld2(rsrc_t r, unsigned voff, unsigned soff) { return __builtin_amdgcn_raw_buffer_load_b64(r, voff, soff, AUX); }
constexpr int NT = 2;

DEV void dma16(rsrc_t r, unsigned voff, unsigned soff, unsigned ldsdst) {
  asm volatile("s_mov_b32 m0, %0\n\ts_nop 0\n\tbuffer_load_dwordx4 %1, %2, %3 offen lds" :: "s"(ldsdst), "v"(voff), "s"(r), "s"(soff) : "memory");
}
DEV void dma24(rsrc_t r, unsigned v0, unsigned v1, unsigned soff, unsigned ldsdst) {
  asm volatile("s_mov_b32 m0, %0\n\ts_nop 0\n\tbuffer_load_dwordx4 %1, %3, %4 offen nt lds\n\t"
               "s_mov_b32 exec_hi, 0\n\ts_add_u32 m0, m0, 0x400\n\ts_nop 0\n\tbuffer_load_dwordx4 %2, %3, %4 offen nt lds\n\ts_mov_b32 exec_hi, -1"
               :: "s"(ldsdst), "v"(v0), "v"(v1), "s"(r), "s"(soff) : "memory", "scc");
}
template <int OFF> DEV void ldnt(u32x4& d, rsrc_t r, unsigned voff, unsigned soff) {
  asm volatile("buffer_load_dwordx4 %0, %1, %2, %3 offen offset:%4 nt" : "=v"(d) : "v"(voff), "s"(r), "s"(soff), "n"(OFF) : "memory");
}
template <int OFF> DEV void ldpl(u32x4& d, rsrc_t r, unsigned voff) {
  asm volatile("buffer_load_dwordx4 %0, %1, %2, 0 offen offset:%3" : "=v"(d) : "v"(voff), "s"(r), "n"(OFF) : "memory");
}
template <int AUX> DEV void bst4(u32x4 v, rsrc_t r, unsigned voff, unsigned soff) { __builtin_amdgcn_raw_buffer_store_b128(v, r, voff, soff, AUX); }
template <int N> DEV void vmwait() { asm volatile("s_waitcnt vmcnt(%0)" :: "n"(N) : "memory"); }
DEV unsigned epoch(const void* ws) {
  asm volatile("" : "+s"(ws));
  return *(const __attribute__((address_space(4))) unsigned*)ws + KCHK;
}
DEV f32x2 pkmul(f32x2 a, f32x2 b) { return a * b; }
DEV f32x2 pkadd(f32x2 a, f32x2 b) { return a + b; }
DEV f32x2 pkfma(f32x2 a, f32x2 b, f32x2 c) { return __builtin_elementwise_fma(a, b, c); }
DEV float dot2(unsigned a, unsigned b, float c) {
  return __builtin_amdgcn_fdot2_f32_bf16(__builtin_bit_cast(bf16x2, a), __builtin_bit_cast(bf16x2, b), c, false);
}
template <int CTRL> DEV float dppadd(float x) {
  return cut(x + __builtin_bit_cast(float, __builtin_amdgcn_update_dpp(0, __builtin_bit_cast(int, x), CTRL, 0xf, 0xf, true)));
}

#define BAR() bar()
#define TSX(k)
#define RTX(k)
#define VALX(k, x)
#define TSC(k)
#define TSSAVE()
#define BARARGS
#define BARPASS
#define TSINIT()
#define TSFIN()

#define PRB0()
#define PRB(k)
#define PRBEND()

struct Prod {
  u32x4 W[18];
  u32x4 ring[4][6];
};
struct ProdC {
  lds_char* smem;
  unsigned xw[2], rw, br[2], lw[2], lr[2], vd[2], voH, vw, lds0, ldsv;
  u32x2 I4;
  bool rvalid, odd;
  rsrc_t rhi, rlo, rwv;
};
DEV void lat_read(u32x4 (&B)[6], const ProdC& C, int t, int k0, int k1) {
#pragma unroll
  for (int ks = k0; ks < k1; ++ks) B[ks] = *(lds_u32x4*)(C.smem + C.br[ks & 1] + (t & 3) * 6144 + (ks >> 1) * 128);
}
DEV void lat_mm(const Prod& P, const u32x4 (&B)[6], const ProdC& C, int t) {
  const f32x4 z4 = {0.f, 0.f, 0.f, 0.f};
  f32x4 T[3];
#pragma unroll
  for (int ks = 0; ks < 6; ++ks)
#pragma unroll
    for (int tt = 0; tt < 3; ++tt) {
      T[tt] = mfma32(P.W[tt * 6 + ks], B[ks], ks == 0 ? z4 : T[tt]);
      OPQ(T[tt]);
      SB();
    }
#pragma unroll
  for (int tt = 0; tt < 3; ++tt) *(lds_f32x4*)(C.smem + C.lw[tt & 1] + (t & 1) * 12288 + (tt >> 1) * 128) = T[tt];
}
template <int M> DEV void ld_hi1(u32x4& d, const ProdC& C, unsigned so0, unsigned so1) { ldnt<(M >> 1) * 128>(d, C.rhi, C.voH, (M & 1) ? so1 : so0); }
DEV void ld_hi(u32x4 (&dst)[6], const ProdC& C, unsigned so) {
  const unsigned so1 = so + 196608;
  ld_hi1<0>(dst[0], C, so, so1); ld_hi1<1>(dst[1], C, so, so1); ld_hi1<2>(dst[2], C, so, so1);
  ld_hi1<3>(dst[3], C, so, so1); ld_hi1<4>(dst[4], C, so, so1); ld_hi1<5>(dst[5], C, so, so1);
}
template <int M, bool LOAD, bool LAT, int WAIT>
DEV void psub(Prod& P, const ProdC& C, int t, unsigned so, unsigned so1, f32x4 (&d)[6][2], f32x4 (&lat)[3][2], u32x4 (&B)[6],
              float (&ss)[2], float (&sm)[2]) {
  if constexpr (M < 6) {
    if (WAIT >= 0) vmwait<(WAIT >= 0 ? WAIT - M : 0)>();
    OPQ(P.ring[t & 3][M]);
    const u32x4 F = P.ring[t & 3][M];
    d[M][0] = mfma4(C.I4, u32x2{F[0], F[1]}, lat[M >> 1][0]);
    d[M][1] = mfma4(C.I4, u32x2{F[2], F[3]}, lat[M >> 1][1]);
    OPQ(d[M][0]); OPQ(d[M][1]);
    SB();
    if (LOAD) ld_hi1<M>(P.ring[t & 3][M], C, so, so1);
    if (M == 1) {
#pragma unroll
      for (int u = 0; u < 2; ++u) lat[2][u] = *(lds_f32x4*)(C.smem + C.lr[u] + (t & 1) * 12288 + 512);
    }
    if (LAT && M >= 3) lat_read(B, C, t + 1, 2 * (M - 3), 2 * (M - 3) + 2);
  }
  if constexpr (M > 0) {
    constexpr int k = M - 1, ab = k & 1, L = k >> 1;
    const u32x4 x = cvt8(d[k][0], d[k][1]);
    *(lds_u32x4*)(C.smem + C.xw[ab] + (t & 1) * 24576 + L * 8192) = x;
#pragma unroll
    for (int r = 0; r < 4; ++r) { ss[ab] = dot2(x[r], x[r], ss[ab]); sm[ab] = dot2(x[r], 0x3F803F80u, sm[ab]); }
  }
  SB();
}
template <bool LOAD, bool LAT, int NWV, int WAIT> DEV void pstep(Prod& P, const ProdC& C, int t, unsigned so, unsigned sl, unsigned wv BARARGS) {
  TSC(0);
  float ss[2] = {0.f, 0.f}, sm[2] = {0.f, 0.f};
  f32x4 d[6][2], lat[3][2];
  u32x4 B[6];
#pragma unroll
  for (int L = 0; L < 2; ++L)
#pragma unroll
    for (int u = 0; u < 2; ++u) lat[L][u] = *(lds_f32x4*)(C.smem + C.lr[u] + (t & 1) * 12288 + L * 256);
#pragma unroll
  for (int n = 0; n < NWV; ++n) dma16(C.rwv, C.vw, wv + n * 4096, C.ldsv + wv + n * 4096);
  if (LOAD) dma24(C.rlo, C.vd[0], C.vd[1], sl, C.lds0 + (t & 3) * 6144);
  const unsigned so1 = so + 196608;
  SB();
  TSC(1);
  psub<0, LOAD, LAT, WAIT>(P, C, t, so, so1, d, lat, B, ss, sm);
  psub<1, LOAD, LAT, WAIT>(P, C, t, so, so1, d, lat, B, ss, sm);
  TSC(2);
  psub<2, LOAD, LAT, WAIT>(P, C, t, so, so1, d, lat, B, ss, sm);
  psub<3, LOAD, LAT, WAIT>(P, C, t, so, so1, d, lat, B, ss, sm);
  TSC(3);
  psub<4, LOAD, LAT, WAIT>(P, C, t, so, so1, d, lat, B, ss, sm);
  psub<5, LOAD, LAT, WAIT>(P, C, t, so, so1, d, lat, B, ss, sm);
  psub<6, LOAD, LAT, WAIT>(P, C, t, so, so1, d, lat, B, ss, sm);
  TSC(4);
#pragma unroll
  for (int ab = 0; ab < 2; ++ab) {
    ss[ab] = dppadd<0x141>(dppadd<0x4E>(dppadd<0xB1>(ss[ab])));
    sm[ab] = dppadd<0x141>(dppadd<0x4E>(dppadd<0xB1>(sm[ab])));
  }
  const float mu = cut((C.odd ? sm[1] : sm[0]) * (1.0f / 192.0f));
  const float var = __builtin_fmaxf((C.odd ? ss[1] : ss[0]) * (1.0f / 192.0f) - mu * mu, 0.0f);
  const float rs = __builtin_amdgcn_rsqf(var + 1e-5f);
  if (C.rvalid) *(lds_f32*)(C.smem + C.rw + (t & 1) * 256) = rs;
  SB();
  TSC(5);
  if (LAT) lat_mm(P, B, C, t + 1);
  TSC(6);
}

struct Cons {
  f32x16 U[6];
  u32x4 Ab[12];
  float mrun, lsum;
};
struct ConsC { lds_char* smem; unsigned xr[4], xt[2][2], ra; };
DEV u32x4 row_read(const ConsC& C, int s) { return *(lds_u32x4*)(C.smem + C.xr[s & 3] + (s >> 2) * 8192); }
DEV u32x4 col_read(const ConsC& C, int o) {
  const int ks = o / 6, ct = o % 6;
  const u32x2 a0 = trread(C.smem + C.xt[ct & 1][0] + (ct >> 1) * 8192 + ks * 2048);
  const u32x2 a1 = trread(C.smem + C.xt[ct & 1][1] + (ct >> 1) * 8192 + ks * 2048);
  return u32x4{a0[0], a0[1], a1[0], a1[1]};
}
DEV void cstep(Cons& K, ConsC& C, int dx, int dr, bool first BARARGS) {
  TSC(0);
  u32x4 X[12];
  f32x4 rr[4];
#pragma unroll
  for (int s = 0; s < 12; ++s) X[s] = row_read(C, s);
#pragma unroll
  for (int a = 0; a < 4; ++a) rr[a] = *(lds_f32x4*)(C.smem + C.ra + a * 32);
  SB();
  f32x16 S = {0.f, 0.f, 0.f, 0.f, 0.f, 0.f, 0.f, 0.f, 0.f, 0.f, 0.f, 0.f, 0.f, 0.f, 0.f, 0.f};
#pragma unroll
  for (int s = 0; s < 12; ++s) {
    S = mfma3232(X[s], K.Ab[s], S);
    OPQ(S);
    SB();
    if (s == 0) TSC(1);
    if (s == 11) TSC(2);
    X[s] = col_read(C, s);
    SB();
  }
#pragma unroll
  for (int s = 0; s < 4; ++s) { C.xr[s] += dx; C.xt[s >> 1][s & 1] += dx; OPQ(C.xr[s]); OPQ(C.xt[s >> 1][s & 1]); }
  C.ra += dr; OPQ(C.ra);
  SB();
  float V[16];
  float vmax = -3e38f;
#pragma unroll
  for (int e = 0; e < 16; ++e) { V[e] = cut(S[e] * rr[e >> 2][e & 3] - K.mrun); vmax = __builtin_fmaxf(vmax, V[e]); }
  if (first || __builtin_amdgcn_ballot_w64(!(vmax <= THR)) != 0) {
    auto t = __builtin_amdgcn_permlane32_swap(f2u(vmax), f2u(vmax), false, false);
    float dl = __builtin_fmaxf(u2f(t[0]), u2f(t[1]));
    if (!first) dl = __builtin_fmaxf(dl, 0.f);
    const float al = __builtin_amdgcn_exp2f(__builtin_fminf(-dl, 0.f));
    K.mrun += dl; K.lsum *= al;
#pragma unroll
    for (int c = 0; c < 6; ++c) K.U[c] *= al;
#pragma unroll
    for (int e = 0; e < 16; ++e) V[e] -= dl;
  }
  TSC(3);
  u32x4 Pb[2];
  float pe[16];
#pragma unroll
  for (int e = 0; e < 8; ++e) pe[e] = __builtin_amdgcn_exp2f(V[e]);
#pragma unroll
  for (int e = 0; e < 8; e += 2) Pb[0][e >> 1] = cvtpk(cut(pe[e] * rr[e >> 2][e & 3]), cut(pe[e + 1] * rr[e >> 2][(e & 3) + 1]));
  SB();
  TSC(4);
#pragma unroll
  for (int ct = 0; ct < 6; ++ct) {
    K.U[ct] = mfma3232(X[ct], Pb[0], K.U[ct]);
    OPQ(K.U[ct]);
    SB();
    if (ct < 4) {
      const int e = 8 + 2 * ct;
      pe[e] = __builtin_amdgcn_exp2f(V[e]); pe[e + 1] = __builtin_amdgcn_exp2f(V[e + 1]);
      Pb[1][ct] = cvtpk(cut(pe[e] * rr[e >> 2][e & 3]), cut(pe[e + 1] * rr[e >> 2][(e & 3) + 1]));
    } else {
      const int e = 4 * (ct - 4);
      K.lsum += cut(pe[e] + pe[e + 1]) + cut(pe[e + 2] + pe[e + 3]);
    }
    SB();
  }
  TSC(5);
#pragma unroll
  for (int ct = 0; ct < 6; ++ct) {
    K.U[ct] = mfma3232(X[6 + ct], Pb[1], K.U[ct]);
    OPQ(K.U[ct]);
    SB();
    if (ct < 2) { const int e = 8 + 4 * ct; K.lsum += cut(pe[e] + pe[e + 1]) + cut(pe[e + 2] + pe[e + 3]); }
    SB();
  }
  TSC(6);
}
DEV void cfinish(Cons& K, lds_char* smem, int h, int b, rsrc_t rws, unsigned ek BARARGS) {
  const int lane = laneid(), i = lane & 15, g = lane >> 4;
  const int xo = (h >> 1) ? LDS_LAT : LDS_X, PH = h & 1;
  f32x4 Uo[12];
  lds_char* ex = smem + xo + PH * 12288 + lane * 16;
#pragma unroll
  for (int ct = 0; ct < 6; ++ct)
#pragma unroll
    for (int a = 0; a < 2; ++a) {
      f32x4 k0, k1;
#pragma unroll
      for (int r = 0; r < 4; ++r) {
        auto sw = __builtin_amdgcn_permlane16_swap(f2u(K.U[ct][8 * a + r]), f2u(K.U[ct][8 * a + 4 + r]), false, false);
        k0[r] = u2f(sw[0]); k1[r] = u2f(sw[1]);
      }
      Uo[2 * ct + a] = k0;
      *(lds_f32x4*)(ex + (2 * ct + a) * 1024) = k1;
      SB();
    }
  TSX(51);
  float mo, lo_;
  {
    auto t = __builtin_amdgcn_permlane32_swap(f2u(K.lsum), f2u(K.lsum), false, false);
    const float l = u2f(t[0]) + u2f(t[1]);
    auto sl = __builtin_amdgcn_permlane16_swap(f2u(l), f2u(l), false, false);
    auto sm = __builtin_amdgcn_permlane16_swap(f2u(K.mrun), f2u(K.mrun), false, false);
    mo = u2f(sm[0]); lo_ = u2f(sl[0]);
    *(__attribute__((address_space(3))) f32x2*)(smem + LDS_ML + h * 512 + lane * 8) = f32x2{u2f(sm[1]), u2f(sl[1])};
  }
  BAR();
  const f32x2 ml = *(__attribute__((address_space(3))) f32x2*)(smem + LDS_ML + (h ^ 1) * 512 + lane * 8);
  lds_char* ey = smem + xo + (1 - PH) * 12288 + lane * 16;
  f32x4 o[12];
#pragma unroll
  for (int c = 0; c < 12; ++c) o[c] = *(lds_f32x4*)(ey + c * 1024);
  lds_char* wv = smem + LDS_WV + h * 18432 + (8 * (g & 1) + 4 * (g >> 1) + (i >> 2)) * 96 + (i & 3) * 8;
  u32x4 Wv[18];
#pragma unroll
  for (int n = 0; n < 18; ++n) {
    const u32x2 a0 = trread(wv + (n / 3) * 3072 + (n % 3) * 32), a1 = trread(wv + (n / 3) * 3072 + 1536 + (n % 3) * 32);
    Wv[n] = u32x4{a0[0], a0[1], a1[0], a1[1]};
  }
  SB();
  const float mn = __builtin_fmaxf(mo, ml[0]);
  const float fa = __builtin_amdgcn_exp2f(mo - mn), fb = __builtin_amdgcn_exp2f(ml[0] - mn);
  const float l = lo_ * fa + ml[1] * fb, il = __builtin_amdgcn_rcpf(l);
  const unsigned lse = (unsigned)__builtin_amdgcn_ds_bpermute((4 * g + (i & 3)) * 4, (int)f2u(mn + __builtin_amdgcn_logf(l)));
  const f32x2 fa2 = {fa * il, fa * il}, fb2 = {fb * il, fb * il};
  f32x2 u2[24], us[2];
#pragma unroll
  for (int c = 0; c < 24; ++c) {
    const f32x2 own = {Uo[c >> 1][(c & 1) * 2], Uo[c >> 1][(c & 1) * 2 + 1]}, oth = {o[c >> 1][(c & 1) * 2], o[c >> 1][(c & 1) * 2 + 1]};
    u2[c] = pkfma(oth, fb2, pkmul(own, fa2));
    us[c & 1] = c < 2 ? u2[c] : pkadd(us[c & 1], u2[c]);
  }
  TSX(52);
  const float um = xg_sum((us[0][0] + us[0][1]) + (us[1][0] + us[1][1])) * (-1.0f / 192.0f);
  const f32x2 um2 = {um, um};
  const f32x4 z4 = {0.f, 0.f, 0.f, 0.f};
  f32x4 cx[3] = {z4, z4, z4};
#pragma unroll
  for (int s = 0; s < 6; ++s) {
    u32x4 ub;
#pragma unroll
    for (int r = 0; r < 4; ++r) { const f32x2 v = pkadd(u2[4 * s + r], um2); ub[r] = cvtpk(v[0], v[1]); }
#pragma unroll
    for (int dt = 0; dt < 3; ++dt) cx[dt] = mfma32(ub, Wv[3 * s + dt], cx[dt]);
  }
  TSX(53);
  const u32x4 ra = cvt8(cx[0], cx[1]);
  u32x4 rb = {cvtpk(cx[2][0], cx[2][1]), cvtpk(cx[2][2], cx[2][3]), lse, 0u};
  rb[3] = (ra[0] + ra[1] + ra[2]) + (ra[3] + rb[0] + rb[1]) + lse + ek;
  const unsigned vo = (lane >> 2) * 32768 + (lane & 3) * 16, so = WS_REC + h * 524288 + b * 64;
  bst4<SC1>(ra, rws, vo, so);
  bst4<SC1>(rb, rws, vo, so + 16384);
}

struct AIn { u32x4 q[6]; };
DEV void a_issue(AIn& I, unsigned lbase, int h, int cs, const void* q_, const void* wq_, const void* wk_, const void* wm_) {
  const int lane = laneid();
  const rsrc_t rq = mkrsrc(q_, 6144u), rwq = mkrsrc(wq_, 73728u), rwk = mkrsrc(wk_, 73728u), rwm = mkrsrc(wm_, 73728u);
  const unsigned vl = lane * 16;
#pragma unroll
  for (int n = 0; n < 18; ++n) dma16(rwq, vl, h * 18432 + n * 1024, lbase + LDS_AWQ + n * 1024);
  unsigned vo[3];
#pragma unroll
  for (int m = 0; m < 3; ++m) { const unsigned o = m * 1024 + vl; vo[m] = (o / 96) * 384 + o % 96; }
#pragma unroll
  for (int n = 0; n < 18; ++n) dma16(rwm, vo[n % 3], (n / 3) * 12288 + cs * 96, lbase + LDS_AWM + n * 1024);
#pragma unroll
  for (int n = 0; n < 5; ++n) dma16(rwk, vl, h * 18432 + cs * 4608 + n * 1024, lbase + LDS_AWK + n * 1024);
  const unsigned vq = (lane & 15) * 384 + (lane >> 4) * 16;
  ldpl<0>(I.q[0], rq, vq); ldpl<64>(I.q[1], rq, vq); ldpl<128>(I.q[2], rq, vq);
  ldpl<192>(I.q[3], rq, vq); ldpl<256>(I.q[4], rq, vq); ldpl<320>(I.q[5], rq, vq);
}
DEV void a_compute(AIn& I, lds_char* smem, int h, int cs, rsrc_t rws, unsigned ek) {
  const int lane = laneid(), i = lane & 15, g = lane >> 4;
  vmwait<0>();
#pragma unroll
  for (int s = 0; s < 6; ++s) OPQ(I.q[s]);
  SB();
  lds_char* a = smem + (8 * g + (i >> 2)) * 96 + (i & 3) * 8;
  const f32x4 z4 = {0.f, 0.f, 0.f, 0.f};
  f32x4 Q[3], M[3], T[3];
  u32x4 W[18], V[18];
  u32x2 Kf[9];
#pragma unroll
  for (int n = 0; n < 18; ++n) {
    const u32x2 a0 = trread(a + LDS_AWQ + (n / 3) * 3072 + (n % 3) * 32), a1 = trread(a + LDS_AWQ + (n / 3) * 3072 + 384 + (n % 3) * 32);
    W[n] = u32x4{a0[0], a0[1], a1[0], a1[1]};
  }
#pragma unroll
  for (int n = 0; n < 18; ++n) {
    const u32x2 a0 = trread(a + LDS_AWM + (n / 3) * 3072 + (n % 3) * 32), a1 = trread(a + LDS_AWM + (n / 3) * 3072 + 384 + (n % 3) * 32);
    V[n] = u32x4{a0[0], a0[1], a1[0], a1[1]};
  }
#pragma unroll
  for (int n = 0; n < 9; ++n) Kf[n] = *(__attribute__((address_space(3))) u32x2*)(smem + LDS_AWK + i * 96 + g * 8 + (n / 3) * 1536 + (n % 3) * 32);
  SB();
#pragma unroll
  for (int n = 0; n < 18; ++n) Q[n % 3] = mfma32(W[n], I.q[n / 3], n < 3 ? z4 : Q[n % 3]);
#pragma unroll
  for (int n = 0; n < 18; ++n) M[n % 3] = mfma32(V[n], I.q[n / 3], n < 3 ? z4 : M[n % 3]);
#pragma unroll
  for (int dt = 0; dt < 3; ++dt) {
    const u32x2 qb = {cvtpk(Q[dt][0], Q[dt][1]), cvtpk(Q[dt][2], Q[dt][3])};
#pragma unroll
    for (int ct = 0; ct < 3; ++ct) T[ct] = mfma16(Kf[3 * ct + dt], qb, dt == 0 ? z4 : T[ct]);
  }
  const float s1 = 1.4426950408889634f * 0.14433756729740643f;
  unsigned P[6];
#pragma unroll
  for (int n = 0; n < 6; ++n)
    P[n] = cvtpk((T[n >> 1][(n & 1) * 2] + 0.5f * M[n >> 1][(n & 1) * 2]) * s1, (T[n >> 1][(n & 1) * 2 + 1] + 0.5f * M[n >> 1][(n & 1) * 2 + 1]) * s1);
  const unsigned so = WS_A + h * 8192 + cs * 2048;
  bst4<SC1>(u32x4{P[0], P[1], P[2], P[0] + P[1] + P[2] + ek}, rws, lane * 16, so);
  bst4<SC1>(u32x4{P[4], P[5], P[3], P[4] + P[5] + P[3] + ek}, rws, lane * 16 + 1024, so);
}
DEV void a_poll(u32x4 (&G)[8], int h, rsrc_t rws, unsigned ek BARARGS) {
  const int lane = laneid();
  RTX(44);
  for (int spin = 0;; ++spin) {
    VALX(46, spin);
    asm volatile("" ::: "memory");
#pragma unroll
    for (int n = 0; n < 8; ++n) G[n] = bld4<SC1>(rws, lane * 16 + (n & 3) * 1024, WS_A + h * 8192 + (n >> 2) * 4096);
    bool bad = false;
#pragma unroll
    for (int n = 0; n < 8; ++n) bad |= G[n][0] + G[n][1] + G[n][2] + ek != G[n][3];
    if (__builtin_amdgcn_ballot_w64(bad) == 0 || spin >= SPIN_MAX) break;
  }
  RTX(45);
}
DEV void a_centre(const u32x4 (&G)[8], lds_char* smem, int h) {
  const int lane = laneid(), i = lane & 15, g = lane >> 4;
  float sm[2] = {0.f, 0.f};
#pragma unroll
  for (int n = 0; n < 24; ++n) sm[n & 1] = dot2(G[n / 3][n % 3], 0x3F803F80u, sm[n & 1]);
  const float mu = xg_sum(sm[0] + sm[1]) * (-1.0f / 192.0f);
  const f32x4 mu4 = {mu, mu, mu, mu};
  const int d = lane & 3;
  const u32x2 I4 = {d == 0 ? 0x00003F80u : (d == 1 ? 0x3F800000u : 0u), d == 2 ? 0x00003F80u : (d == 3 ? 0x3F800000u : 0u)};
  lds_char* dst = smem + LDS_AF + (h >> 1) * 12288 + (g >> 1) * 512 + ((h & 1) * 16 + i) * 16 + (g & 1) * 8;
#pragma unroll
  for (int s = 0; s < 12; ++s) {
    const int cs = s / 3, ct = s % 3;
    const u32x2 v = ct == 0 ? u32x2{G[2 * cs][0], G[2 * cs][1]} : (ct == 1 ? u32x2{G[2 * cs][2], G[2 * cs + 1][2]} : u32x2{G[2 * cs + 1][0], G[2 * cs + 1][1]});
    const f32x4 c = mfma4(I4, v, mu4);
    *(__attribute__((address_space(3))) u32x2*)(dst + s * 1024) = u32x2{cvtpk(c[0], c[1]), cvtpk(c[2], c[3])};
  }
}
template <int CTRL> DEV float dppmov(float x) { return __builtin_bit_cast(float, __builtin_amdgcn_update_dpp(0, __builtin_bit_cast(int, x), CTRL, 0xf, 0xf, true)); }
DEV float quads_sum(float x) { x += dppmov<0x124>(x); return x + dppmov<0x128>(x); }
DEV float quads_max(float x) { x = __builtin_fmaxf(x, dppmov<0x124>(x)); return __builtin_fmaxf(x, dppmov<0x128>(x)); }
DEV void reduce1(lds_char* smem, int r, int p, rsrc_t rws, unsigned ek BARARGS) {
  const int lane = laneid(), i = lane & 15, g = lane >> 4, d = lane & 3;
  const unsigned m0 = d == 0 ? 0x0000FFFFu : (d == 1 ? 0xFFFF0000u : 0u), m1 = d == 2 ? 0x0000FFFFu : (d == 3 ? 0xFFFF0000u : 0u);
  u32x4 RA[4], RB[4];
  const unsigned so = WS_REC + r * 32768 + p * 4096;
  RTX(44);
  for (int spin = 0;; ++spin) {
    VALX(46, spin);
    asm volatile("" ::: "memory");
#pragma unroll
    for (int j = 0; j < 4; ++j) { RB[j] = bld4<SC1>(rws, lane * 16 + j * 1024, so + 16384); RA[j] = bld4<SC1>(rws, lane * 16 + j * 1024, so); }
    bool bad = false;
#pragma unroll
    for (int j = 0; j < 4; ++j) bad |= (RA[j][0] + RA[j][1] + RA[j][2]) + (RA[j][3] + RB[j][0] + RB[j][1]) + RB[j][2] + ek != RB[j][3];
    if (__builtin_amdgcn_ballot_w64(bad) == 0 || spin >= SPIN_MAX) break;
  }
  RTX(45);
  const float M = xg_max(quads_max(__builtin_fmaxf(__builtin_fmaxf(u2f(RB[0][2]), u2f(RB[1][2])), __builtin_fmaxf(u2f(RB[2][2]), u2f(RB[3][2])))));
  const f32x4 z4 = {0.f, 0.f, 0.f, 0.f};
  f32x4 acc[3] = {z4, z4, z4};
  float L = 0.f;
#pragma unroll
  for (int j = 0; j < 4; ++j) {
    const float wf = __builtin_amdgcn_exp2f(u2f(RB[j][2]) - M);
    const unsigned wb = cvtpk(wf, wf);
    L += u2f(wb & 0xFFFF0000u);
    const u32x2 dg = {wb & m0, wb & m1};
    acc[0] = mfma4(dg, u32x2{RA[j][0], RA[j][1]}, acc[0]);
    acc[1] = mfma4(dg, u32x2{RA[j][2], RA[j][3]}, acc[1]);
    acc[2] = mfma4(dg, u32x2{RB[j][0], RB[j][1]}, acc[2]);
  }
  L = xg_sum(quads_sum(L));
  float u[6];
  f32x4 v;
#pragma unroll
  for (int k = 0; k < 6; ++k) {
    auto t = __builtin_amdgcn_permlane32_swap(f2u(acc[k % 3][k / 3]), f2u(acc[k % 3][2 + k / 3]), false, false);
    u[k] = u2f(t[0]) + u2f(t[1]);
  }
#pragma unroll
  for (int k = 0; k < 3; ++k) {
    auto t = __builtin_amdgcn_permlane16_swap(f2u(u[k]), f2u(u[k + 3]), false, false);
    v[k] = quads_sum(u2f(t[0]) + u2f(t[1]));
  }
  v[3] = 0.f;
  if (i < 4) *(lds_f32x4*)(smem + LDS_RED + p * 256 + g * 64 + d * 16) = v;
  if (lane < 4) *(__attribute__((address_space(3))) f32x2*)(smem + LDS_RED + 1024 + p * 32 + d * 8) = f32x2{M, L};
  bar();
  if (p == 0) {
    const f32x4 x = *(lds_f32x4*)(smem + LDS_RED + (i >> 2) * 256 + g * 64 + d * 16);
    const f32x2 ml = *(__attribute__((address_space(3))) f32x2*)(smem + LDS_RED + 1024 + (i >> 2) * 32 + g * 8);
    const float f = __builtin_amdgcn_exp2f(ml[0] - quads_max(ml[0]));
    const float il = __builtin_amdgcn_rcpf(quads_sum(ml[1] * f));
    u32x4 o;
#pragma unroll
    for (int k = 0; k < 3; ++k) { const float c = quads_sum(x[k] * f) * il; o[k] = cvtpk(c, dppmov<0xB1>(c)); }
    o[3] = o[0] + o[1] + o[2] + ek;
    if (i == 0 || i == 2) bst4<SC1>(o, rws, (d >> 1) * 64 + g * 16, WS_CTX + r * 128);
  }
}
DEV void reduce2_issue(u32x2& qr, unsigned lbase, int nt, const void* q_, const void* wo_) {
  const int lane = laneid();
  const rsrc_t rq = mkrsrc(q_, 6144u), rwo = mkrsrc(wo_, 73728u);
  const unsigned vo = (lane >> 1) * 384 + (lane & 1) * 16, vq = (lane & 15) * 384 + (lane >> 4) * 8;
#pragma unroll
  for (int n = 0; n < 6; ++n) dma16(rwo, vo, n * 12288 + nt * 32, lbase + LDS_WO + n * 1024);
  asm volatile("buffer_load_dwordx2 %0, %1, %2, %3 offen" : "=v"(qr) : "v"(vq), "s"(rq), "s"(nt * 32) : "memory");
}
DEV void reduce2(u32x2& qr, lds_char* smem, int nt, rsrc_t rws, unsigned ek, void* out_ BARARGS) {
  const int lane = laneid(), i = lane & 15, g = lane >> 4, rho = i >> 2;
  vmwait<0>();
  OPQ(qr);
  SB();
  lds_char* a0 = smem + LDS_WO + (16 * (rho >> 1) + 4 * g + (rho & 1)) * 32 + (i & 3) * 8;
  lds_char* a1 = smem + LDS_WO + (32 + 4 * g + (rho & 1)) * 32 + (i & 3) * 8;
  u32x4 W[8], G[8];
#pragma unroll
  for (int m = 0; m < 8; ++m) {
    const u32x2 x0 = trread(a0 + ((m >> 1) * 48 + 2 * (m & 1)) * 32), x1 = trread(a1 + ((m >> 1) * 48 + 2 * (m & 1)) * 32);
    W[m] = u32x4{x0[0], x0[1], x1[0], x1[1]};
    OPQ(W[m]);
  }
  const unsigned vo = (i >> 2) * 512 + g * 128 + (i & 3) * 16;
  RTX(44);
  for (int spin = 0;; ++spin) {
    VALX(46, spin);
    asm volatile("" ::: "memory");
#pragma unroll
    for (int m = 0; m < 8; ++m) G[m] = bld4<SC1>(rws, vo + (m & 1) * 64 + ((m >> 1) & 1) * 2048, WS_CTX + (m >> 2) * 4096);
    bool bad = false;
#pragma unroll
    for (int m = 0; m < 8; ++m) bad |= G[m][0] + G[m][1] + G[m][2] + ek != G[m][3];
    if (__builtin_amdgcn_ballot_w64(bad) == 0 || spin >= SPIN_MAX) break;
  }
  RTX(45);
  f32x4 c[2] = {{u2f(qr[0] << 16), u2f(qr[0] & 0xFFFF0000u), u2f(qr[1] << 16), u2f(qr[1] & 0xFFFF0000u)}, {0.f, 0.f, 0.f, 0.f}};
#pragma unroll
  for (int m = 0; m < 8; ++m) { G[m][3] = 0u; c[m & 1] = mfma32(W[m], G[m], c[m & 1]); }
  const f32x4 o = c[0] + c[1];
  __builtin_amdgcn_raw_buffer_store_b64(u32x2{cvtpk(o[0], o[1]), cvtpk(o[2], o[3])}, mkrsrc(out_, 6144u), i * 384 + g * 8, nt * 32, 0);
  if (nt == 0 && lane == 0) __builtin_amdgcn_raw_buffer_store_b32(ek - KCHK + 1u, rws, 0, 0, 0);
}

__global__ void __launch_bounds__(512, 2)
m2f_main(const void* __restrict__ fhi_, const void* __restrict__ flo_, const void* __restrict__ wlat_,
         const void* __restrict__ wv_, const void* __restrict__ q_, const void* __restrict__ wq_, const void* __restrict__ wk_,
         const void* __restrict__ wm_, const void* __restrict__ wo_, void* __restrict__ ws_, void* __restrict__ out_) {
  __shared__ __attribute__((aligned(16))) char smem_[LDS_TOTAL];
  lds_char* smem = (lds_char*)smem_;
  const int w = __builtin_amdgcn_readfirstlane(__builtin_amdgcn_workitem_id_x() >> 6);
  const int b = __builtin_amdgcn_workgroup_id_x();
  TSINIT();
  PRB0();

  if (w < 4) {
    const int p = w, lane = laneid(), i = lane & 15, g = lane >> 4;
#define PRIO_P 2
    __builtin_amdgcn_s_setprio(PRIO_P);
    if (b < 16) bar();
    const rsrc_t rwl = mkrsrc(wlat_, 73728u);
    ProdC C;
    C.smem = smem;
    C.rhi = mkrsrc(fhi_, 100663296u);
    C.rlo = mkrsrc(flo_, 25165824u);
    C.rwv = mkrsrc(wv_, 73728u);
    const unsigned j = lane >> 3, c7 = lane & 7;
    const unsigned lbase = (unsigned)(uintptr_t)smem;
    C.voH = j * 384 + c7 * 16;
    C.vw = lane * 16;
    C.lds0 = lbase + LDS_LO + p * 1536;
    C.ldsv = lbase + LDS_WV;
#pragma unroll
    for (int k = 0; k < 2; ++k) {
      const unsigned o = k * 1024 + (lane & (k ? 31 : 63)) * 16, px = o / 384, pc = (o % 384) / 16;
      C.vd[k] = px * 384 + ((pc ^ (2 * p + (px >> 1))) * 16);
    }
    const unsigned soL = (unsigned)(b * 256 + p * 4) * 384u;
    const unsigned soH = (unsigned)(b * 1024 + p * 8) * 384u;
    Prod P;
    dma24(C.rlo, C.vd[0], C.vd[1], soL, C.lds0);
    dma24(C.rlo, C.vd[0], C.vd[1], soL + 6144, C.lds0 + 6144);
    SB();
    {
      unsigned vo[6];
#pragma unroll
      for (int m = 0; m < 6; ++m) {
        const unsigned o = m * 1024 + lane * 16, k = o / 384, pc = (o % 384) / 16;
        vo[m] = k * 384 + ((pc ^ (2 * (((k >> 1) & 1) | ((k >> 3) << 1)))) * 16);
      }
      const unsigned so1 = soH + 196608, wo = p * 18432, wl = lbase + LDS_WL + p * 18432;
#define WGRP(M) ld_hi1<M>(P.ring[0][M], C, soH, so1); \
      for (int n = 3 * M; n < 3 * M + 3; ++n) dma16(rwl, vo[n % 6], wo + (n / 6) * 6144, wl + n * 1024);
      WGRP(0) WGRP(1) WGRP(2) WGRP(3) WGRP(4) WGRP(5)
#undef WGRP
    }
    SB();
    ld_hi(P.ring[1], C, soH + 12288);
    dma24(C.rlo, C.vd[0], C.vd[1], soL + 2 * 6144, C.lds0 + 2 * 6144);
    SB();
    {
      const int d = lane & 3;
      C.I4[0] = d == 0 ? 0x00003F80u : (d == 1 ? 0x3F800000u : 0u);
      C.I4[1] = d == 2 ? 0x00003F80u : (d == 3 ? 0x3F800000u : 0u);
#pragma unroll
      for (int ab = 0; ab < 2; ++ab) {
        C.xw[ab] = LDS_X + (16 * p + 8 * ab + j) * 128 + ((c7 ^ (((j >> 1) & 1) * 4 + 2 * ab + (j >> 2))) * 16);
        C.br[ab] = LDS_LO + i * 384 + (((4 * ab + g) ^ (i >> 1)) * 16);
        C.lw[ab] = LDS_LAT + i * 768 + (((12 * p + 4 * ab + g) ^ (i & 7)) * 16);
        C.lr[ab] = LDS_LAT + (4 * p + (j >> 1)) * 768 + (((2 * c7 + ab) ^ ((4 * p + (j >> 1)) & 7)) * 16);
      }
      C.rvalid = c7 < 2; C.odd = lane & 1;
      C.rw = LDS_R + (16 * p + 8 * (lane & 1) + j) * 4;
#pragma unroll
      for (int ab = 0; ab < 2; ++ab) { OPQ(C.xw[ab]); OPQ(C.br[ab]); OPQ(C.lw[ab]); OPQ(C.lr[ab]); OPQ(C.vd[ab]); }
      OPQ(C.rw); OPQ(C.vw);
    }
    SB();
    unsigned ek = epoch(ws_);
    SB();
    vmwait<8>();
    asm volatile("" : "+s"(ek));
    BAR();
    PRB(1);
    ld_hi(P.ring[2], C, soH + 2 * 12288);
    SB();
    {
      const unsigned rp = i >> 2, cp = i & 3, s = (rp >> 1) | ((g & 1) << 1);
      u32x4 B[6];
      lat_read(B, C, 0, 0, 6);
#pragma unroll
      for (int tt = 0; tt < 3; ++tt) {
        const unsigned ct = 3 * p + tt;
        lds_char* a = smem + LDS_WL + (8 * g + rp) * 384 + (((ct & 3) ^ s) * 32) + cp * 8 + (ct >> 2) * 128;
#pragma unroll
        for (int ks = 0; ks < 6; ++ks) {
          const u32x2 a0 = trread(a + ks * 12288), a1 = trread(a + ks * 12288 + 1536);
          P.W[tt * 6 + ks] = u32x4{a0[0], a0[1], a1[0], a1[1]};
        }
      }
      SB();
      lat_mm(P, B, C, 0);
    }
    BAR();
    PRB(2);
    dma24(C.rlo, C.vd[0], C.vd[1], soL + 3 * 6144, C.lds0 + 3 * 6144);
    ld_hi(P.ring[3], C, soH + 3 * 12288);
    SB();
#pragma unroll 1
    for (int u = 0; u < 3; ++u) {
      const unsigned so = soH + (u + 1) * 49152, sl = soL + (u + 1) * 24576, wv = p * 1024 + u * 20480;
      pstep<true, true, 1, -1>(P, C, 0, so, sl, wv BARPASS); vmwait<22>(); BAR(); if (u == 0) PRB(3);
      pstep<true, true, 1, -1>(P, C, 1, so + 12288, sl + 6144, wv + 4096 BARPASS); vmwait<22>(); BAR(); if (u == 0) PRB(4);
      pstep<true, true, 1, -1>(P, C, 2, so + 2 * 12288, sl + 2 * 6144, wv + 2 * 4096 BARPASS); vmwait<22>(); BAR();
      pstep<true, true, 2, -1>(P, C, 3, so + 3 * 12288, sl + 3 * 6144, wv + 3 * 4096 BARPASS); vmwait<22>(); BAR();
    }
    pstep<false, true, 0, -1>(P, C, 0, 0, 0, 0 BARPASS); vmwait<14>(); BAR();
    pstep<false, true, 0, -1>(P, C, 1, 0, 0, 0 BARPASS); vmwait<6>(); TSSAVE(); BAR();
    pstep<false, true, 3, -1>(P, C, 2, 0, 0, p * 1024 + 61440 BARPASS); BAR();
    pstep<false, false, 0, 8>(P, C, 3, 0, 0, 0 BARPASS); BAR();
    PRB(6);
    const bool l2 = p == 0 && b >= L2_B0 && b < L2_B0 + 12;
    u32x2 qr;
    if (l2) { reduce2_issue(qr, lbase, b - L2_B0, q_, wo_); vmwait<7>(); } else vmwait<0>();
    BAR();
    PRB(7);
    if (b >= L1_B0 && b < L1_B0 + 64) reduce1(smem, b - L1_B0, p, mkrsrc(ws_, WS_BYTES), ek BARPASS);
    if (l2) reduce2(qr, smem, b - L2_B0, mkrsrc(ws_, WS_BYTES), ek, out_ BARPASS);
    PRB(9);
    PRBEND();
  } else {
    const int h = w - 4, hp = h >> 1, ph = h & 1, lane = laneid(), i = lane & 15, g = lane >> 4;
    const rsrc_t rws = mkrsrc(ws_, WS_BYTES);
    Cons K;
#define PRIO_C 0
    __builtin_amdgcn_s_setprio(PRIO_C);
    unsigned ek;
    if (b < 16 && h == 0) {
      AIn I;
      a_issue(I, (unsigned)(uintptr_t)smem, b >> 2, b & 3, q_, wq_, wk_, wm_);
      ek = epoch(ws_);
      asm volatile("s_barrier" ::: "memory");
      RTX(47);
      a_compute(I, smem, b >> 2, b & 3, rws, ek);
      RTX(48);
    } else {
      if (b < 16) bar();
      ek = epoch(ws_);
    }
    asm volatile("" : "+s"(ek));
    u32x4 G[8];
    BAR();
    a_poll(G, h, rws, ek BARPASS);
    BAR();
    a_centre(G, smem, h);
    ConsC C;
    C.smem = smem;
    const unsigned hl = lane >> 5;
    {
      const unsigned m = lane & 31, row = 32 * ph + m, f = ((m >> 1) & 1) * 4 + ((m >> 2) & 3);
#pragma unroll
      for (int s = 0; s < 4; ++s) C.xr[s] = LDS_X + row * 128 + (((2 * s + hl) ^ f) * 16);
      const unsigned cb = g & 1, rp = i >> 2, cp = i & 3;
#pragma unroll
      for (int c = 0; c < 2; ++c)
#pragma unroll
        for (int sec = 0; sec < 2; ++sec)
          C.xt[c][sec] = LDS_X + (32 * ph + 8 * sec + 4 * hl + rp) * 128 + (((4 * c + 2 * cb + (cp >> 1)) ^ ((rp >> 1) * 4 + hl + 2 * sec)) * 16) + (cp & 1) * 8;
      C.ra = LDS_R + (32 * ph + 4 * hl) * 4;
#pragma unroll
      for (int s = 0; s < 4; ++s) { OPQ(C.xr[s]); OPQ(C.xt[s >> 1][s & 1]); }
      OPQ(C.ra);
    }
#pragma unroll
    for (int c = 0; c < 6; ++c)
#pragma unroll
      for (int e = 0; e < 16; ++e) K.U[c][e] = 0.f;
    K.mrun = 0.f; K.lsum = 0.f;
#pragma unroll 1
    for (int t = 0; t < 16; ++t) {
      BAR();
      if (t == 0) {
        lds_char* af = smem + LDS_AF + hp * 12288 + (laneid() ^ (16 * ph)) * 16;
#pragma unroll
        for (int s = 0; s < 12; ++s) K.Ab[s] = *(lds_u32x4*)(af + s * 1024);
      }
      cstep(K, C, (t & 1) ? -24576 : 24576, (t & 1) ? -256 : 256, t == 0 BARPASS);
    }
    TSX(50);
    cfinish(K, smem, h, b, rws, ek BARPASS);
  }
  TSFIN();
}

__global__ void tiny_w(unsigned* ws) { unsigned long long t; asm volatile("s_memrealtime %0\n\ts_waitcnt lgkmcnt(0)" : "=s"(t) :: "memory"); if (threadIdx.x == 0) ws[2u << 20] = (unsigned)t; }
__global__ void tiny_s(unsigned* ws) { unsigned t0 = __builtin_amdgcn_readfirstlane(ws[2u << 20]); for (int it_ = 0; it_ < 100000; ++it_) { unsigned long long t_; asm volatile("s_sleep 1\n\ts_memrealtime %0\n\ts_waitcnt lgkmcnt(0)" : "=s"(t_) :: "memory"); if ((unsigned)t_ - t0 >= 6000u) break; } }
extern "C" void kernel_launch(void* const* d_in, const int* in_sizes, int n_in, void* d_out, int out_size, void* d_ws,
                              size_t ws_size, hipStream_t stream) {
  tiny_w<<<1, 64, 0, stream>>>((unsigned*)d_ws);
  m2f_main<<<256, 512, 0, stream>>>(d_in[1], d_in[2], d_in[3], d_in[6], d_in[0], d_in[4], d_in[5], d_in[8], d_in[7], d_ws, d_out);
  tiny_s<<<1, 64, 0, stream>>>((unsigned*)d_ws);
}
```

```cpp
#define PROBE 1
#define PROBE_S -1
#define PROBE_B 128
#define PROBE_CLK "s_memrealtime"
#define PROBE_MUL 4u
#define PROBE_DIV 1u
#include <hip/hip_runtime.h>
#include <stdint.h>

typedef __attribute__((ext_vector_type(8))) __bf16 bf16x8;
typedef __attribute__((ext_vector_type(2))) __bf16 bf16x2;
typedef __attribute__((ext_vector_type(4))) short s16x4;
typedef __attribute__((ext_vector_type(4))) float f32x4;
typedef __attribute__((ext_vector_type(2))) float f32x2;
typedef __attribute__((ext_vector_type(4))) unsigned u32x4;
typedef __attribute__((ext_vector_type(2))) unsigned u32x2;
typedef __attribute__((address_space(3))) char lds_char;
typedef __attribute__((address_space(3))) s16x4 lds_s16x4;
typedef __attribute__((address_space(3))) u32x4 lds_u32x4;
typedef __attribute__((address_space(3))) f32x4 lds_f32x4;
typedef __attribute__((address_space(3))) float lds_f32;

typedef __attribute__((ext_vector_type(16))) float f32x16;
#define DEV __device__ __forceinline__
#define SB() __builtin_amdgcn_sched_barrier(0)
#define OPQ(x) asm volatile("" : "+v"(x))

constexpr int LDS_X = 0;
constexpr int LDS_WL = 24576;
constexpr int LDS_WV = 49152;
constexpr int LDS_LO = 110592;
constexpr int LDS_LAT = 135168;
constexpr int LDS_R = 159744;
constexpr int LDS_ML = 160256;
constexpr int LDS_TOTAL = 162304;
constexpr int LDS_AF = 86016;
constexpr int LDS_AWQ = 0, LDS_AWM = LDS_LAT, LDS_AWK = LDS_LAT + 18432;
constexpr int LDS_WO = LDS_LO + 2 * 6144, LDS_RED = LDS_LO + 3 * 6144;
constexpr float THR = 16.0f;
constexpr unsigned WS_A = 4096, WS_REC = 65536, WS_CTX = WS_REC + 64 * 32768, WS_BYTES = WS_CTX + 8192;
constexpr unsigned KCHK = 0x9E3779B9u;
constexpr int SC1 = 16, SPIN_MAX = 1 << 15;
constexpr int L1_B0 = 64, L2_B0 = 128;
#define APOLL 0

DEV float cut(float x) { asm("" : "+v"(x)); return x; }
DEV unsigned cvtpk(float lo, float hi) {
  f32x2 v = {lo, hi};
  return __builtin_bit_cast(unsigned, __builtin_convertvector(v, bf16x2));
}
DEV u32x4 cvt8(f32x4 a, f32x4 b) { return u32x4{cvtpk(a[0], a[1]), cvtpk(a[2], a[3]), cvtpk(b[0], b[1]), cvtpk(b[2], b[3])}; }
DEV f32x4 mfma32(u32x4 a, u32x4 b, f32x4 c) {
  return __builtin_amdgcn_mfma_f32_16x16x32_bf16(__builtin_bit_cast(bf16x8, a), __builtin_bit_cast(bf16x8, b), c, 0, 0, 0);
}
DEV f32x16 mfma3232(u32x4 a, u32x4 b, f32x16 c) {
  return __builtin_amdgcn_mfma_f32_32x32x16_bf16(__builtin_bit_cast(bf16x8, a), __builtin_bit_cast(bf16x8, b), c, 0, 0, 0);
}
DEV unsigned f2u(float x) { return __builtin_bit_cast(unsigned, x); }
DEV float u2f(unsigned x) { return __builtin_bit_cast(float, x); }
DEV f32x4 mfma16(u32x2 a, u32x2 b, f32x4 c) {
  return __builtin_amdgcn_mfma_f32_16x16x16bf16_1k(__builtin_bit_cast(s16x4, a), __builtin_bit_cast(s16x4, b), c, 0, 0, 0);
}
DEV f32x4 mfma4(u32x2 a, u32x2 b, f32x4 c) {
  return __builtin_amdgcn_mfma_f32_4x4x4bf16_1k(__builtin_bit_cast(s16x4, a), __builtin_bit_cast(s16x4, b), c, 0, 0, 0);
}
DEV u32x2 trread(lds_char* p) {
  return __builtin_bit_cast(u32x2, __builtin_amdgcn_ds_read_tr16_b64_v4i16((lds_s16x4*)p));
}
template <int CTRL, int BANK> DEV unsigned dppu(unsigned old, unsigned src) {
  return (unsigned)__builtin_amdgcn_update_dpp((int)old, (int)src, CTRL, 0xf, BANK, false);
}
template <int CTRL, int BANK> DEV float dppf(float old, float src) {
  return __builtin_bit_cast(float, dppu<CTRL, BANK>(__builtin_bit_cast(unsigned, old), __builtin_bit_cast(unsigned, src)));
}
constexpr int DPP_ID = 0xE4, DPP_ROR8 = 0x128;
DEV float xg_sum(float x) {
  auto s = __builtin_amdgcn_permlane16_swap(f2u(x), f2u(x), false, false);
  x = u2f(s[0]) + u2f(s[1]);
  auto t = __builtin_amdgcn_permlane32_swap(f2u(x), f2u(x), false, false);
  return u2f(t[0]) + u2f(t[1]);
}
DEV float xg_max(float x) {
  auto s = __builtin_amdgcn_permlane16_swap(f2u(x), f2u(x), false, false);
  x = __builtin_fmaxf(u2f(s[0]), u2f(s[1]));
  auto t = __builtin_amdgcn_permlane32_swap(f2u(x), f2u(x), false, false);
  return __builtin_fmaxf(u2f(t[0]), u2f(t[1]));
}
DEV int laneid() {
  int l;
  asm volatile("v_mbcnt_lo_u32_b32 %0, -1, 0\n\tv_mbcnt_hi_u32_b32 %0, -1, %0" : "=v"(l));
  __builtin_assume(l >= 0 && l < 64);
  return l;
}
DEV void bar() { SB(); __builtin_amdgcn_s_waitcnt(0xC07F); asm volatile("s_barrier" ::: "memory"); SB(); }
typedef __amdgpu_buffer_rsrc_t rsrc_t;
DEV rsrc_t mkrsrc(const void* p, unsigned bytes) { return __builtin_amdgcn_make_buffer_rsrc((void*)p, (short)0, (int)bytes, 0x00020000); }
template <int AUX> DEV u32x4 bld4(rsrc_t r, unsigned voff, unsigned soff) { return __builtin_amdgcn_raw_buffer_load_b128(r, voff, soff, AUX); }
template <int AUX> DEV u32x2 bld2(rsrc_t r, unsigned voff, unsigned soff) { return __builtin_amdgcn_raw_buffer_load_b64(r, voff, soff, AUX); }
constexpr int NT = 2;

DEV void dma16(rsrc_t r, unsigned voff, unsigned soff, unsigned ldsdst) {
  asm volatile("s_mov_b32 m0, %0\n\ts_nop 0\n\tbuffer_load_dwordx4 %1, %2, %3 offen lds" :: "s"(ldsdst), "v"(voff), "s"(r), "s"(soff) : "memory");
}
DEV void dma24(rsrc_t r, unsigned v0, unsigned v1, unsigned soff, unsigned ldsdst) {
  asm volatile("s_mov_b32 m0, %0\n\ts_nop 0\n\tbuffer_load_dwordx4 %1, %3, %4 offen nt lds\n\t"
               "s_mov_b32 exec_hi, 0\n\ts_add_u32 m0, m0, 0x400\n\ts_nop 0\n\tbuffer_load_dwordx4 %2, %3, %4 offen nt lds\n\ts_mov_b32 exec_hi, -1"
               :: "s"(ldsdst), "v"(v0), "v"(v1), "s"(r), "s"(soff) : "memory", "scc");
}
template <int OFF> DEV void ldnt(u32x4& d, rsrc_t r, unsigned voff, unsigned soff) {
  asm volatile("buffer_load_dwordx4 %0, %1, %2, %3 offen offset:%4 nt" : "=v"(d) : "v"(voff), "s"(r), "s"(soff), "n"(OFF) : "memory");
}
template <int OFF> DEV void ldpl(u32x4& d, rsrc_t r, unsigned voff) {
  asm volatile("buffer_load_dwordx4 %0, %1, %2, 0 offen offset:%3" : "=v"(d) : "v"(voff), "s"(r), "n"(OFF) : "memory");
}
template <int AUX> DEV void bst4(u32x4 v, rsrc_t r, unsigned voff, unsigned soff) { __builtin_amdgcn_raw_buffer_store_b128(v, r, voff, soff, AUX); }
template <int N> DEV void vmwait() { asm volatile("s_waitcnt vmcnt(%0)" :: "n"(N) : "memory"); }
DEV unsigned epoch(const void* ws) {
  asm volatile("" : "+s"(ws));
  return *(const __attribute__((address_space(4))) unsigned*)ws + KCHK;
}
DEV f32x2 pkmul(f32x2 a, f32x2 b) { return a * b; }
DEV f32x2 pkadd(f32x2 a, f32x2 b) { return a + b; }
DEV f32x2 pkfma(f32x2 a, f32x2 b, f32x2 c) { return __builtin_elementwise_fma(a, b, c); }
DEV float dot2(unsigned a, unsigned b, float c) {
  return __builtin_amdgcn_fdot2_f32_bf16(__builtin_bit_cast(bf16x2, a), __builtin_bit_cast(bf16x2, b), c, false);
}
template <int CTRL> DEV float dppadd(float x) {
  return cut(x + __builtin_bit_cast(float, __builtin_amdgcn_update_dpp(0, __builtin_bit_cast(int, x), CTRL, 0xf, 0xf, true)));
}

#define BAR() bar()
#define TSX(k)
#define RTX(k)
#define VALX(k, x)
#define TSC(k)
#define TSSAVE()
#define BARARGS
#define BARPASS
#define TSINIT()
#define TSFIN()

#define PRB0() unsigned long long prb0_, prbk_, prbs_; asm volatile("s_memrealtime %0\n\ts_waitcnt lgkmcnt(0)" : "=s"(prb0_) :: "memory"); asm volatile(PROBE_CLK " %0\n\ts_waitcnt lgkmcnt(0)" : "=s"(prbs_) :: "memory"); prbk_ = prbs_
#define PRB(k) do { if (PROBE == k) { SB(); asm volatile(PROBE_CLK " %0\n\ts_waitcnt lgkmcnt(0)" : "=s"(prbk_) :: "memory"); SB(); } if (PROBE_S == k) { SB(); asm volatile(PROBE_CLK " %0\n\ts_waitcnt lgkmcnt(0)" : "=s"(prbs_) :: "memory"); SB(); } } while (0)
#define PRBEND() do { if (b == PROBE_B && w == 0) { const unsigned lim_ = 6000u + (unsigned)(prbk_ - prbs_) * PROBE_MUL / PROBE_DIV; for (int it_ = 0; it_ < 100000; ++it_) { unsigned long long t_; asm volatile("s_sleep 1\n\ts_memrealtime %0\n\ts_waitcnt lgkmcnt(0)" : "=s"(t_) :: "memory"); if ((unsigned)(t_ - prb0_) >= lim_) break; } } } while (0)

struct Prod {
  u32x4 W[18];
  u32x4 ring[4][6];
};
struct ProdC {
  lds_char* smem;
  unsigned xw[2], rw, br[2], lw[2], lr[2], vd[2], voH, vw, lds0, ldsv;
  u32x2 I4;
  bool rvalid, odd;
  rsrc_t rhi, rlo, rwv;
};
DEV void lat_read(u32x4 (&B)[6], const ProdC& C, int t, int k0, int k1) {
#pragma unroll
  for (int ks = k0; ks < k1; ++ks) B[ks] = *(lds_u32x4*)(C.smem + C.br[ks & 1] + (t & 3) * 6144 + (ks >> 1) * 128);
}
DEV void lat_mm(const Prod& P, const u32x4 (&B)[6], const ProdC& C, int t) {
  const f32x4 z4 = {0.f, 0.f, 0.f, 0.f};
  f32x4 T[3];
#pragma unroll
  for (int ks = 0; ks < 6; ++ks)
#pragma unroll
    for (int tt = 0; tt < 3; ++tt) {
      T[tt] = mfma32(P.W[tt * 6 + ks], B[ks], ks == 0 ? z4 : T[tt]);
      OPQ(T[tt]);
      SB();
    }
#pragma unroll
  for (int tt = 0; tt < 3; ++tt) *(lds_f32x4*)(C.smem + C.lw[tt & 1] + (t & 1) * 12288 + (tt >> 1) * 128) = T[tt];
}
template <int M> DEV void ld_hi1(u32x4& d, const ProdC& C, unsigned so0, unsigned so1) { ldnt<(M >> 1) * 128>(d, C.rhi, C.voH, (M & 1) ? so1 : so0); }
DEV void ld_hi(u32x4 (&dst)[6], const ProdC& C, unsigned so) {
  const unsigned so1 = so + 196608;
  ld_hi1<0>(dst[0], C, so, so1); ld_hi1<1>(dst[1], C, so, so1); ld_hi1<2>(dst[2], C, so, so1);
  ld_hi1<3>(dst[3], C, so, so1); ld_hi1<4>(dst[4], C, so, so1); ld_hi1<5>(dst[5], C, so, so1);
}
template <int M, bool LOAD, bool LAT, int WAIT>
DEV void psub(Prod& P, const ProdC& C, int t, unsigned so, unsigned so1, f32x4 (&d)[6][2], f32x4 (&lat)[3][2], u32x4 (&B)[6],
              float (&ss)[2], float (&sm)[2]) {
  if constexpr (M < 6) {
    if (WAIT >= 0) vmwait<(WAIT >= 0 ? WAIT - M : 0)>();
    OPQ(P.ring[t & 3][M]);
    const u32x4 F = P.ring[t & 3][M];
    d[M][0] = mfma4(C.I4, u32x2{F[0], F[1]}, lat[M >> 1][0]);
    d[M][1] = mfma4(C.I4, u32x2{F[2], F[3]}, lat[M >> 1][1]);
    OPQ(d[M][0]); OPQ(d[M][1]);
    SB();
    if (LOAD) ld_hi1<M>(P.ring[t & 3][M], C, so, so1);
    if (M == 1) {
#pragma unroll
      for (int u = 0; u < 2; ++u) lat[2][u] = *(lds_f32x4*)(C.smem + C.lr[u] + (t & 1) * 12288 + 512);
    }
    if (LAT && M >= 3) lat_read(B, C, t + 1, 2 * (M - 3), 2 * (M - 3) + 2);
  }
  if constexpr (M > 0) {
    constexpr int k = M - 1, ab = k & 1, L = k >> 1;
    const u32x4 x = cvt8(d[k][0], d[k][1]);
    *(lds_u32x4*)(C.smem + C.xw[ab] + (t & 1) * 24576 + L * 8192) = x;
#pragma unroll
    for (int r = 0; r < 4; ++r) { ss[ab] = dot2(x[r], x[r], ss[ab]); sm[ab] = dot2(x[r], 0x3F803F80u, sm[ab]); }
  }
  SB();
}
template <bool LOAD, bool LAT, int NWV, int WAIT> DEV void pstep(Prod& P, const ProdC& C, int t, unsigned so, unsigned sl, unsigned wv BARARGS) {
  TSC(0);
  float ss[2] = {0.f, 0.f}, sm[2] = {0.f, 0.f};
  f32x4 d[6][2], lat[3][2];
  u32x4 B[6];
#pragma unroll
  for (int L = 0; L < 2; ++L)
#pragma unroll
    for (int u = 0; u < 2; ++u) lat[L][u] = *(lds_f32x4*)(C.smem + C.lr[u] + (t & 1) * 12288 + L * 256);
#pragma unroll
  for (int n = 0; n < NWV; ++n) dma16(C.rwv, C.vw, wv + n * 4096, C.ldsv + wv + n * 4096);
  if (LOAD) dma24(C.rlo, C.vd[0], C.vd[1], sl, C.lds0 + (t & 3) * 6144);
  const unsigned so1 = so + 196608;
  SB();
  TSC(1);
  psub<0, LOAD, LAT, WAIT>(P, C, t, so, so1, d, lat, B, ss, sm);
  psub<1, LOAD, LAT, WAIT>(P, C, t, so, so1, d, lat, B, ss, sm);
  TSC(2);
  psub<2, LOAD, LAT, WAIT>(P, C, t, so, so1, d, lat, B, ss, sm);
  psub<3, LOAD, LAT, WAIT>(P, C, t, so, so1, d, lat, B, ss, sm);
  TSC(3);
  psub<4, LOAD, LAT, WAIT>(P, C, t, so, so1, d, lat, B, ss, sm);
  psub<5, LOAD, LAT, WAIT>(P, C, t, so, so1, d, lat, B, ss, sm);
  psub<6, LOAD, LAT, WAIT>(P, C, t, so, so1, d, lat, B, ss, sm);
  TSC(4);
#pragma unroll
  for (int ab = 0; ab < 2; ++ab) {
    ss[ab] = dppadd<0x141>(dppadd<0x4E>(dppadd<0xB1>(ss[ab])));
    sm[ab] = dppadd<0x141>(dppadd<0x4E>(dppadd<0xB1>(sm[ab])));
  }
  const float mu = cut((C.odd ? sm[1] : sm[0]) * (1.0f / 192.0f));
  const float var = __builtin_fmaxf((C.odd ? ss[1] : ss[0]) * (1.0f / 192.0f) - mu * mu, 0.0f);
  const float rs = __builtin_amdgcn_rsqf(var + 1e-5f);
  if (C.rvalid) *(lds_f32*)(C.smem + C.rw + (t & 1) * 256) = rs;
  SB();
  TSC(5);
  if (LAT) lat_mm(P, B, C, t + 1);
  TSC(6);
}

struct Cons {
  f32x16 U[6];
  u32x4 Ab[12];
  float mrun, lsum;
};
struct ConsC { lds_char* smem; unsigned xr[4], xt[2][2], ra; };
DEV u32x4 row_read(const ConsC& C, int s) { return *(lds_u32x4*)(C.smem + C.xr[s & 3] + (s >> 2) * 8192); }
DEV u32x4 col_read(const ConsC& C, int o) {
  const int ks = o / 6, ct = o % 6;
  const u32x2 a0 = trread(C.smem + C.xt[ct & 1][0] + (ct >> 1) * 8192 + ks * 2048);
  const u32x2 a1 = trread(C.smem + C.xt[ct & 1][1] + (ct >> 1) * 8192 + ks * 2048);
  return u32x4{a0[0], a0[1], a1[0], a1[1]};
}
DEV void cstep(Cons& K, ConsC& C, int dx, int dr, bool first BARARGS) {
  TSC(0);
  u32x4 X[12];
  f32x4 rr[4];
#pragma unroll
  for (int s = 0; s < 12; ++s) X[s] = row_read(C, s);
#pragma unroll
  for (int a = 0; a < 4; ++a) rr[a] = *(lds_f32x4*)(C.smem + C.ra + a * 32);
  SB();
  f32x16 S = {0.f, 0.f, 0.f, 0.f, 0.f, 0.f, 0.f, 0.f, 0.f, 0.f, 0.f, 0.f, 0.f, 0.f, 0.f, 0.f};
#pragma unroll
  for (int s = 0; s < 12; ++s) {
    S = mfma3232(X[s], K.Ab[s], S);
    OPQ(S);
    SB();
    if (s == 0) TSC(1);
    if (s == 11) TSC(2);
    X[s] = col_read(C, s);
    SB();
  }
#pragma unroll
  for (int s = 0; s < 4; ++s) { C.xr[s] += dx; C.xt[s >> 1][s & 1] += dx; OPQ(C.xr[s]); OPQ(C.xt[s >> 1][s & 1]); }
  C.ra += dr; OPQ(C.ra);
  SB();
  float V[16];
  float vmax = -3e38f;
#pragma unroll
  for (int e = 0; e < 16; ++e) { V[e] = cut(S[e] * rr[e >> 2][e & 3] - K.mrun); vmax = __builtin_fmaxf(vmax, V[e]); }
  if (first || __builtin_amdgcn_ballot_w64(!(vmax <= THR)) != 0) {
    auto t = __builtin_amdgcn_permlane32_swap(f2u(vmax), f2u(vmax), false, false);
    float dl = __builtin_fmaxf(u2f(t[0]), u2f(t[1]));
    if (!first) dl = __builtin_fmaxf(dl, 0.f);
    const float al = __builtin_amdgcn_exp2f(__builtin_fminf(-dl, 0.f));
    K.mrun += dl; K.lsum *= al;
#pragma unroll
    for (int c = 0; c < 6; ++c) K.U[c] *= al;
#pragma unroll
    for (int e = 0; e < 16; ++e) V[e] -= dl;
  }
  TSC(3);
  u32x4 Pb[2];
  float pe[16];
#pragma unroll
  for (int e = 0; e < 8; ++e) pe[e] = __builtin_amdgcn_exp2f(V[e]);
#pragma unroll
  for (int e = 0; e < 8; e += 2) Pb[0][e >> 1] = cvtpk(cut(pe[e] * rr[e >> 2][e & 3]), cut(pe[e + 1] * rr[e >> 2][(e & 3) + 1]));
  SB();
  TSC(4);
#pragma unroll
  for (int ct = 0; ct < 6; ++ct) {
    K.U[ct] = mfma3232(X[ct], Pb[0], K.U[ct]);
    OPQ(K.U[ct]);
    SB();
    if (ct < 4) {
      const int e = 8 + 2 * ct;
      pe[e] = __builtin_amdgcn_exp2f(V[e]); pe[e + 1] = __builtin_amdgcn_exp2f(V[e + 1]);
      Pb[1][ct] = cvtpk(cut(pe[e] * rr[e >> 2][e & 3]), cut(pe[e + 1] * rr[e >> 2][(e & 3) + 1]));
    } else {
      const int e = 4 * (ct - 4);
      K.lsum += cut(pe[e] + pe[e + 1]) + cut(pe[e + 2] + pe[e + 3]);
    }
    SB();
  }
  TSC(5);
#pragma unroll
  for (int ct = 0; ct < 6; ++ct) {
    K.U[ct] = mfma3232(X[6 + ct], Pb[1], K.U[ct]);
    OPQ(K.U[ct]);
    SB();
    if (ct < 2) { const int e = 8 + 4 * ct; K.lsum += cut(pe[e] + pe[e + 1]) + cut(pe[e + 2] + pe[e + 3]); }
    SB();
  }
  TSC(6);
}
DEV void cfinish(Cons& K, lds_char* smem, int h, int b, rsrc_t rws, unsigned ek BARARGS) {
  const int lane = laneid(), i = lane & 15, g = lane >> 4;
  const int xo = (h >> 1) ? LDS_LAT : LDS_X, PH = h & 1;
  f32x4 Uo[12];
  lds_char* ex = smem + xo + PH * 12288 + lane * 16;
#pragma unroll
  for (int ct = 0; ct < 6; ++ct)
#pragma unroll
    for (int a = 0; a < 2; ++a) {
      f32x4 k0, k1;
#pragma unroll
      for (int r = 0; r < 4; ++r) {
        auto sw = __builtin_amdgcn_permlane16_swap(f2u(K.U[ct][8 * a + r]), f2u(K.U[ct][8 * a + 4 + r]), false, false);
        k0[r] = u2f(sw[0]); k1[r] = u2f(sw[1]);
      }
      Uo[2 * ct + a] = k0;
      *(lds_f32x4*)(ex + (2 * ct + a) * 1024) = k1;
      SB();
    }
  TSX(51);
  float mo, lo_;
  {
    auto t = __builtin_amdgcn_permlane32_swap(f2u(K.lsum), f2u(K.lsum), false, false);
    const float l = u2f(t[0]) + u2f(t[1]);
    auto sl = __builtin_amdgcn_permlane16_swap(f2u(l), f2u(l), false, false);
    auto sm = __builtin_amdgcn_permlane16_swap(f2u(K.mrun), f2u(K.mrun), false, false);
    mo = u2f(sm[0]); lo_ = u2f(sl[0]);
    *(__attribute__((address_space(3))) f32x2*)(smem + LDS_ML + h * 512 + lane * 8) = f32x2{u2f(sm[1]), u2f(sl[1])};
  }
  BAR();
  const f32x2 ml = *(__attribute__((address_space(3))) f32x2*)(smem + LDS_ML + (h ^ 1) * 512 + lane * 8);
  lds_char* ey = smem + xo + (1 - PH) * 12288 + lane * 16;
  f32x4 o[12];
#pragma unroll
  for (int c = 0; c < 12; ++c) o[c] = *(lds_f32x4*)(ey + c * 1024);
  lds_char* wv = smem + LDS_WV + h * 18432 + (8 * (g & 1) + 4 * (g >> 1) + (i >> 2)) * 96 + (i & 3) * 8;
  u32x4 Wv[18];
#pragma unroll
  for (int n = 0; n < 18; ++n) {
    const u32x2 a0 = trread(wv + (n / 3) * 3072 + (n % 3) * 32), a1 = trread(wv + (n / 3) * 3072 + 1536 + (n % 3) * 32);
    Wv[n] = u32x4{a0[0], a0[1], a1[0], a1[1]};
  }
  SB();
  const float mn = __builtin_fmaxf(mo, ml[0]);
  const float fa = __builtin_amdgcn_exp2f(mo - mn), fb = __builtin_amdgcn_exp2f(ml[0] - mn);
  const float l = lo_ * fa + ml[1] * fb, il = __builtin_amdgcn_rcpf(l);
  const unsigned lse = (unsigned)__builtin_amdgcn_ds_bpermute((4 * g + (i & 3)) * 4, (int)f2u(mn + __builtin_amdgcn_logf(l)));
  const f32x2 fa2 = {fa * il, fa * il}, fb2 = {fb * il, fb * il};
  f32x2 u2[24], us[2];
#pragma unroll
  for (int c = 0; c < 24; ++c) {
    const f32x2 own = {Uo[c >> 1][(c & 1) * 2], Uo[c >> 1][(c & 1) * 2 + 1]}, oth = {o[c >> 1][(c & 1) * 2], o[c >> 1][(c & 1) * 2 + 1]};
    u2[c] = pkfma(oth, fb2, pkmul(own, fa2));
    us[c & 1] = c < 2 ? u2[c] : pkadd(us[c & 1], u2[c]);
  }
  TSX(52);
  const float um = xg_sum((us[0][0] + us[0][1]) + (us[1][0] + us[1][1])) * (-1.0f / 192.0f);
  const f32x2 um2 = {um, um};
  const f32x4 z4 = {0.f, 0.f, 0.f, 0.f};
  f32x4 cx[3] = {z4, z4, z4};
#pragma unroll
  for (int s = 0; s < 6; ++s) {
    u32x4 ub;
#pragma unroll
    for (int r = 0; r < 4; ++r) { const f32x2 v = pkadd(u2[4 * s + r], um2); ub[r] = cvtpk(v[0], v[1]); }
#pragma unroll
    for (int dt = 0; dt < 3; ++dt) cx[dt] = mfma32(ub, Wv[3 * s + dt], cx[dt]);
  }
  TSX(53);
  const u32x4 ra = cvt8(cx[0], cx[1]);
  u32x4 rb = {cvtpk(cx[2][0], cx[2][1]), cvtpk(cx[2][2], cx[2][3]), lse, 0u};
  rb[3] = (ra[0] + ra[1] + ra[2]) + (ra[3] + rb[0] + rb[1]) + lse + ek;
  const unsigned vo = (lane >> 2) * 32768 + (lane & 3) * 16, so = WS_REC + h * 524288 + b * 64;
  bst4<SC1>(ra, rws, vo, so);
  bst4<SC1>(rb, rws, vo, so + 16384);
}

struct AIn { u32x4 q[6]; };
DEV void a_issue(AIn& I, unsigned lbase, int h, int cs, const void* q_, const void* wq_, const void* wk_, const void* wm_) {
  const int lane = laneid();
  const rsrc_t rq = mkrsrc(q_, 6144u), rwq = mkrsrc(wq_, 73728u), rwk = mkrsrc(wk_, 73728u), rwm = mkrsrc(wm_, 73728u);
  const unsigned vl = lane * 16;
#pragma unroll
  for (int n = 0; n < 18; ++n) dma16(rwq, vl, h * 18432 + n * 1024, lbase + LDS_AWQ + n * 1024);
  unsigned vo[3];
#pragma unroll
  for (int m = 0; m < 3; ++m) { const unsigned o = m * 1024 + vl; vo[m] = (o / 96) * 384 + o % 96; }
#pragma unroll
  for (int n = 0; n < 18; ++n) dma16(rwm, vo[n % 3], (n / 3) * 12288 + cs * 96, lbase + LDS_AWM + n * 1024);
#pragma unroll
  for (int n = 0; n < 5; ++n) dma16(rwk, vl, h * 18432 + cs * 4608 + n * 1024, lbase + LDS_AWK + n * 1024);
  const unsigned vq = (lane & 15) * 384 + (lane >> 4) * 16;
  ldpl<0>(I.q[0], rq, vq); ldpl<64>(I.q[1], rq, vq); ldpl<128>(I.q[2], rq, vq);
  ldpl<192>(I.q[3], rq, vq); ldpl<256>(I.q[4], rq, vq); ldpl<320>(I.q[5], rq, vq);
}
DEV void a_compute(AIn& I, lds_char* smem, int h, int cs, rsrc_t rws, unsigned ek) {
  const int lane = laneid(), i = lane & 15, g = lane >> 4;
  vmwait<0>();
#pragma unroll
  for (int s = 0; s < 6; ++s) OPQ(I.q[s]);
  SB();
  lds_char* a = smem + (8 * g + (i >> 2)) * 96 + (i & 3) * 8;
  const f32x4 z4 = {0.f, 0.f, 0.f, 0.f};
  f32x4 Q[3], M[3], T[3];
  u32x4 W[18], V[18];
  u32x2 Kf[9];
#pragma unroll
  for (int n = 0; n < 18; ++n) {
    const u32x2 a0 = trread(a + LDS_AWQ + (n / 3) * 3072 + (n % 3) * 32), a1 = trread(a + LDS_AWQ + (n / 3) * 3072 + 384 + (n % 3) * 32);
    W[n] = u32x4{a0[0], a0[1], a1[0], a1[1]};
  }
#pragma unroll
  for (int n = 0; n < 18; ++n) {
    const u32x2 a0 = trread(a + LDS_AWM + (n / 3) * 3072 + (n % 3) * 32), a1 = trread(a + LDS_AWM + (n / 3) * 3072 + 384 + (n % 3) * 32);
    V[n] = u32x4{a0[0], a0[1], a1[0], a1[1]};
  }
#pragma unroll
  for (int n = 0; n < 9; ++n) Kf[n] = *(__attribute__((address_space(3))) u32x2*)(smem + LDS_AWK + i * 96 + g * 8 + (n / 3) * 1536 + (n % 3) * 32);
  SB();
#pragma unroll
  for (int n = 0; n < 18; ++n) Q[n % 3] = mfma32(W[n], I.q[n / 3], n < 3 ? z4 : Q[n % 3]);
#pragma unroll
  for (int n = 0; n < 18; ++n) M[n % 3] = mfma32(V[n], I.q[n / 3], n < 3 ? z4 : M[n % 3]);
#pragma unroll
  for (int dt = 0; dt < 3; ++dt) {
    const u32x2 qb = {cvtpk(Q[dt][0], Q[dt][1]), cvtpk(Q[dt][2], Q[dt][3])};
#pragma unroll
    for (int ct = 0; ct < 3; ++ct) T[ct] = mfma16(Kf[3 * ct + dt], qb, dt == 0 ? z4 : T[ct]);
  }
  const float s1 = 1.4426950408889634f * 0.14433756729740643f;
  unsigned P[6];
#pragma unroll
  for (int n = 0; n < 6; ++n)
    P[n] = cvtpk((T[n >> 1][(n & 1) * 2] + 0.5f * M[n >> 1][(n & 1) * 2]) * s1, (T[n >> 1][(n & 1) * 2 + 1] + 0.5f * M[n >> 1][(n & 1) * 2 + 1]) * s1);
  const unsigned so = WS_A + h * 8192 + cs * 2048;
  bst4<SC1>(u32x4{P[0], P[1], P[2], P[0] + P[1] + P[2] + ek}, rws, lane * 16, so);
  bst4<SC1>(u32x4{P[4], P[5], P[3], P[4] + P[5] + P[3] + ek}, rws, lane * 16 + 1024, so);
}
DEV void a_poll(u32x4 (&G)[8], int h, rsrc_t rws, unsigned ek BARARGS) {
  const int lane = laneid();
  RTX(44);
  for (int spin = 0;; ++spin) {
    VALX(46, spin);
    asm volatile("" ::: "memory");
#pragma unroll
    for (int n = 0; n < 8; ++n) G[n] = bld4<SC1>(rws, lane * 16 + (n & 3) * 1024, WS_A + h * 8192 + (n >> 2) * 4096);
    bool bad = false;
#pragma unroll
    for (int n = 0; n < 8; ++n) bad |= G[n][0] + G[n][1] + G[n][2] + ek != G[n][3];
    if (__builtin_amdgcn_ballot_w64(bad) == 0 || spin >= SPIN_MAX) break;
  }
  RTX(45);
}
DEV void a_centre(const u32x4 (&G)[8], lds_char* smem, int h) {
  const int lane = laneid(), i = lane & 15, g = lane >> 4;
  float sm[2] = {0.f, 0.f};
#pragma unroll
  for (int n = 0; n < 24; ++n) sm[n & 1] = dot2(G[n / 3][n % 3], 0x3F803F80u, sm[n & 1]);
  const float mu = xg_sum(sm[0] + sm[1]) * (-1.0f / 192.0f);
  const f32x4 mu4 = {mu, mu, mu, mu};
  const int d = lane & 3;
  const u32x2 I4 = {d == 0 ? 0x00003F80u : (d == 1 ? 0x3F800000u : 0u), d == 2 ? 0x00003F80u : (d == 3 ? 0x3F800000u : 0u)};
  lds_char* dst = smem + LDS_AF + (h >> 1) * 12288 + (g >> 1) * 512 + ((h & 1) * 16 + i) * 16 + (g & 1) * 8;
#pragma unroll
  for (int s = 0; s < 12; ++s) {
    const int cs = s / 3, ct = s % 3;
    const u32x2 v = ct == 0 ? u32x2{G[2 * cs][0], G[2 * cs][1]} : (ct == 1 ? u32x2{G[2 * cs][2], G[2 * cs + 1][2]} : u32x2{G[2 * cs + 1][0], G[2 * cs + 1][1]});
    const f32x4 c = mfma4(I4, v, mu4);
    *(__attribute__((address_space(3))) u32x2*)(dst + s * 1024) = u32x2{cvtpk(c[0], c[1]), cvtpk(c[2], c[3])};
  }
}
template <int CTRL> DEV float dppmov(float x) { return __builtin_bit_cast(float, __builtin_amdgcn_update_dpp(0, __builtin_bit_cast(int, x), CTRL, 0xf, 0xf, true)); }
DEV float quads_sum(float x) { x += dppmov<0x124>(x); return x + dppmov<0x128>(x); }
DEV float quads_max(float x) { x = __builtin_fmaxf(x, dppmov<0x124>(x)); return __builtin_fmaxf(x, dppmov<0x128>(x)); }
DEV void reduce1(lds_char* smem, int r, int p, rsrc_t rws, unsigned ek BARARGS) {
  const int lane = laneid(), i = lane & 15, g = lane >> 4, d = lane & 3;
  const unsigned m0 = d == 0 ? 0x0000FFFFu : (d == 1 ? 0xFFFF0000u : 0u), m1 = d == 2 ? 0x0000FFFFu : (d == 3 ? 0xFFFF0000u : 0u);
  u32x4 RA[4], RB[4];
  const unsigned so = WS_REC + r * 32768 + p * 4096;
  RTX(44);
  for (int spin = 0;; ++spin) {
    VALX(46, spin);
    asm volatile("" ::: "memory");
#pragma unroll
    for (int j = 0; j < 4; ++j) { RB[j] = bld4<SC1>(rws, lane * 16 + j * 1024, so + 16384); RA[j] = bld4<SC1>(rws, lane * 16 + j * 1024, so); }
    bool bad = false;
#pragma unroll
    for (int j = 0; j < 4; ++j) bad |= (RA[j][0] + RA[j][1] + RA[j][2]) + (RA[j][3] + RB[j][0] + RB[j][1]) + RB[j][2] + ek != RB[j][3];
    if (__builtin_amdgcn_ballot_w64(bad) == 0 || spin >= SPIN_MAX) break;
  }
  RTX(45);
  const float M = xg_max(quads_max(__builtin_fmaxf(__builtin_fmaxf(u2f(RB[0][2]), u2f(RB[1][2])), __builtin_fmaxf(u2f(RB[2][2]), u2f(RB[3][2])))));
  const f32x4 z4 = {0.f, 0.f, 0.f, 0.f};
  f32x4 acc[3] = {z4, z4, z4};
  float L = 0.f;
#pragma unroll
  for (int j = 0; j < 4; ++j) {
    const float wf = __builtin_amdgcn_exp2f(u2f(RB[j][2]) - M);
    const unsigned wb = cvtpk(wf, wf);
    L += u2f(wb & 0xFFFF0000u);
    const u32x2 dg = {wb & m0, wb & m1};
    acc[0] = mfma4(dg, u32x2{RA[j][0], RA[j][1]}, acc[0]);
    acc[1] = mfma4(dg, u32x2{RA[j][2], RA[j][3]}, acc[1]);
    acc[2] = mfma4(dg, u32x2{RB[j][0], RB[j][1]}, acc[2]);
  }
  L = xg_sum(quads_sum(L));
  float u[6];
  f32x4 v;
#pragma unroll
  for (int k = 0; k < 6; ++k) {
    auto t = __builtin_amdgcn_permlane32_swap(f2u(acc[k % 3][k / 3]), f2u(acc[k % 3][2 + k / 3]), false, false);
    u[k] = u2f(t[0]) + u2f(t[1]);
  }
#pragma unroll
  for (int k = 0; k < 3; ++k) {
    auto t = __builtin_amdgcn_permlane16_swap(f2u(u[k]), f2u(u[k + 3]), false, false);
    v[k] = quads_sum(u2f(t[0]) + u2f(t[1]));
  }
  v[3] = 0.f;
  if (i < 4) *(lds_f32x4*)(smem + LDS_RED + p * 256 + g * 64 + d * 16) = v;
  if (lane < 4) *(__attribute__((address_space(3))) f32x2*)(smem + LDS_RED + 1024 + p * 32 + d * 8) = f32x2{M, L};
  bar();
  if (p == 0) {
    const f32x4 x = *(lds_f32x4*)(smem + LDS_RED + (i >> 2) * 256 + g * 64 + d * 16);
    const f32x2 ml = *(__attribute__((address_space(3))) f32x2*)(smem + LDS_RED + 1024 + (i >> 2) * 32 + g * 8);
    const float f = __builtin_amdgcn_exp2f(ml[0] - quads_max(ml[0]));
    const float il = __builtin_amdgcn_rcpf(quads_sum(ml[1] * f));
    u32x4 o;
#pragma unroll
    for (int k = 0; k < 3; ++k) { const float c = quads_sum(x[k] * f) * il; o[k] = cvtpk(c, dppmov<0xB1>(c)); }
    o[3] = o[0] + o[1] + o[2] + ek;
    if (i == 0 || i == 2) bst4<SC1>(o, rws, (d >> 1) * 64 + g * 16, WS_CTX + r * 128);
  }
}
DEV void reduce2_issue(u32x2& qr, unsigned lbase, int nt, const void* q_, const void* wo_) {
  const int lane = laneid();
  const rsrc_t rq = mkrsrc(q_, 6144u), rwo = mkrsrc(wo_, 73728u);
  const unsigned vo = (lane >> 1) * 384 + (lane & 1) * 16, vq = (lane & 15) * 384 + (lane >> 4) * 8;
#pragma unroll
  for (int n = 0; n < 6; ++n) dma16(rwo, vo, n * 12288 + nt * 32, lbase + LDS_WO + n * 1024);
  asm volatile("buffer_load_dwordx2 %0, %1, %2, %3 offen" : "=v"(qr) : "v"(vq), "s"(rq), "s"(nt * 32) : "memory");
}
DEV void reduce2(u32x2& qr, lds_char* smem, int nt, rsrc_t rws, unsigned ek, void* out_ BARARGS) {
  const int lane = laneid(), i = lane & 15, g = lane >> 4, rho = i >> 2;
  vmwait<0>();
  OPQ(qr);
  SB();
  lds_char* a0 = smem + LDS_WO + (16 * (rho >> 1) + 4 * g + (rho & 1)) * 32 + (i & 3) * 8;
  lds_char* a1 = smem + LDS_WO + (32 + 4 * g + (rho & 1)) * 32 + (i & 3) * 8;
  u32x4 W[8], G[8];
#pragma unroll
  for (int m = 0; m < 8; ++m) {
    const u32x2 x0 = trread(a0 + ((m >> 1) * 48 + 2 * (m & 1)) * 32), x1 = trread(a1 + ((m >> 1) * 48 + 2 * (m & 1)) * 32);
    W[m] = u32x4{x0[0], x0[1], x1[0], x1[1]};
    OPQ(W[m]);
  }
  const unsigned vo = (i >> 2) * 512 + g * 128 + (i & 3) * 16;
  RTX(44);
  for (int spin = 0;; ++spin) {
    VALX(46, spin);
    asm volatile("" ::: "memory");
#pragma unroll
    for (int m = 0; m < 8; ++m) G[m] = bld4<SC1>(rws, vo + (m & 1) * 64 + ((m >> 1) & 1) * 2048, WS_CTX + (m >> 2) * 4096);
    bool bad = false;
#pragma unroll
    for (int m = 0; m < 8; ++m) bad |= G[m][0] + G[m][1] + G[m][2] + ek != G[m][3];
    if (__builtin_amdgcn_ballot_w64(bad) == 0 || spin >= SPIN_MAX) break;
  }
  RTX(45);
  f32x4 c[2] = {{u2f(qr[0] << 16), u2f(qr[0] & 0xFFFF0000u), u2f(qr[1] << 16), u2f(qr[1] & 0xFFFF0000u)}, {0.f, 0.f, 0.f, 0.f}};
#pragma unroll
  for (int m = 0; m < 8; ++m) { G[m][3] = 0u; c[m & 1] = mfma32(W[m], G[m], c[m & 1]); }
  const f32x4 o = c[0] + c[1];
  __builtin_amdgcn_raw_buffer_store_b64(u32x2{cvtpk(o[0], o[1]), cvtpk(o[2], o[3])}, mkrsrc(out_, 6144u), i * 384 + g * 8, nt * 32, 0);
  if (nt == 0 && lane == 0) __builtin_amdgcn_raw_buffer_store_b32(ek - KCHK + 1u, rws, 0, 0, 0);
}

__global__ void __launch_bounds__(512, 2)
m2f_main(const void* __restrict__ fhi_, const void* __restrict__ flo_, const void* __restrict__ wlat_,
         const void* __restrict__ wv_, const void* __restrict__ q_, const void* __restrict__ wq_, const void* __restrict__ wk_,
         const void* __restrict__ wm_, const void* __restrict__ wo_, void* __restrict__ ws_, void* __restrict__ out_) {
  __shared__ __attribute__((aligned(16))) char smem_[LDS_TOTAL];
  lds_char* smem = (lds_char*)smem_;
  const int w = __builtin_amdgcn_readfirstlane(__builtin_amdgcn_workitem_id_x() >> 6);
  const int b = __builtin_amdgcn_workgroup_id_x();
  TSINIT();
  PRB0();

  if (w < 4) {
    const int p = w, lane = laneid(), i = lane & 15, g = lane >> 4;
#define PRIO_P 2
    __builtin_amdgcn_s_setprio(PRIO_P);
    if (b < 16) bar();
    const rsrc_t rwl = mkrsrc(wlat_, 73728u);
    ProdC C;
    C.smem = smem;
    C.rhi = mkrsrc(fhi_, 100663296u);
    C.rlo = mkrsrc(flo_, 25165824u);
    C.rwv = mkrsrc(wv_, 73728u);
    const unsigned j = lane >> 3, c7 = lane & 7;
    const unsigned lbase = (unsigned)(uintptr_t)smem;
    C.voH = j * 384 + c7 * 16;
    C.vw = lane * 16;
    C.lds0 = lbase + LDS_LO + p * 1536;
    C.ldsv = lbase + LDS_WV;
#pragma unroll
    for (int k = 0; k < 2; ++k) {
      const unsigned o = k * 1024 + (lane & (k ? 31 : 63)) * 16, px = o / 384, pc = (o % 384) / 16;
      C.vd[k] = px * 384 + ((pc ^ (2 * p + (px >> 1))) * 16);
    }
    const unsigned soL = (unsigned)(b * 256 + p * 4) * 384u;
    const unsigned soH = (unsigned)(b * 1024 + p * 8) * 384u;
    Prod P;
    dma24(C.rlo, C.vd[0], C.vd[1], soL, C.lds0);
    dma24(C.rlo, C.vd[0], C.vd[1], soL + 6144, C.lds0 + 6144);
    SB();
    {
      unsigned vo[6];
#pragma unroll
      for (int m = 0; m < 6; ++m) {
        const unsigned o = m * 1024 + lane * 16, k = o / 384, pc = (o % 384) / 16;
        vo[m] = k * 384 + ((pc ^ (2 * (((k >> 1) & 1) | ((k >> 3) << 1)))) * 16);
      }
      const unsigned so1 = soH + 196608, wo = p * 18432, wl = lbase + LDS_WL + p * 18432;
#define WGRP(M) ld_hi1<M>(P.ring[0][M], C, soH, so1); \
      for (int n = 3 * M; n < 3 * M + 3; ++n) dma16(rwl, vo[n % 6], wo + (n / 6) * 6144, wl + n * 1024);
      WGRP(0) WGRP(1) WGRP(2) WGRP(3) WGRP(4) WGRP(5)
#undef WGRP
    }
    SB();
    ld_hi(P.ring[1], C, soH + 12288);
    dma24(C.rlo, C.vd[0], C.vd[1], soL + 2 * 6144, C.lds0 + 2 * 6144);
    SB();
    {
      const int d = lane & 3;
      C.I4[0] = d == 0 ? 0x00003F80u : (d == 1 ? 0x3F800000u : 0u);
      C.I4[1] = d == 2 ? 0x00003F80u : (d == 3 ? 0x3F800000u : 0u);
#pragma unroll
      for (int ab = 0; ab < 2; ++ab) {
        C.xw[ab] = LDS_X + (16 * p + 8 * ab + j) * 128 + ((c7 ^ (((j >> 1) & 1) * 4 + 2 * ab + (j >> 2))) * 16);
        C.br[ab] = LDS_LO + i * 384 + (((4 * ab + g) ^ (i >> 1)) * 16);
        C.lw[ab] = LDS_LAT + i * 768 + (((12 * p + 4 * ab + g) ^ (i & 7)) * 16);
        C.lr[ab] = LDS_LAT + (4 * p + (j >> 1)) * 768 + (((2 * c7 + ab) ^ ((4 * p + (j >> 1)) & 7)) * 16);
      }
      C.rvalid = c7 < 2; C.odd = lane & 1;
      C.rw = LDS_R + (16 * p + 8 * (lane & 1) + j) * 4;
#pragma unroll
      for (int ab = 0; ab < 2; ++ab) { OPQ(C.xw[ab]); OPQ(C.br[ab]); OPQ(C.lw[ab]); OPQ(C.lr[ab]); OPQ(C.vd[ab]); }
      OPQ(C.rw); OPQ(C.vw);
    }
    SB();
    unsigned ek = epoch(ws_);
    SB();
    vmwait<8>();
    asm volatile("" : "+s"(ek));
    BAR();
    PRB(1);
    ld_hi(P.ring[2], C, soH + 2 * 12288);
    SB();
    {
      const unsigned rp = i >> 2, cp = i & 3, s = (rp >> 1) | ((g & 1) << 1);
      u32x4 B[6];
      lat_read(B, C, 0, 0, 6);
#pragma unroll
      for (int tt = 0; tt < 3; ++tt) {
        const unsigned ct = 3 * p + tt;
        lds_char* a = smem + LDS_WL + (8 * g + rp) * 384 + (((ct & 3) ^ s) * 32) + cp * 8 + (ct >> 2) * 128;
#pragma unroll
        for (int ks = 0; ks < 6; ++ks) {
          const u32x2 a0 = trread(a + ks * 12288), a1 = trread(a + ks * 12288 + 1536);
          P.W[tt * 6 + ks] = u32x4{a0[0], a0[1], a1[0], a1[1]};
        }
      }
      SB();
      lat_mm(P, B, C, 0);
    }
    BAR();
    PRB(2);
    dma24(C.rlo, C.vd[0], C.vd[1], soL + 3 * 6144, C.lds0 + 3 * 6144);
    ld_hi(P.ring[3], C, soH + 3 * 12288);
    SB();
#pragma unroll 1
    for (int u = 0; u < 3; ++u) {
      const unsigned so = soH + (u + 1) * 49152, sl = soL + (u + 1) * 24576, wv = p * 1024 + u * 20480;
      pstep<true, true, 1, -1>(P, C, 0, so, sl, wv BARPASS); vmwait<22>(); BAR(); if (u == 0) PRB(3);
      pstep<true, true, 1, -1>(P, C, 1, so + 12288, sl + 6144, wv + 4096 BARPASS); vmwait<22>(); BAR(); if (u == 0) PRB(4);
      pstep<true, true, 1, -1>(P, C, 2, so + 2 * 12288, sl + 2 * 6144, wv + 2 * 4096 BARPASS); vmwait<22>(); BAR();
      pstep<true, true, 2, -1>(P, C, 3, so + 3 * 12288, sl + 3 * 6144, wv + 3 * 4096 BARPASS); vmwait<22>(); BAR();
    }
    pstep<false, true, 0, -1>(P, C, 0, 0, 0, 0 BARPASS); vmwait<14>(); BAR();
    pstep<false, true, 0, -1>(P, C, 1, 0, 0, 0 BARPASS); vmwait<6>(); TSSAVE(); BAR();
    pstep<false, true, 3, -1>(P, C, 2, 0, 0, p * 1024 + 61440 BARPASS); BAR();
    pstep<false, false, 0, 8>(P, C, 3, 0, 0, 0 BARPASS); BAR();
    PRB(6);
    const bool l2 = p == 0 && b >= L2_B0 && b < L2_B0 + 12;
    u32x2 qr;
    if (l2) { reduce2_issue(qr, lbase, b - L2_B0, q_, wo_); vmwait<7>(); } else vmwait<0>();
    BAR();
    PRB(7);
    if (b >= L1_B0 && b < L1_B0 + 64) reduce1(smem, b - L1_B0, p, mkrsrc(ws_, WS_BYTES), ek BARPASS);
    if (l2) reduce2(qr, smem, b - L2_B0, mkrsrc(ws_, WS_BYTES), ek, out_ BARPASS);
    PRB(9);
    PRBEND();
  } else {
    const int h = w - 4, hp = h >> 1, ph = h & 1, lane = laneid(), i = lane & 15, g = lane >> 4;
    const rsrc_t rws = mkrsrc(ws_, WS_BYTES);
    Cons K;
#define PRIO_C 0
    __builtin_amdgcn_s_setprio(PRIO_C);
    unsigned ek;
    if (b < 16 && h == 0) {
      AIn I;
      a_issue(I, (unsigned)(uintptr_t)smem, b >> 2, b & 3, q_, wq_, wk_, wm_);
      ek = epoch(ws_);
      asm volatile("s_barrier" ::: "memory");
      RTX(47);
      a_compute(I, smem, b >> 2, b & 3, rws, ek);
      RTX(48);
    } else {
      if (b < 16) bar();
      ek = epoch(ws_);
    }
    asm volatile("" : "+s"(ek));
    u32x4 G[8];
    BAR();
    a_poll(G, h, rws, ek BARPASS);
    BAR();
    a_centre(G, smem, h);
    ConsC C;
    C.smem = smem;
    const unsigned hl = lane >> 5;
    {
      const unsigned m = lane & 31, row = 32 * ph + m, f = ((m >> 1) & 1) * 4 + ((m >> 2) & 3);
#pragma unroll
      for (int s = 0; s < 4; ++s) C.xr[s] = LDS_X + row * 128 + (((2 * s + hl) ^ f) * 16);
      const unsigned cb = g & 1, rp = i >> 2, cp = i & 3;
#pragma unroll
      for (int c = 0; c < 2; ++c)
#pragma unroll
        for (int sec = 0; sec < 2; ++sec)
          C.xt[c][sec] = LDS_X + (32 * ph + 8 * sec + 4 * hl + rp) * 128 + (((4 * c + 2 * cb + (cp >> 1)) ^ ((rp >> 1) * 4 + hl + 2 * sec)) * 16) + (cp & 1) * 8;
      C.ra = LDS_R + (32 * ph + 4 * hl) * 4;
#pragma unroll
      for (int s = 0; s < 4; ++s) { OPQ(C.xr[s]); OPQ(C.xt[s >> 1][s & 1]); }
      OPQ(C.ra);
    }
#pragma unroll
    for (int c = 0; c < 6; ++c)
#pragma unroll
      for (int e = 0; e < 16; ++e) K.U[c][e] = 0.f;
    K.mrun = 0.f; K.lsum = 0.f;
#pragma unroll 1
    for (int t = 0; t < 16; ++t) {
      BAR();
      if (t == 0) {
        lds_char* af = smem + LDS_AF + hp * 12288 + (laneid() ^ (16 * ph)) * 16;
#pragma unroll
        for (int s = 0; s < 12; ++s) K.Ab[s] = *(lds_u32x4*)(af + s * 1024);
      }
      cstep(K, C, (t & 1) ? -24576 : 24576, (t & 1) ? -256 : 256, t == 0 BARPASS);
    }
    TSX(50);
    cfinish(K, smem, h, b, rws, ek BARPASS);
  }
  TSFIN();
}

extern "C" void kernel_launch(void* const* d_in, const int* in_sizes, int n_in, void* d_out, int out_size, void* d_ws,
                              size_t ws_size, hipStream_t stream) {
  m2f_main<<<256, 512, 0, stream>>>(d_in[1], d_in[2], d_in[3], d_in[6], d_in[0], d_in[4], d_in[5], d_in[8], d_in[7], d_ws, d_out);
}
```

```cpp
#include <hip/hip_runtime.h>
#include <stdint.h>

typedef __attribute__((ext_vector_type(8))) __bf16 bf16x8;
typedef __attribute__((ext_vector_type(2))) __bf16 bf16x2;
typedef __attribute__((ext_vector_type(4))) short s16x4;
typedef __attribute__((ext_vector_type(4))) float f32x4;
typedef __attribute__((ext_vector_type(2))) float f32x2;
typedef __attribute__((ext_vector_type(4))) unsigned u32x4;
typedef __attribute__((ext_vector_type(2))) unsigned u32x2;
typedef __attribute__((address_space(3))) char lds_char;
typedef __attribute__((address_space(3))) s16x4 lds_s16x4;
typedef __attribute__((address_space(3))) u32x4 lds_u32x4;
typedef __attribute__((address_space(3))) f32x4 lds_f32x4;
typedef __attribute__((address_space(3))) float lds_f32;

typedef __attribute__((ext_vector_type(16))) float f32x16;
#define DEV __device__ __forceinline__
#define SB() __builtin_amdgcn_sched_barrier(0)
#define OPQ(x) asm volatile("" : "+v"(x))

constexpr int LDS_X = 0;
constexpr int LDS_WL = 24576;
constexpr int LDS_WV = 49152;
constexpr int LDS_LO = 110592;
constexpr int LDS_LAT = 135168;
constexpr int LDS_R = 159744;
constexpr int LDS_ML = 160256;
constexpr int LDS_TOTAL = 162304;
constexpr int LDS_AF = 73728;
constexpr int LDS_LT0 = 98304;
constexpr int LDS_AWQ = 0, LDS_AWM = LDS_LAT, LDS_AWK = LDS_LAT + 18432;
constexpr int LDS_WO = LDS_LO + 2 * 6144, LDS_RED = LDS_LO + 3 * 6144;
constexpr float THR = 16.0f;
constexpr unsigned WS_A = 4096, WS_REC = 65536, WS_CTX = WS_REC + 64 * 32768, WS_BYTES = WS_CTX + 8192;
constexpr unsigned KCHK = 0x9E3779B9u;
constexpr int SC1 = 16, SPIN_MAX = 1 << 15;
constexpr int L1_B0 = 64, L2_B0 = 128;
#define NH1 6

DEV float cut(float x) { asm("" : "+v"(x)); return x; }
DEV unsigned cvtpk(float lo, float hi) {
  f32x2 v = {lo, hi};
  return __builtin_bit_cast(unsigned, __builtin_convertvector(v, bf16x2));
}
DEV u32x4 cvt8(f32x4 a, f32x4 b) { return u32x4{cvtpk(a[0], a[1]), cvtpk(a[2], a[3]), cvtpk(b[0], b[1]), cvtpk(b[2], b[3])}; }
DEV f32x4 mfma32(u32x4 a, u32x4 b, f32x4 c) {
  return __builtin_amdgcn_mfma_f32_16x16x32_bf16(__builtin_bit_cast(bf16x8, a), __builtin_bit_cast(bf16x8, b), c, 0, 0, 0);
}
DEV f32x16 mfma3232(u32x4 a, u32x4 b, f32x16 c) {
  return __builtin_amdgcn_mfma_f32_32x32x16_bf16(__builtin_bit_cast(bf16x8, a), __builtin_bit_cast(bf16x8, b), c, 0, 0, 0);
}
DEV unsigned f2u(float x) { return __builtin_bit_cast(unsigned, x); }
DEV float u2f(unsigned x) { return __builtin_bit_cast(float, x); }
DEV f32x4 mfma16(u32x2 a, u32x2 b, f32x4 c) {
  return __builtin_amdgcn_mfma_f32_16x16x16bf16_1k(__builtin_bit_cast(s16x4, a), __builtin_bit_cast(s16x4, b), c, 0, 0, 0);
}
DEV f32x4 mfma4(u32x2 a, u32x2 b, f32x4 c) {
  return __builtin_amdgcn_mfma_f32_4x4x4bf16_1k(__builtin_bit_cast(s16x4, a), __builtin_bit_cast(s16x4, b), c, 0, 0, 0);
}
DEV u32x2 trread(lds_char* p) {
  return __builtin_bit_cast(u32x2, __builtin_amdgcn_ds_read_tr16_b64_v4i16((lds_s16x4*)p));
}
template <int CTRL, int BANK> DEV unsigned dppu(unsigned old, unsigned src) {
  return (unsigned)__builtin_amdgcn_update_dpp((int)old, (int)src, CTRL, 0xf, BANK, false);
}
template <int CTRL, int BANK> DEV float dppf(float old, float src) {
  return __builtin_bit_cast(float, dppu<CTRL, BANK>(__builtin_bit_cast(unsigned, old), __builtin_bit_cast(unsigned, src)));
}
constexpr int DPP_ID = 0xE4, DPP_ROR8 = 0x128;
DEV float xg_sum(float x) {
  auto s = __builtin_amdgcn_permlane16_swap(f2u(x), f2u(x), false, false);
  x = u2f(s[0]) + u2f(s[1]);
  auto t = __builtin_amdgcn_permlane32_swap(f2u(x), f2u(x), false, false);
  return u2f(t[0]) + u2f(t[1]);
}
DEV float xg_max(float x) {
  auto s = __builtin_amdgcn_permlane16_swap(f2u(x), f2u(x), false, false);
  x = __builtin_fmaxf(u2f(s[0]), u2f(s[1]));
  auto t = __builtin_amdgcn_permlane32_swap(f2u(x), f2u(x), false, false);
  return __builtin_fmaxf(u2f(t[0]), u2f(t[1]));
}
DEV int laneid() {
  int l;
  asm volatile("v_mbcnt_lo_u32_b32 %0, -1, 0\n\tv_mbcnt_hi_u32_b32 %0, -1, %0" : "=v"(l));
  __builtin_assume(l >= 0 && l < 64);
  return l;
}
DEV void bar() { SB(); __builtin_amdgcn_s_waitcnt(0xC07F); asm volatile("s_barrier" ::: "memory"); SB(); }
typedef __amdgpu_buffer_rsrc_t rsrc_t;
DEV rsrc_t mkrsrc(const void* p, unsigned bytes) { return __builtin_amdgcn_make_buffer_rsrc((void*)p, (short)0, (int)bytes, 0x00020000); }
template <int AUX> DEV u32x4 bld4(rsrc_t r, unsigned voff, unsigned soff) { return __builtin_amdgcn_raw_buffer_load_b128(r, voff, soff, AUX); }
template <int AUX> DEV u32x2 bld2(rsrc_t r, unsigned voff, unsigned soff) { return __builtin_amdgcn_raw_buffer_load_b64(r, voff, soff, AUX); }
constexpr int NT = 2;

DEV void dma16(rsrc_t r, unsigned voff, unsigned soff, unsigned ldsdst) {
  asm volatile("s_mov_b32 m0, %0\n\ts_nop 0\n\tbuffer_load_dwordx4 %1, %2, %3 offen lds" :: "s"(ldsdst), "v"(voff), "s"(r), "s"(soff) : "memory");
}
DEV void dma24(rsrc_t r, unsigned v0, unsigned v1, unsigned soff, unsigned ldsdst) {
  asm volatile("s_mov_b32 m0, %0\n\ts_nop 0\n\tbuffer_load_dwordx4 %1, %3, %4 offen nt lds\n\t"
               "s_mov_b32 exec_hi, 0\n\ts_add_u32 m0, m0, 0x400\n\ts_nop 0\n\tbuffer_load_dwordx4 %2, %3, %4 offen nt lds\n\ts_mov_b32 exec_hi, -1"
               :: "s"(ldsdst), "v"(v0), "v"(v1), "s"(r), "s"(soff) : "memory", "scc");
}
template <int OFF> DEV void ldnt(u32x4& d, rsrc_t r, unsigned voff, unsigned soff) {
  asm volatile("buffer_load_dwordx4 %0, %1, %2, %3 offen offset:%4 nt" : "=v"(d) : "v"(voff), "s"(r), "s"(soff), "n"(OFF) : "memory");
}
template <int OFF> DEV void ldpl(u32x4& d, rsrc_t r, unsigned voff) {
  asm volatile("buffer_load_dwordx4 %0, %1, %2, 0 offen offset:%3" : "=v"(d) : "v"(voff), "s"(r), "n"(OFF) : "memory");
}
template <int AUX> DEV void bst4(u32x4 v, rsrc_t r, unsigned voff, unsigned soff) { __builtin_amdgcn_raw_buffer_store_b128(v, r, voff, soff, AUX); }
template <int N> DEV void vmwait() { asm volatile("s_waitcnt vmcnt(%0)" :: "n"(N) : "memory"); }
DEV unsigned epoch(const void* ws) {
  asm volatile("" : "+s"(ws));
  return *(const __attribute__((address_space(4))) unsigned*)ws + KCHK;
}
DEV f32x2 pkmul(f32x2 a, f32x2 b) { return a * b; }
DEV f32x2 pkadd(f32x2 a, f32x2 b) { return a + b; }
DEV f32x2 pkfma(f32x2 a, f32x2 b, f32x2 c) { return __builtin_elementwise_fma(a, b, c); }
DEV float dot2(unsigned a, unsigned b, float c) {
  return __builtin_amdgcn_fdot2_f32_bf16(__builtin_bit_cast(bf16x2, a), __builtin_bit_cast(bf16x2, b), c, false);
}
template <int CTRL> DEV float dppadd(float x) {
  return cut(x + __builtin_bit_cast(float, __builtin_amdgcn_update_dpp(0, __builtin_bit_cast(int, x), CTRL, 0xf, 0xf, true)));
}

#define TSU 1
#define BAR() bar()
#define TSX(k)
#define RTX(k)
#define VALX(k, x)
#define TSC(k)
#define TSSAVE()
#define BARARGS
#define BARPASS
#define TSINIT()
#define TSFIN()

#define PRB0()
#define PRB(k)
#define PRBEND()

struct Prod {
  u32x4 W[18];
  u32x4 ring[4][6];
  f32x4 lat[3][2];
};
struct ProdC {
  lds_char* smem;
  unsigned xw[2], rw, br[2], lw[2], lr[2], vd[2], voH, vw, lds0, ldsv;
  u32x2 I4;
  bool odd;
  rsrc_t rhi, rlo, rwv;
};
DEV u32x4 lo_read(const ProdC& C, int slot, int ks) { return *(lds_u32x4*)(C.smem + C.br[ks & 1] + slot * 6144 + (ks >> 1) * 128); }
DEV void lat_write(const f32x4 (&T)[3], const ProdC& C, int off) {
#pragma unroll
  for (int tt = 0; tt < 3; ++tt) *(lds_f32x4*)(C.smem + C.lw[tt & 1] + off + (tt >> 1) * 128) = T[tt];
}
DEV f32x4 lat_read(const ProdC& C, int off, int k) { return *(lds_f32x4*)(C.smem + C.lr[k & 1] + off + (k >> 1) * 256); }
template <int M> DEV void ld_hi1(u32x4& d, const ProdC& C, unsigned so0, unsigned so1) { ldnt<(M >> 1) * 128>(d, C.rhi, C.voH, (M & 1) ? so1 : so0); }
DEV void ld_hi(u32x4 (&dst)[6], const ProdC& C, unsigned so) {
  const unsigned so1 = so + 196608;
  ld_hi1<0>(dst[0], C, so, so1); ld_hi1<1>(dst[1], C, so, so1); ld_hi1<2>(dst[2], C, so, so1);
  ld_hi1<3>(dst[3], C, so, so1); ld_hi1<4>(dst[4], C, so, so1); ld_hi1<5>(dst[5], C, so, so1);
}
template <int CTRL> DEV float dppmov(float x) { return __builtin_bit_cast(float, __builtin_amdgcn_update_dpp(0, __builtin_bit_cast(int, x), CTRL, 0xf, 0xf, true)); }
struct Stat { f32x4 q2[2], q1[2]; float a0, a1, v, w, mu; };
DEV void stat_op(Stat& Z, const ProdC& C, int n, int slot) {
  if (n == 0) { Z.v = cut(C.odd ? Z.q1[1][0] : Z.q1[0][0]); Z.w = cut(C.odd ? Z.q1[0][0] : Z.q1[1][0]); }
  if (n == 1) Z.a0 = cut(Z.q2[0][0] + dppmov<0x55>(Z.q2[0][1]));
  if (n == 2) Z.a1 = cut(Z.q2[1][1] + dppmov<0x00>(Z.q2[1][0]));
  if (n == 3) Z.v = cut(Z.v + dppmov<0xB1>(Z.w));
  if (n == 4) Z.a0 = cut(Z.a0 + dppmov<0xAA>(Z.q2[0][2]));
  if (n == 5) Z.a1 = cut(Z.a1 + dppmov<0xAA>(Z.q2[1][2]));
  if (n == 6) Z.v = cut(Z.v + dppmov<0x4E>(Z.v));
  if (n == 7) Z.a0 = cut(Z.a0 + dppmov<0xFF>(Z.q2[0][3]));
  if (n == 8) Z.a1 = cut(Z.a1 + dppmov<0xFF>(Z.q2[1][3]));
  if (n == 9) Z.v = cut(Z.v + dppmov<0x104>(Z.v));
  if (n == 10) Z.a0 = cut(C.odd ? Z.a1 : Z.a0);
  if (n == 11) Z.mu = cut(Z.v * (1.0f / 192.0f));
  if (n == 12) Z.a0 = cut(Z.a0 + dppmov<0x104>(Z.a0));
  if (n == 13) Z.w = cut(Z.mu * Z.mu);
  if (n == 14) Z.a0 = cut(__builtin_fmaxf(Z.a0 * (1.0f / 192.0f) - Z.w, 0.0f) + 1e-5f);
  if (n == 15) Z.a0 = cut(__builtin_amdgcn_rsqf(Z.a0));
  if (n == 17) *(lds_f32*)(C.smem + C.rw + (slot & 1) * 256) = Z.a0;
}
template <int S, int M, int WAIT> DEV void padd(Prod& P, const ProdC& C, f32x4 (&d)[6][2]) {
  if (WAIT >= 0) vmwait<(WAIT >= 0 ? WAIT - M : 0)>();
  OPQ(P.ring[S][M]);
  const u32x4 F = P.ring[S][M];
  d[M][0] = mfma4(C.I4, u32x2{F[0], F[1]}, P.lat[M >> 1][0]);
  d[M][1] = mfma4(C.I4, u32x2{F[2], F[3]}, P.lat[M >> 1][1]);
  OPQ(d[M][0]); OPQ(d[M][1]);
  SB();
}
template <int S, int M, bool HI, bool LAT> DEV void pcvt(Prod& P, const ProdC& C, const f32x4 (&d)[6][2], u32x4 (&x)[6], u32x4 (&B)[6], unsigned so, unsigned so1) {
  x[M] = cvt8(d[M][0], d[M][1]);
  *(lds_u32x4*)(C.smem + C.xw[M & 1] + (S & 1) * 24576 + (M >> 1) * 8192) = x[M];
  SB();
  if (HI) ld_hi1<M>(P.ring[S][M], C, so, so1);
  if (LAT && M >= 3) { B[2 * (M - 3)] = lo_read(C, (S + 2) & 3, 2 * (M - 3)); B[2 * (M - 3) + 1] = lo_read(C, (S + 2) & 3, 2 * (M - 3) + 1); }
  SB();
}
template <int S, bool HI, bool LAT, bool RD, int NWV, int WAIT>
DEV void pstep(Prod& P, const ProdC& C, bool lo, unsigned so, unsigned sl, unsigned wv BARARGS) {
  TSC(0);
  f32x4 d[6][2];
  u32x4 x[6], B[6];
#pragma unroll
  for (int n = 0; n < NWV; ++n) dma16(C.rwv, C.vw, wv + n * 4096, C.ldsv + wv + n * 4096);
  if (lo) dma24(C.rlo, C.vd[0], C.vd[1], sl, C.lds0 + ((S + 1) & 3) * 6144);
  const unsigned so1 = so + 196608;
  SB();
  TSC(1);
  if (WAIT < 0) {
    padd<S, 0, WAIT>(P, C, d); padd<S, 1, WAIT>(P, C, d); padd<S, 2, WAIT>(P, C, d);
    padd<S, 3, WAIT>(P, C, d); padd<S, 4, WAIT>(P, C, d); padd<S, 5, WAIT>(P, C, d);
    TSC(2);
    pcvt<S, 0, HI, LAT>(P, C, d, x, B, so, so1); pcvt<S, 1, HI, LAT>(P, C, d, x, B, so, so1); pcvt<S, 2, HI, LAT>(P, C, d, x, B, so, so1);
    pcvt<S, 3, HI, LAT>(P, C, d, x, B, so, so1); pcvt<S, 4, HI, LAT>(P, C, d, x, B, so, so1); pcvt<S, 5, HI, LAT>(P, C, d, x, B, so, so1);
  } else {
    padd<S, 0, WAIT>(P, C, d); padd<S, 1, WAIT>(P, C, d);
    pcvt<S, 0, HI, LAT>(P, C, d, x, B, so, so1); padd<S, 2, WAIT>(P, C, d);
    pcvt<S, 1, HI, LAT>(P, C, d, x, B, so, so1); padd<S, 3, WAIT>(P, C, d);
    pcvt<S, 2, HI, LAT>(P, C, d, x, B, so, so1); padd<S, 4, WAIT>(P, C, d);
    pcvt<S, 3, HI, LAT>(P, C, d, x, B, so, so1); padd<S, 5, WAIT>(P, C, d);
    TSC(2);
    pcvt<S, 4, HI, LAT>(P, C, d, x, B, so, so1); pcvt<S, 5, HI, LAT>(P, C, d, x, B, so, so1);
  }
  TSC(3);
  const f32x4 z4 = {0.f, 0.f, 0.f, 0.f};
  const u32x2 ones = {0x3F803F80u, 0x3F803F80u};
  Stat Z;
#pragma unroll
  for (int n = 0; n < 6; ++n)
#pragma unroll
    for (int ab = 0; ab < 2; ++ab) {
      const u32x2 v = {x[(n >> 1) * 2 + ab][(n & 1) * 2], x[(n >> 1) * 2 + ab][(n & 1) * 2 + 1]};
      Z.q1[ab] = mfma4(ones, v, n == 0 ? z4 : Z.q1[ab]);
      Z.q2[ab] = mfma4(v, v, n == 0 ? z4 : Z.q2[ab]);
      OPQ(Z.q1[ab]); OPQ(Z.q2[ab]);
      SB();
    }
  TSC(4);
  f32x4 G[3];
#pragma unroll
  for (int n = 0; n < 18; ++n) {
    if (LAT) { G[n % 3] = mfma32(P.W[(n % 3) * 6 + n / 3], B[n / 3], n < 3 ? z4 : G[n % 3]); OPQ(G[n % 3]); SB(); }
    if (RD && n % 2 == 0 && n < 12) P.lat[n >> 2][(n >> 1) & 1] = lat_read(C, ((S + 1) & 1) * 12288, n >> 1);
    stat_op(Z, C, n, S);
    SB();
  }
  TSC(5);
  if (LAT) lat_write(G, C, (S & 1) * 12288);
  TSC(6);
}

struct Cons {
  f32x16 U[6];
  u32x4 Ab[12];
  float mrun, lsum;
};
struct ConsC { lds_char* smem; unsigned xr[4], xt[2][2], ra; };
DEV u32x4 row_read(const ConsC& C, int s) { return *(lds_u32x4*)(C.smem + C.xr[s & 3] + (s >> 2) * 8192); }
DEV u32x4 col_read(const ConsC& C, int o) {
  const int ks = o / 6, ct = o % 6;
  const u32x2 a0 = trread(C.smem + C.xt[ct & 1][0] + (ct >> 1) * 8192 + ks * 2048);
  const u32x2 a1 = trread(C.smem + C.xt[ct & 1][1] + (ct >> 1) * 8192 + ks * 2048);
  return u32x4{a0[0], a0[1], a1[0], a1[1]};
}
DEV void cstep(Cons& K, ConsC& C, int dx, int dr, bool first BARARGS) {
  TSC(0);
  u32x4 X[12];
  f32x4 rr[4];
#pragma unroll
  for (int s = 0; s < 12; ++s) X[s] = row_read(C, s);
#pragma unroll
  for (int a = 0; a < 4; ++a) rr[a] = *(lds_f32x4*)(C.smem + C.ra + a * 32);
  SB();
  f32x16 S = {0.f, 0.f, 0.f, 0.f, 0.f, 0.f, 0.f, 0.f, 0.f, 0.f, 0.f, 0.f, 0.f, 0.f, 0.f, 0.f};
#pragma unroll
  for (int s = 0; s < 12; ++s) {
    S = mfma3232(X[s], K.Ab[s], S);
    OPQ(S);
    SB();
    if (s == 0) TSC(1);
    if (s == 11) TSC(2);
    X[s] = col_read(C, s);
    SB();
  }
#pragma unroll
  for (int s = 0; s < 4; ++s) { C.xr[s] += dx; C.xt[s >> 1][s & 1] += dx; OPQ(C.xr[s]); OPQ(C.xt[s >> 1][s & 1]); }
  C.ra += dr; OPQ(C.ra);
  SB();
  float V[16];
  float vmax = -3e38f;
#pragma unroll
  for (int e = 0; e < 16; ++e) { V[e] = cut(S[e] * rr[e >> 2][e & 3] - K.mrun); vmax = __builtin_fmaxf(vmax, V[e]); }
  if (first || __builtin_amdgcn_ballot_w64(!(vmax <= THR)) != 0) {
    auto t = __builtin_amdgcn_permlane32_swap(f2u(vmax), f2u(vmax), false, false);
    float dl = __builtin_fmaxf(u2f(t[0]), u2f(t[1]));
    if (!first) dl = __builtin_fmaxf(dl, 0.f);
    const float al = __builtin_amdgcn_exp2f(__builtin_fminf(-dl, 0.f));
    K.mrun += dl; K.lsum *= al;
#pragma unroll
    for (int c = 0; c < 6; ++c) K.U[c] *= al;
#pragma unroll
    for (int e = 0; e < 16; ++e) V[e] -= dl;
  }
  TSC(3);
  u32x4 Pb[2];
  float pe[16];
#pragma unroll
  for (int e = 0; e < 8; ++e) pe[e] = __builtin_amdgcn_exp2f(V[e]);
#pragma unroll
  for (int e = 0; e < 8; e += 2) Pb[0][e >> 1] = cvtpk(cut(pe[e] * rr[e >> 2][e & 3]), cut(pe[e + 1] * rr[e >> 2][(e & 3) + 1]));
  SB();
  TSC(4);
#pragma unroll
  for (int ct = 0; ct < 6; ++ct) {
    K.U[ct] = mfma3232(X[ct], Pb[0], K.U[ct]);
    OPQ(K.U[ct]);
    SB();
    if (ct < 4) {
      const int e = 8 + 2 * ct;
      pe[e] = __builtin_amdgcn_exp2f(V[e]); pe[e + 1] = __builtin_amdgcn_exp2f(V[e + 1]);
      Pb[1][ct] = cvtpk(cut(pe[e] * rr[e >> 2][e & 3]), cut(pe[e + 1] * rr[e >> 2][(e & 3) + 1]));
    } else {
      const int e = 4 * (ct - 4);
      K.lsum += cut(pe[e] + pe[e + 1]) + cut(pe[e + 2] + pe[e + 3]);
    }
    SB();
  }
  TSC(5);
#pragma unroll
  for (int ct = 0; ct < 6; ++ct) {
    K.U[ct] = mfma3232(X[6 + ct], Pb[1], K.U[ct]);
    OPQ(K.U[ct]);
    SB();
    if (ct < 2) { const int e = 8 + 4 * ct; K.lsum += cut(pe[e] + pe[e + 1]) + cut(pe[e + 2] + pe[e + 3]); }
    SB();
  }
  TSC(6);
}
DEV void cfinish(Cons& K, lds_char* smem, int h, int b, rsrc_t rws, unsigned ek BARARGS) {
  const int lane = laneid(), i = lane & 15, g = lane >> 4;
  const int xo = (h >> 1) ? LDS_LAT : LDS_X, PH = h & 1;
  f32x4 Uo[12];
  lds_char* ex = smem + xo + PH * 12288 + lane * 16;
#pragma unroll
  for (int ct = 0; ct < 6; ++ct)
#pragma unroll
    for (int a = 0; a < 2; ++a) {
      f32x4 k0, k1;
#pragma unroll
      for (int r = 0; r < 4; ++r) {
        auto sw = __builtin_amdgcn_permlane16_swap(f2u(K.U[ct][8 * a + r]), f2u(K.U[ct][8 * a + 4 + r]), false, false);
        k0[r] = u2f(sw[0]); k1[r] = u2f(sw[1]);
      }
      Uo[2 * ct + a] = k0;
      *(lds_f32x4*)(ex + (2 * ct + a) * 1024) = k1;
      SB();
    }
  TSX(51);
  float mo, lo_;
  {
    auto t = __builtin_amdgcn_permlane32_swap(f2u(K.lsum), f2u(K.lsum), false, false);
    const float l = u2f(t[0]) + u2f(t[1]);
    auto sl = __builtin_amdgcn_permlane16_swap(f2u(l), f2u(l), false, false);
    auto sm = __builtin_amdgcn_permlane16_swap(f2u(K.mrun), f2u(K.mrun), false, false);
    mo = u2f(sm[0]); lo_ = u2f(sl[0]);
    *(__attribute__((address_space(3))) f32x2*)(smem + LDS_ML + h * 512 + lane * 8) = f32x2{u2f(sm[1]), u2f(sl[1])};
  }
  BAR();
  const f32x2 ml = *(__attribute__((address_space(3))) f32x2*)(smem + LDS_ML + (h ^ 1) * 512 + lane * 8);
  lds_char* ey = smem + xo + (1 - PH) * 12288 + lane * 16;
  f32x4 o[12];
#pragma unroll
  for (int c = 0; c < 12; ++c) o[c] = *(lds_f32x4*)(ey + c * 1024);
  lds_char* wv = smem + LDS_WV + h * 18432 + (8 * (g & 1) + 4 * (g >> 1) + (i >> 2)) * 96 + (i & 3) * 8;
  u32x4 Wv[18];
#pragma unroll
  for (int n = 0; n < 18; ++n) {
    const u32x2 a0 = trread(wv + (n / 3) * 3072 + (n % 3) * 32), a1 = trread(wv + (n / 3) * 3072 + 1536 + (n % 3) * 32);
    Wv[n] = u32x4{a0[0], a0[1], a1[0], a1[1]};
  }
  SB();
  const float mn = __builtin_fmaxf(mo, ml[0]);
  const float fa = __builtin_amdgcn_exp2f(mo - mn), fb = __builtin_amdgcn_exp2f(ml[0] - mn);
  const float l = lo_ * fa + ml[1] * fb, il = __builtin_amdgcn_rcpf(l);
  const unsigned lse = (unsigned)__builtin_amdgcn_ds_bpermute((4 * g + (i & 3)) * 4, (int)f2u(mn + __builtin_amdgcn_logf(l)));
  const f32x2 fa2 = {fa * il, fa * il}, fb2 = {fb * il, fb * il};
  f32x2 u2[24], us[2];
#pragma unroll
  for (int c = 0; c < 24; ++c) {
    const f32x2 own = {Uo[c >> 1][(c & 1) * 2], Uo[c >> 1][(c & 1) * 2 + 1]}, oth = {o[c >> 1][(c & 1) * 2], o[c >> 1][(c & 1) * 2 + 1]};
    u2[c] = pkfma(oth, fb2, pkmul(own, fa2));
    us[c & 1] = c < 2 ? u2[c] : pkadd(us[c & 1], u2[c]);
  }
  TSX(52);
  const float um = xg_sum((us[0][0] + us[0][1]) + (us[1][0] + us[1][1])) * (-1.0f / 192.0f);
  const f32x2 um2 = {um, um};
  const f32x4 z4 = {0.f, 0.f, 0.f, 0.f};
  f32x4 cx[3] = {z4, z4, z4};
#pragma unroll
  for (int s = 0; s < 6; ++s) {
    u32x4 ub;
#pragma unroll
    for (int r = 0; r < 4; ++r) { const f32x2 v = pkadd(u2[4 * s + r], um2); ub[r] = cvtpk(v[0], v[1]); }
#pragma unroll
    for (int dt = 0; dt < 3; ++dt) cx[dt] = mfma32(ub, Wv[3 * s + dt], cx[dt]);
  }
  TSX(53);
  const u32x4 ra = cvt8(cx[0], cx[1]);
  u32x4 rb = {cvtpk(cx[2][0], cx[2][1]), cvtpk(cx[2][2], cx[2][3]), lse, 0u};
  rb[3] = (ra[0] + ra[1] + ra[2]) + (ra[3] + rb[0] + rb[1]) + lse + ek;
  const unsigned vo = (lane >> 2) * 32768 + (lane & 3) * 16, so = WS_REC + h * 524288 + b * 64;
  bst4<SC1>(ra, rws, vo, so);
  bst4<SC1>(rb, rws, vo, so + 16384);
}

struct AIn { u32x4 q[6]; };
DEV void a_issue(AIn& I, unsigned lbase, int h, int cs, const void* q_, const void* wq_, const void* wk_, const void* wm_) {
  const int lane = laneid();
  const rsrc_t rq = mkrsrc(q_, 6144u), rwq = mkrsrc(wq_, 73728u), rwk = mkrsrc(wk_, 73728u), rwm = mkrsrc(wm_, 73728u);
  const unsigned vl = lane * 16;
#pragma unroll
  for (int n = 0; n < 18; ++n) dma16(rwq, vl, h * 18432 + n * 1024, lbase + LDS_AWQ + n * 1024);
  unsigned vo[3];
#pragma unroll
  for (int m = 0; m < 3; ++m) { const unsigned o = m * 1024 + vl; vo[m] = (o / 96) * 384 + o % 96; }
#pragma unroll
  for (int n = 0; n < 18; ++n) dma16(rwm, vo[n % 3], (n / 3) * 12288 + cs * 96, lbase + LDS_AWM + n * 1024);
#pragma unroll
  for (int n = 0; n < 5; ++n) dma16(rwk, vl, h * 18432 + cs * 4608 + n * 1024, lbase + LDS_AWK + n * 1024);
  const unsigned vq = (lane & 15) * 384 + (lane >> 4) * 16;
  ldpl<0>(I.q[0], rq, vq); ldpl<64>(I.q[1], rq, vq); ldpl<128>(I.q[2], rq, vq);
  ldpl<192>(I.q[3], rq, vq); ldpl<256>(I.q[4], rq, vq); ldpl<320>(I.q[5], rq, vq);
}
DEV void a_compute(AIn& I, lds_char* smem, int h, int cs, rsrc_t rws, unsigned ek) {
  const int lane = laneid(), i = lane & 15, g = lane >> 4;
  vmwait<0>();
#pragma unroll
  for (int s = 0; s < 6; ++s) OPQ(I.q[s]);
  SB();
  lds_char* a = smem + (8 * g + (i >> 2)) * 96 + (i & 3) * 8;
  const f32x4 z4 = {0.f, 0.f, 0.f, 0.f};
  f32x4 Q[3], M[3], T[3];
  u32x4 W[18], V[18];
  u32x2 Kf[9];
#pragma unroll
  for (int n = 0; n < 18; ++n) {
    const u32x2 a0 = trread(a + LDS_AWQ + (n / 3) * 3072 + (n % 3) * 32), a1 = trread(a + LDS_AWQ + (n / 3) * 3072 + 384 + (n % 3) * 32);
    W[n] = u32x4{a0[0], a0[1], a1[0], a1[1]};
  }
#pragma unroll
  for (int n = 0; n < 18; ++n) {
    const u32x2 a0 = trread(a + LDS_AWM + (n / 3) * 3072 + (n % 3) * 32), a1 = trread(a + LDS_AWM + (n / 3) * 3072 + 384 + (n % 3) * 32);
    V[n] = u32x4{a0[0], a0[1], a1[0], a1[1]};
  }
#pragma unroll
  for (int n = 0; n < 9; ++n) Kf[n] = *(__attribute__((address_space(3))) u32x2*)(smem + LDS_AWK + i * 96 + g * 8 + (n / 3) * 1536 + (n % 3) * 32);
  SB();
#pragma unroll
  for (int n = 0; n < 18; ++n) Q[n % 3] = mfma32(W[n], I.q[n / 3], n < 3 ? z4 : Q[n % 3]);
#pragma unroll
  for (int n = 0; n < 18; ++n) M[n % 3] = mfma32(V[n], I.q[n / 3], n < 3 ? z4 : M[n % 3]);
#pragma unroll
  for (int dt = 0; dt < 3; ++dt) {
    const u32x2 qb = {cvtpk(Q[dt][0], Q[dt][1]), cvtpk(Q[dt][2], Q[dt][3])};
#pragma unroll
    for (int ct = 0; ct < 3; ++ct) T[ct] = mfma16(Kf[3 * ct + dt], qb, dt == 0 ? z4 : T[ct]);
  }
  const float s1 = 1.4426950408889634f * 0.14433756729740643f;
  unsigned P[6];
#pragma unroll
  for (int n = 0; n < 6; ++n)
    P[n] = cvtpk((T[n >> 1][(n & 1) * 2] + 0.5f * M[n >> 1][(n & 1) * 2]) * s1, (T[n >> 1][(n & 1) * 2 + 1] + 0.5f * M[n >> 1][(n & 1) * 2 + 1]) * s1);
  const unsigned so = WS_A + h * 8192 + cs * 2048;
  bst4<SC1>(u32x4{P[0], P[1], P[2], P[0] + P[1] + P[2] + ek}, rws, lane * 16, so);
  bst4<SC1>(u32x4{P[4], P[5], P[3], P[4] + P[5] + P[3] + ek}, rws, lane * 16 + 1024, so);
}
template <int OFF> DEV void ldsc(u32x4& d, rsrc_t r, unsigned voff, unsigned soff) {
  asm volatile("buffer_load_dwordx4 %0, %1, %2, %3 offen offset:%4 sc1" : "=v"(d) : "v"(voff), "s"(r), "s"(soff), "n"(OFF) : "memory");
}
DEV void a_fetch(u32x4 (&G)[8], int h, rsrc_t rws) {
  const unsigned vo = laneid() * 16, s0 = WS_A + h * 8192, s1 = s0 + 4096;
  ldsc<0>(G[0], rws, vo, s0); ldsc<1024>(G[1], rws, vo, s0); ldsc<2048>(G[2], rws, vo, s0); ldsc<3072>(G[3], rws, vo, s0);
  ldsc<0>(G[4], rws, vo, s1); ldsc<1024>(G[5], rws, vo, s1); ldsc<2048>(G[6], rws, vo, s1); ldsc<3072>(G[7], rws, vo, s1);
}
DEV void a_poll(u32x4 (&G)[8], int h, rsrc_t rws, unsigned ek BARARGS) {
  for (int spin = 0;; ++spin) {
    VALX(46, spin);
    if (spin) a_fetch(G, h, rws);
    vmwait<0>();
#pragma unroll
    for (int n = 0; n < 8; ++n) OPQ(G[n]);
    bool bad = false;
#pragma unroll
    for (int n = 0; n < 8; ++n) bad |= G[n][0] + G[n][1] + G[n][2] + ek != G[n][3];
    if (__builtin_amdgcn_ballot_w64(bad) == 0 || spin >= SPIN_MAX) break;
  }
  RTX(45);
}
DEV void a_centre(const u32x4 (&G)[8], lds_char* smem, int h) {
  const int lane = laneid(), i = lane & 15, g = lane >> 4;
  float sm[2] = {0.f, 0.f};
#pragma unroll
  for (int n = 0; n < 24; ++n) sm[n & 1] = dot2(G[n / 3][n % 3], 0x3F803F80u, sm[n & 1]);
  const float mu = xg_sum(sm[0] + sm[1]) * (-1.0f / 192.0f);
  const f32x4 mu4 = {mu, mu, mu, mu};
  const int d = lane & 3;
  const u32x2 I4 = {d == 0 ? 0x00003F80u : (d == 1 ? 0x3F800000u : 0u), d == 2 ? 0x00003F80u : (d == 3 ? 0x3F800000u : 0u)};
  lds_char* dst = smem + LDS_AF + (h >> 1) * 12288 + (g >> 1) * 512 + ((h & 1) * 16 + i) * 16 + (g & 1) * 8;
#pragma unroll
  for (int s = 0; s < 12; ++s) {
    const int cs = s / 3, ct = s % 3;
    const u32x2 v = ct == 0 ? u32x2{G[2 * cs][0], G[2 * cs][1]} : (ct == 1 ? u32x2{G[2 * cs][2], G[2 * cs + 1][2]} : u32x2{G[2 * cs + 1][0], G[2 * cs + 1][1]});
    const f32x4 c = mfma4(I4, v, mu4);
    *(__attribute__((address_space(3))) u32x2*)(dst + s * 1024) = u32x2{cvtpk(c[0], c[1]), cvtpk(c[2], c[3])};
  }
}
DEV float quads_sum(float x) { x += dppmov<0x124>(x); return x + dppmov<0x128>(x); }
DEV float quads_max(float x) { x = __builtin_fmaxf(x, dppmov<0x124>(x)); return __builtin_fmaxf(x, dppmov<0x128>(x)); }
DEV void reduce1(lds_char* smem, int r, int p, rsrc_t rws, unsigned ek BARARGS) {
  const int lane = laneid(), i = lane & 15, g = lane >> 4, d = lane & 3;
  const unsigned m0 = d == 0 ? 0x0000FFFFu : (d == 1 ? 0xFFFF0000u : 0u), m1 = d == 2 ? 0x0000FFFFu : (d == 3 ? 0xFFFF0000u : 0u);
  u32x4 RA[4], RB[4];
  const unsigned so = WS_REC + r * 32768 + p * 4096;
  RTX(44);
  for (int spin = 0;; ++spin) {
    VALX(46, spin);
    asm volatile("" ::: "memory");
#pragma unroll
    for (int j = 0; j < 4; ++j) { RB[j] = bld4<SC1>(rws, lane * 16 + j * 1024, so + 16384); RA[j] = bld4<SC1>(rws, lane * 16 + j * 1024, so); }
    bool bad = false;
#pragma unroll
    for (int j = 0; j < 4; ++j) bad |= (RA[j][0] + RA[j][1] + RA[j][2]) + (RA[j][3] + RB[j][0] + RB[j][1]) + RB[j][2] + ek != RB[j][3];
    if (__builtin_amdgcn_ballot_w64(bad) == 0 || spin >= SPIN_MAX) break;
  }
  RTX(45);
  const float M = xg_max(quads_max(__builtin_fmaxf(__builtin_fmaxf(u2f(RB[0][2]), u2f(RB[1][2])), __builtin_fmaxf(u2f(RB[2][2]), u2f(RB[3][2])))));
  const f32x4 z4 = {0.f, 0.f, 0.f, 0.f};
  f32x4 acc[3] = {z4, z4, z4};
  float L = 0.f;
#pragma unroll
  for (int j = 0; j < 4; ++j) {
    const float wf = __builtin_amdgcn_exp2f(u2f(RB[j][2]) - M);
    const unsigned wb = cvtpk(wf, wf);
    L += u2f(wb & 0xFFFF0000u);
    const u32x2 dg = {wb & m0, wb & m1};
    acc[0] = mfma4(dg, u32x2{RA[j][0], RA[j][1]}, acc[0]);
    acc[1] = mfma4(dg, u32x2{RA[j][2], RA[j][3]}, acc[1]);
    acc[2] = mfma4(dg, u32x2{RB[j][0], RB[j][1]}, acc[2]);
  }
  L = xg_sum(quads_sum(L));
  float u[6];
  f32x4 v;
#pragma unroll
  for (int k = 0; k < 6; ++k) {
    auto t = __builtin_amdgcn_permlane32_swap(f2u(acc[k % 3][k / 3]), f2u(acc[k % 3][2 + k / 3]), false, false);
    u[k] = u2f(t[0]) + u2f(t[1]);
  }
#pragma unroll
  for (int k = 0; k < 3; ++k) {
    auto t = __builtin_amdgcn_permlane16_swap(f2u(u[k]), f2u(u[k + 3]), false, false);
    v[k] = quads_sum(u2f(t[0]) + u2f(t[1]));
  }
  v[3] = 0.f;
  if (i < 4) *(lds_f32x4*)(smem + LDS_RED + p * 256 + g * 64 + d * 16) = v;
  if (lane < 4) *(__attribute__((address_space(3))) f32x2*)(smem + LDS_RED + 1024 + p * 32 + d * 8) = f32x2{M, L};
  bar();
  if (p == 0) {
    const f32x4 x = *(lds_f32x4*)(smem + LDS_RED + (i >> 2) * 256 + g * 64 + d * 16);
    const f32x2 ml = *(__attribute__((address_space(3))) f32x2*)(smem + LDS_RED + 1024 + (i >> 2) * 32 + g * 8);
    const float f = __builtin_amdgcn_exp2f(ml[0] - quads_max(ml[0]));
    const float il = __builtin_amdgcn_rcpf(quads_sum(ml[1] * f));
    u32x4 o;
#pragma unroll
    for (int k = 0; k < 3; ++k) { const float c = quads_sum(x[k] * f) * il; o[k] = cvtpk(c, dppmov<0xB1>(c)); }
    o[3] = o[0] + o[1] + o[2] + ek;
    if (i == 0 || i == 2) bst4<SC1>(o, rws, (d >> 1) * 64 + g * 16, WS_CTX + r * 128);
  }
}
DEV void reduce2_issue(u32x2& qr, unsigned lbase, int nt, const void* q_, const void* wo_) {
  const int lane = laneid();
  const rsrc_t rq = mkrsrc(q_, 6144u), rwo = mkrsrc(wo_, 73728u);
  const unsigned vo = (lane >> 1) * 384 + (lane & 1) * 16, vq = (lane & 15) * 384 + (lane >> 4) * 8;
#pragma unroll
  for (int n = 0; n < 6; ++n) dma16(rwo, vo, n * 12288 + nt * 32, lbase + LDS_WO + n * 1024);
  asm volatile("buffer_load_dwordx2 %0, %1, %2, %3 offen" : "=v"(qr) : "v"(vq), "s"(rq), "s"(nt * 32) : "memory");
}
DEV void reduce2(u32x2& qr, lds_char* smem, int nt, rsrc_t rws, unsigned ek, void* out_ BARARGS) {
  const int lane = laneid(), i = lane & 15, g = lane >> 4, rho = i >> 2;
  vmwait<0>();
  OPQ(qr);
  SB();
  lds_char* a0 = smem + LDS_WO + (16 * (rho >> 1) + 4 * g + (rho & 1)) * 32 + (i & 3) * 8;
  lds_char* a1 = smem + LDS_WO + (32 + 4 * g + (rho & 1)) * 32 + (i & 3) * 8;
  u32x4 W[8], G[8];
#pragma unroll
  for (int m = 0; m < 8; ++m) {
    const u32x2 x0 = trread(a0 + ((m >> 1) * 48 + 2 * (m & 1)) * 32), x1 = trread(a1 + ((m >> 1) * 48 + 2 * (m & 1)) * 32);
    W[m] = u32x4{x0[0], x0[1], x1[0], x1[1]};
    OPQ(W[m]);
  }
  const unsigned vo = (i >> 2) * 512 + g * 128 + (i & 3) * 16;
  RTX(44);
  for (int spin = 0;; ++spin) {
    VALX(46, spin);
    asm volatile("" ::: "memory");
#pragma unroll
    for (int m = 0; m < 8; ++m) G[m] = bld4<SC1>(rws, vo + (m & 1) * 64 + ((m >> 1) & 1) * 2048, WS_CTX + (m >> 2) * 4096);
    bool bad = false;
#pragma unroll
    for (int m = 0; m < 8; ++m) bad |= G[m][0] + G[m][1] + G[m][2] + ek != G[m][3];
    if (__builtin_amdgcn_ballot_w64(bad) == 0 || spin >= SPIN_MAX) break;
  }
  RTX(45);
  f32x4 c[2] = {{u2f(qr[0] << 16), u2f(qr[0] & 0xFFFF0000u), u2f(qr[1] << 16), u2f(qr[1] & 0xFFFF0000u)}, {0.f, 0.f, 0.f, 0.f}};
#pragma unroll
  for (int m = 0; m < 8; ++m) { G[m][3] = 0u; c[m & 1] = mfma32(W[m], G[m], c[m & 1]); }
  const f32x4 o = c[0] + c[1];
  __builtin_amdgcn_raw_buffer_store_b64(u32x2{cvtpk(o[0], o[1]), cvtpk(o[2], o[3])}, mkrsrc(out_, 6144u), i * 384 + g * 8, nt * 32, 0);
  if (nt == 0 && lane == 0) __builtin_amdgcn_raw_buffer_store_b32(ek - KCHK + 1u, rws, 0, 0, 0);
}

__global__ void __launch_bounds__(512, 2)
m2f_main(const void* __restrict__ fhi_, const void* __restrict__ flo_, const void* __restrict__ wlat_,
         const void* __restrict__ wv_, const void* __restrict__ q_, const void* __restrict__ wq_, const void* __restrict__ wk_,
         const void* __restrict__ wm_, const void* __restrict__ wo_, void* __restrict__ ws_, void* __restrict__ out_) {
  __shared__ __attribute__((aligned(16))) char smem_[LDS_TOTAL];
  lds_char* smem = (lds_char*)smem_;
  const int w = __builtin_amdgcn_readfirstlane(__builtin_amdgcn_workitem_id_x() >> 6);
  const int b = __builtin_amdgcn_workgroup_id_x();
  TSINIT();
  PRB0();

  if (w < 4) {
    const int p = w, lane = laneid(), i = lane & 15, g = lane >> 4;
#define PRIO_P 0
    __builtin_amdgcn_s_setprio(PRIO_P);
    const rsrc_t rwl = mkrsrc(wlat_, 73728u);
    ProdC C;
    C.smem = smem;
    C.rhi = mkrsrc(fhi_, 100663296u);
    C.rlo = mkrsrc(flo_, 25165824u);
    C.rwv = mkrsrc(wv_, 73728u);
    const unsigned j = lane >> 3, c7 = lane & 7;
    const unsigned lbase = (unsigned)(uintptr_t)smem;
    C.voH = j * 384 + c7 * 16;
    C.vw = lane * 16;
    C.lds0 = lbase + LDS_LO + p * 1536;
    C.ldsv = lbase + LDS_WV;
#pragma unroll
    for (int k = 0; k < 2; ++k) {
      const unsigned o = k * 1024 + (lane & (k ? 31 : 63)) * 16, px = o / 384, pc = (o % 384) / 16;
      C.vd[k] = px * 384 + ((pc ^ (2 * p + (px >> 1))) * 16);
    }
    const unsigned soL = (unsigned)(b * 256 + p * 4) * 384u;
    const unsigned soH = (unsigned)(b * 1024 + p * 8) * 384u;
    Prod P;
    dma24(C.rlo, C.vd[0], C.vd[1], soL, C.lds0);
    dma24(C.rlo, C.vd[0], C.vd[1], soL + 6144, C.lds0 + 6144);
    SB();
    if (b < 16) bar();
    {
      unsigned vo[6];
#pragma unroll
      for (int m = 0; m < 6; ++m) {
        const unsigned o = m * 1024 + lane * 16, k = o / 384, pc = (o % 384) / 16;
        vo[m] = k * 384 + ((pc ^ (2 * (((k >> 1) & 1) | ((k >> 3) << 1)))) * 16);
      }
      const unsigned so1 = soH + 196608, wo = p * 18432, wl = lbase + LDS_WL + p * 18432;
#define WGRP(M) ld_hi1<M>(P.ring[0][M], C, soH, so1); if (M < NH1) ld_hi1<M>(P.ring[1][M], C, soH + 12288, so1 + 12288); \
      for (int n = 3 * M; n < 3 * M + 3; ++n) dma16(rwl, vo[n % 6], wo + (n / 6) * 6144, wl + n * 1024);
      WGRP(0) WGRP(1) WGRP(2) WGRP(3) WGRP(4) WGRP(5)
#undef WGRP
#define HREST(M) if (M >= NH1) ld_hi1<M>(P.ring[1][M], C, soH + 12288, so1 + 12288);
      HREST(0) HREST(1) HREST(2) HREST(3) HREST(4) HREST(5)
#undef HREST
    }
    SB();
    dma24(C.rlo, C.vd[0], C.vd[1], soL + 2 * 6144, C.lds0 + 2 * 6144);
    dma24(C.rlo, C.vd[0], C.vd[1], soL + 3 * 6144, C.lds0 + 3 * 6144);
    ld_hi(P.ring[2], C, soH + 2 * 12288);
    SB();
    {
      const int d = lane & 3;
      C.I4[0] = d == 0 ? 0x00003F80u : (d == 1 ? 0x3F800000u : 0u);
      C.I4[1] = d == 2 ? 0x00003F80u : (d == 3 ? 0x3F800000u : 0u);
#pragma unroll
      for (int ab = 0; ab < 2; ++ab) {
        C.xw[ab] = LDS_X + (16 * p + 8 * ab + j) * 128 + ((c7 ^ (((j >> 1) & 1) * 4 + 2 * ab + (j >> 2))) * 16);
        C.br[ab] = LDS_LO + i * 384 + (((4 * ab + g) ^ (i >> 1)) * 16);
        C.lw[ab] = LDS_LAT + i * 768 + (((12 * p + 4 * ab + g) ^ (i & 7)) * 16);
        C.lr[ab] = LDS_LAT + (4 * p + (j >> 1)) * 768 + (((2 * c7 + ab) ^ ((4 * p + (j >> 1)) & 7)) * 16);
      }
      C.odd = lane & 1;
      C.rw = c7 < 2 ? LDS_R + (16 * p + 8 * (lane & 1) + j) * 4 : LDS_ML + lane * 4;
#pragma unroll
      for (int ab = 0; ab < 2; ++ab) { OPQ(C.xw[ab]); OPQ(C.br[ab]); OPQ(C.lw[ab]); OPQ(C.lr[ab]); OPQ(C.vd[ab]); }
      OPQ(C.rw); OPQ(C.vw);
    }
    SB();
    unsigned ek = epoch(ws_);
    SB();
    vmwait<16 - NH1>();
    asm volatile("" : "+s"(ek));
    BAR();
    PRB(1);
    {
      const unsigned rp = i >> 2, cp = i & 3, s = (rp >> 1) | ((g & 1) << 1);
      u32x4 B[12];
#pragma unroll
      for (int n = 0; n < 12; ++n) B[n] = lo_read(C, n / 6, n % 6);
#pragma unroll
      for (int ks = 0; ks < 6; ++ks)
#pragma unroll
        for (int tt = 0; tt < 3; ++tt) {
          const unsigned ct = 3 * p + tt;
          lds_char* a = smem + LDS_WL + (8 * g + rp) * 384 + (((ct & 3) ^ s) * 32) + cp * 8 + (ct >> 2) * 128;
          const u32x2 a0 = trread(a + ks * 12288), a1 = trread(a + ks * 12288 + 1536);
          P.W[tt * 6 + ks] = u32x4{a0[0], a0[1], a1[0], a1[1]};
        }
      SB();
      const f32x4 z4 = {0.f, 0.f, 0.f, 0.f};
      f32x4 G0[3], G1[3];
#pragma unroll
      for (int n = 0; n < 18; ++n) {
        G0[n % 3] = mfma32(P.W[(n % 3) * 6 + n / 3], B[n / 3], n < 3 ? z4 : G0[n % 3]);
        G1[n % 3] = mfma32(P.W[(n % 3) * 6 + n / 3], B[6 + n / 3], n < 3 ? z4 : G1[n % 3]);
        OPQ(G0[n % 3]); OPQ(G1[n % 3]);
        SB();
      }
      lat_write(G0, C, LDS_LT0 - LDS_LAT);
      lat_write(G1, C, 12288);
    }
    vmwait<8>();
    BAR();
    PRB(2);
#pragma unroll
    for (int k = 0; k < 6; ++k) P.lat[k >> 1][k & 1] = lat_read(C, LDS_LT0 - LDS_LAT, k);
    dma24(C.rlo, C.vd[0], C.vd[1], soL + 4 * 6144, C.lds0);
    ld_hi(P.ring[3], C, soH + 3 * 12288);
    SB();
#pragma unroll 1
    for (int u = 0; u < 3; ++u) {
      const unsigned so = soH + (u + 1) * 49152, sl = soL + 6144 + (u + 1) * 24576, wv = p * 1024 + u * 20480;
      pstep<0, true, true, true, 1, -1>(P, C, true, so, sl, wv BARPASS); vmwait<23>(); BAR(); if (u == 0) PRB(3);
      pstep<1, true, true, true, 1, -1>(P, C, true, so + 12288, sl + 6144, wv + 4096 BARPASS); vmwait<24>(); BAR(); if (u == 0) PRB(4);
      pstep<2, true, true, true, 1, -1>(P, C, true, so + 2 * 12288, sl + 2 * 6144, wv + 2 * 4096 BARPASS); vmwait<24>(); BAR();
      pstep<3, true, true, true, 2, -1>(P, C, u < 2, so + 3 * 12288, sl + 3 * 6144, wv + 3 * 4096 BARPASS); vmwait<23>(); BAR();
    }
    pstep<0, false, true, true, 3, -1>(P, C, false, 0, 0, p * 1024 + 61440 BARPASS); vmwait<17>(); BAR();
    pstep<1, false, true, true, 0, -1>(P, C, false, 0, 0, 0 BARPASS); BAR();
    pstep<2, false, false, true, 0, 16>(P, C, false, 0, 0, 0 BARPASS); BAR();
    pstep<3, false, false, false, 0, 8>(P, C, false, 0, 0, 0 BARPASS); vmwait<0>(); BAR();
    PRB(6);
    const bool l2 = p == 0 && b >= L2_B0 && b < L2_B0 + 12;
    u32x2 qr;
    if (l2) reduce2_issue(qr, lbase, b - L2_B0, q_, wo_);
    BAR();
    PRB(7);
    if (b >= L1_B0 && b < L1_B0 + 64) reduce1(smem, b - L1_B0, p, mkrsrc(ws_, WS_BYTES), ek BARPASS);
    if (l2) reduce2(qr, smem, b - L2_B0, mkrsrc(ws_, WS_BYTES), ek, out_ BARPASS);
    PRB(9);
    PRBEND();
  } else {
    const int h = w - 4, hp = h >> 1, ph = h & 1, lane = laneid(), i = lane & 15, g = lane >> 4;
    const rsrc_t rws = mkrsrc(ws_, WS_BYTES);
    Cons K;
#define PRIO_C 2
    __builtin_amdgcn_s_setprio(PRIO_C);
    unsigned ek, pf;
    {
      const unsigned n = ((b >> 3) - 2) * 4 + h, h0 = (b & 7) >> 2, cs = b & 3;
      const void* src = n < 10 ? wq_ : (n < 16 ? wm_ : (n < 20 ? wk_ : q_));
      const unsigned so = n < 5 ? h0 * 18432 + n * 4096 : (n < 10 ? h0 * 18432 + 36864 + (n - 5) * 4096 : (n < 16 ? (n - 10) * 12288 + ((cs * 96) >> 6) * 64
                        : (n < 20 ? h0 * 18432 + ((n >> 1) & 1) * 36864 + cs * 4608 + (n & 1) * 4096 : (n & 1) * 4096)));
      const unsigned vo = n >= 10 && n < 16 ? (lane >> 1) * 384 + (lane & 1) * 64 : lane * 64;
      asm volatile("buffer_load_dword %0, %1, %2, %3 offen" : "=v"(pf) : "v"(vo), "s"(mkrsrc(src, n < 20 ? 73728u : (n < 22 ? 6144u : 0u))), "s"(so) : "memory");
    }
    if (b < 16 && h == 0) {
      AIn I;
      a_issue(I, (unsigned)(uintptr_t)smem, b >> 2, b & 3, q_, wq_, wk_, wm_);
      ek = epoch(ws_);
      asm volatile("s_barrier" ::: "memory");
      RTX(47);
      a_compute(I, smem, b >> 2, b & 3, rws, ek);
      RTX(48);
    } else {
      if (b < 16) bar();
      ek = epoch(ws_);
    }
    asm volatile("" : "+s"(ek));
    u32x4 G[8];
    BAR();
    RTX(44);
    a_fetch(G, h, rws);
    BAR();
    a_poll(G, h, rws, ek BARPASS);
    OPQ(pf);
    a_centre(G, smem, h);
    ConsC C;
    C.smem = smem;
    const unsigned hl = lane >> 5;
    {
      const unsigned m = lane & 31, row = 32 * ph + m, f = ((m >> 1) & 1) * 4 + ((m >> 2) & 3);
#pragma unroll
      for (int s = 0; s < 4; ++s) C.xr[s] = LDS_X + row * 128 + (((2 * s + hl) ^ f) * 16);
      const unsigned cb = g & 1, rp = i >> 2, cp = i & 3;
#pragma unroll
      for (int c = 0; c < 2; ++c)
#pragma unroll
        for (int sec = 0; sec < 2; ++sec)
          C.xt[c][sec] = LDS_X + (32 * ph + 8 * sec + 4 * hl + rp) * 128 + (((4 * c + 2 * cb + (cp >> 1)) ^ ((rp >> 1) * 4 + hl + 2 * sec)) * 16) + (cp & 1) * 8;
      C.ra = LDS_R + (32 * ph + 4 * hl) * 4;
#pragma unroll
      for (int s = 0; s < 4; ++s) { OPQ(C.xr[s]); OPQ(C.xt[s >> 1][s & 1]); }
      OPQ(C.ra);
    }
#pragma unroll
    for (int c = 0; c < 6; ++c)
#pragma unroll
      for (int e = 0; e < 16; ++e) K.U[c][e] = 0.f;
    K.mrun = 0.f; K.lsum = 0.f;
#pragma unroll 1
    for (int t = 0; t < 16; ++t) {
      BAR();
      if (t == 0) {
        lds_char* af = smem + LDS_AF + hp * 12288 + (laneid() ^ (16 * ph)) * 16;
#pragma unroll
        for (int s = 0; s < 12; ++s) K.Ab[s] = *(lds_u32x4*)(af + s * 1024);
      }
      cstep(K, C, (t & 1) ? -24576 : 24576, (t & 1) ? -256 : 256, t == 0 BARPASS);
    }
    TSX(50);
    cfinish(K, smem, h, b, rws, ek BARPASS);
  }
  TSFIN();
}

extern "C" void kernel_launch(void* const* d_in, const int* in_sizes, int n_in, void* d_out, int out_size, void* d_ws,
                              size_t ws_size, hipStream_t stream) {
  m2f_main<<<256, 512, 0, stream>>>(d_in[1], d_in[2], d_in[3], d_in[6], d_in[0], d_in[4], d_in[5], d_in[8], d_in[7], d_ws, d_out);
}
```

```cpp
#include <hip/hip_runtime.h>
#include <stdint.h>

typedef __attribute__((ext_vector_type(8))) __bf16 bf16x8;
typedef __attribute__((ext_vector_type(2))) __bf16 bf16x2;
typedef __attribute__((ext_vector_type(4))) short s16x4;
typedef __attribute__((ext_vector_type(4))) float f32x4;
typedef __attribute__((ext_vector_type(2))) float f32x2;
typedef __attribute__((ext_vector_type(4))) unsigned u32x4;
typedef __attribute__((ext_vector_type(2))) unsigned u32x2;
typedef __attribute__((address_space(3))) char lds_char;
typedef __attribute__((address_space(3))) s16x4 lds_s16x4;
typedef __attribute__((address_space(3))) u32x4 lds_u32x4;
typedef __attribute__((address_space(3))) f32x4 lds_f32x4;
typedef __attribute__((address_space(3))) float lds_f32;

typedef __attribute__((ext_vector_type(16))) float f32x16;
#define DEV __device__ __forceinline__
#define SB() __builtin_amdgcn_sched_barrier(0)
#define OPQ(x) asm volatile("" : "+v"(x))

constexpr int LDS_X = 0;
constexpr int LDS_WL = 24576;
constexpr int LDS_WV = 49152;
constexpr int LDS_LO = 110592;
constexpr int LDS_LAT = 135168;
constexpr int LDS_R = 159744;
constexpr int LDS_ML = 160256;
constexpr int LDS_TOTAL = 162304;
constexpr int LDS_AF = 73728;
constexpr int LDS_LT0 = 98304;
constexpr int LDS_AWQ = 0, LDS_AWM = LDS_LAT, LDS_AWK = LDS_LAT + 18432;
constexpr int LDS_WO = LDS_LO + 2 * 6144, LDS_RED = LDS_LO + 3 * 6144;
constexpr float THR = 16.0f;
constexpr unsigned WS_A = 4096, WS_REC = 65536, WS_CTX = WS_REC + 64 * 32768, WS_BYTES = WS_CTX + 8192;
constexpr unsigned KCHK = 0x9E3779B9u;
constexpr int SC1 = 16, SPIN_MAX = 1 << 15;
constexpr int L1_B0 = 64, L2_B0 = 128;
#define PRIO_P 0
#define PRIO_A 3
#define PRIO_G 3
#define NH1 6

DEV float cut(float x) { asm("" : "+v"(x)); return x; }
DEV unsigned cvtpk(float lo, float hi) {
  f32x2 v = {lo, hi};
  return __builtin_bit_cast(unsigned, __builtin_convertvector(v, bf16x2));
}
DEV u32x4 cvt8(f32x4 a, f32x4 b) { return u32x4{cvtpk(a[0], a[1]), cvtpk(a[2], a[3]), cvtpk(b[0], b[1]), cvtpk(b[2], b[3])}; }
DEV f32x4 mfma32(u32x4 a, u32x4 b, f32x4 c) {
  return __builtin_amdgcn_mfma_f32_16x16x32_bf16(__builtin_bit_cast(bf16x8, a), __builtin_bit_cast(bf16x8, b), c, 0, 0, 0);
}
DEV f32x16 mfma3232(u32x4 a, u32x4 b, f32x16 c) {
  return __builtin_amdgcn_mfma_f32_32x32x16_bf16(__builtin_bit_cast(bf16x8, a), __builtin_bit_cast(bf16x8, b), c, 0, 0, 0);
}
DEV unsigned f2u(float x) { return __builtin_bit_cast(unsigned, x); }
DEV float u2f(unsigned x) { return __builtin_bit_cast(float, x); }
DEV f32x4 mfma16(u32x2 a, u32x2 b, f32x4 c) {
  return __builtin_amdgcn_mfma_f32_16x16x16bf16_1k(__builtin_bit_cast(s16x4, a), __builtin_bit_cast(s16x4, b), c, 0, 0, 0);
}
DEV f32x4 mfma4(u32x2 a, u32x2 b, f32x4 c) {
  return __builtin_amdgcn_mfma_f32_4x4x4bf16_1k(__builtin_bit_cast(s16x4, a), __builtin_bit_cast(s16x4, b), c, 0, 0, 0);
}
DEV u32x2 trread(lds_char* p) {
  return __builtin_bit_cast(u32x2, __builtin_amdgcn_ds_read_tr16_b64_v4i16((lds_s16x4*)p));
}
template <int CTRL, int BANK> DEV unsigned dppu(unsigned old, unsigned src) {
  return (unsigned)__builtin_amdgcn_update_dpp((int)old, (int)src, CTRL, 0xf, BANK, false);
}
template <int CTRL, int BANK> DEV float dppf(float old, float src) {
  return __builtin_bit_cast(float, dppu<CTRL, BANK>(__builtin_bit_cast(unsigned, old), __builtin_bit_cast(unsigned, src)));
}
constexpr int DPP_ID = 0xE4, DPP_ROR8 = 0x128;
DEV float xg_sum(float x) {
  auto s = __builtin_amdgcn_permlane16_swap(f2u(x), f2u(x), false, false);
  x = u2f(s[0]) + u2f(s[1]);
  auto t = __builtin_amdgcn_permlane32_swap(f2u(x), f2u(x), false, false);
  return u2f(t[0]) + u2f(t[1]);
}
DEV float xg_max(float x) {
  auto s = __builtin_amdgcn_permlane16_swap(f2u(x), f2u(x), false, false);
  x = __builtin_fmaxf(u2f(s[0]), u2f(s[1]));
  auto t = __builtin_amdgcn_permlane32_swap(f2u(x), f2u(x), false, false);
  return __builtin_fmaxf(u2f(t[0]), u2f(t[1]));
}
DEV int laneid() {
  int l;
  asm volatile("v_mbcnt_lo_u32_b32 %0, -1, 0\n\tv_mbcnt_hi_u32_b32 %0, -1, %0" : "=v"(l));
  __builtin_assume(l >= 0 && l < 64);
  return l;
}
DEV void bar() { SB(); __builtin_amdgcn_s_waitcnt(0xC07F); asm volatile("s_barrier" ::: "memory"); SB(); }
typedef __amdgpu_buffer_rsrc_t rsrc_t;
DEV rsrc_t mkrsrc(const void* p, unsigned bytes) { return __builtin_amdgcn_make_buffer_rsrc((void*)p, (short)0, (int)bytes, 0x00020000); }
template <int AUX> DEV u32x4 bld4(rsrc_t r, unsigned voff, unsigned soff) { return __builtin_amdgcn_raw_buffer_load_b128(r, voff, soff, AUX); }
template <int AUX> DEV u32x2 bld2(rsrc_t r, unsigned voff, unsigned soff) { return __builtin_amdgcn_raw_buffer_load_b64(r, voff, soff, AUX); }
constexpr int NT = 2;

DEV void dma16(rsrc_t r, unsigned voff, unsigned soff, unsigned ldsdst) {
  asm volatile("s_mov_b32 m0, %0\n\ts_nop 0\n\tbuffer_load_dwordx4 %1, %2, %3 offen lds" :: "s"(ldsdst), "v"(voff), "s"(r), "s"(soff) : "memory");
}
DEV void dma24(rsrc_t r, unsigned v0, unsigned v1, unsigned soff, unsigned ldsdst) {
  asm volatile("s_mov_b32 m0, %0\n\ts_nop 0\n\tbuffer_load_dwordx4 %1, %3, %4 offen nt lds\n\t"
               "s_mov_b32 exec_hi, 0\n\ts_add_u32 m0, m0, 0x400\n\ts_nop 0\n\tbuffer_load_dwordx4 %2, %3, %4 offen nt lds\n\ts_mov_b32 exec_hi, -1"
               :: "s"(ldsdst), "v"(v0), "v"(v1), "s"(r), "s"(soff) : "memory", "scc");
}
template <int OFF> DEV void ldnt(u32x4& d, rsrc_t r, unsigned voff, unsigned soff) {
  asm volatile("buffer_load_dwordx4 %0, %1, %2, %3 offen offset:%4 nt" : "=v"(d) : "v"(voff), "s"(r), "s"(soff), "n"(OFF) : "memory");
}
template <int OFF> DEV void ldpl(u32x4& d, rsrc_t r, unsigned voff) {
  asm volatile("buffer_load_dwordx4 %0, %1, %2, 0 offen offset:%3" : "=v"(d) : "v"(voff), "s"(r), "n"(OFF) : "memory");
}
template <int AUX> DEV void bst4(u32x4 v, rsrc_t r, unsigned voff, unsigned soff) { __builtin_amdgcn_raw_buffer_store_b128(v, r, voff, soff, AUX); }
template <int N> DEV void vmwait() { asm volatile("s_waitcnt vmcnt(%0)" :: "n"(N) : "memory"); }
DEV unsigned epoch(const void* ws) {
  asm volatile("" : "+s"(ws));
  return *(const __attribute__((address_space(4))) unsigned*)ws + KCHK;
}
DEV f32x2 pkmul(f32x2 a, f32x2 b) { return a * b; }
DEV f32x2 pkadd(f32x2 a, f32x2 b) { return a + b; }
DEV f32x2 pkfma(f32x2 a, f32x2 b, f32x2 c) { return __builtin_elementwise_fma(a, b, c); }
DEV float dot2(unsigned a, unsigned b, float c) {
  return __builtin_amdgcn_fdot2_f32_bf16(__builtin_bit_cast(bf16x2, a), __builtin_bit_cast(bf16x2, b), c, false);
}
template <int CTRL> DEV float dppadd(float x) {
  return cut(x + __builtin_bit_cast(float, __builtin_amdgcn_update_dpp(0, __builtin_bit_cast(int, x), CTRL, 0xf, 0xf, true)));
}

#define TSU 1
#define BAR() bar()
#define TSX(k)
#define RTX(k)
#define VALX(k, x)
#define TSC(k)
#define TSSAVE()
#define BARARGS
#define BARPASS
#define TSINIT()
#define TSFIN()

#define PRB0()
#define PRB(k)
#define PRBEND()

struct Prod {
  u32x4 W[18];
  u32x4 ring[4][6];
  f32x4 lat[3][2];
};
struct ProdC {
  lds_char* smem;
  unsigned xw[2], rw, br[2], lw[2], lr[2], vd[2], voH, vw, lds0, ldsv;
  u32x2 I4;
  bool odd;
  rsrc_t rhi, rlo, rwv;
};
DEV u32x4 lo_read(const ProdC& C, int slot, int ks) { return *(lds_u32x4*)(C.smem + C.br[ks & 1] + slot * 6144 + (ks >> 1) * 128); }
DEV void lat_write(const f32x4 (&T)[3], const ProdC& C, int off) {
#pragma unroll
  for (int tt = 0; tt < 3; ++tt) *(lds_f32x4*)(C.smem + C.lw[tt & 1] + off + (tt >> 1) * 128) = T[tt];
}
DEV f32x4 lat_read(const ProdC& C, int off, int k) { return *(lds_f32x4*)(C.smem + C.lr[k & 1] + off + (k >> 1) * 256); }
template <int M> DEV void ld_hi1(u32x4& d, const ProdC& C, unsigned so0, unsigned so1) { ldnt<(M >> 1) * 128>(d, C.rhi, C.voH, (M & 1) ? so1 : so0); }
DEV void ld_hi(u32x4 (&dst)[6], const ProdC& C, unsigned so) {
  const unsigned so1 = so + 196608;
  ld_hi1<0>(dst[0], C, so, so1); ld_hi1<1>(dst[1], C, so, so1); ld_hi1<2>(dst[2], C, so, so1);
  ld_hi1<3>(dst[3], C, so, so1); ld_hi1<4>(dst[4], C, so, so1); ld_hi1<5>(dst[5], C, so, so1);
}
template <int CTRL> DEV float dppmov(float x) { return __builtin_bit_cast(float, __builtin_amdgcn_update_dpp(0, __builtin_bit_cast(int, x), CTRL, 0xf, 0xf, true)); }
struct Stat { f32x4 q2[2], q1[2]; float a0, a1, v, w, mu; };
DEV void stat_op(Stat& Z, const ProdC& C, int n, int slot) {
  if (n == 0) { Z.v = cut(C.odd ? Z.q1[1][0] : Z.q1[0][0]); Z.w = cut(C.odd ? Z.q1[0][0] : Z.q1[1][0]); }
  if (n == 1) Z.a0 = cut(Z.q2[0][0] + dppmov<0x55>(Z.q2[0][1]));
  if (n == 2) Z.a1 = cut(Z.q2[1][1] + dppmov<0x00>(Z.q2[1][0]));
  if (n == 3) Z.v = cut(Z.v + dppmov<0xB1>(Z.w));
  if (n == 4) Z.a0 = cut(Z.a0 + dppmov<0xAA>(Z.q2[0][2]));
  if (n == 5) Z.a1 = cut(Z.a1 + dppmov<0xAA>(Z.q2[1][2]));
  if (n == 6) Z.v = cut(Z.v + dppmov<0x4E>(Z.v));
  if (n == 7) Z.a0 = cut(Z.a0 + dppmov<0xFF>(Z.q2[0][3]));
  if (n == 8) Z.a1 = cut(Z.a1 + dppmov<0xFF>(Z.q2[1][3]));
  if (n == 9) Z.v = cut(Z.v + dppmov<0x104>(Z.v));
  if (n == 10) Z.a0 = cut(C.odd ? Z.a1 : Z.a0);
  if (n == 11) Z.mu = cut(Z.v * (1.0f / 192.0f));
  if (n == 12) Z.a0 = cut(Z.a0 + dppmov<0x104>(Z.a0));
  if (n == 13) Z.w = cut(Z.mu * Z.mu);
  if (n == 14) Z.a0 = cut(__builtin_fmaxf(Z.a0 * (1.0f / 192.0f) - Z.w, 0.0f) + 1e-5f);
  if (n == 15) Z.a0 = cut(__builtin_amdgcn_rsqf(Z.a0));
  if (n == 17) *(lds_f32*)(C.smem + C.rw + (slot & 1) * 256) = Z.a0;
}
template <int S, int M, int WAIT> DEV void padd(Prod& P, const ProdC& C, f32x4 (&d)[6][2]) {
  if (WAIT >= 0) vmwait<(WAIT >= 0 ? WAIT - M : 0)>();
  OPQ(P.ring[S][M]);
  const u32x4 F = P.ring[S][M];
  d[M][0] = mfma4(C.I4, u32x2{F[0], F[1]}, P.lat[M >> 1][0]);
  d[M][1] = mfma4(C.I4, u32x2{F[2], F[3]}, P.lat[M >> 1][1]);
  OPQ(d[M][0]); OPQ(d[M][1]);
  SB();
}
template <int M> DEV void pcvt(const f32x4 (&d)[6][2], u32x4 (&x)[6]) { x[M] = cvt8(d[M][0], d[M][1]); OPQ(x[M]); SB(); }
template <int S> DEV void x_write(const ProdC& C, const u32x4 (&x)[6], int m) {
  *(lds_u32x4*)(C.smem + C.xw[m & 1] + (S & 1) * 24576 + (m >> 1) * 8192) = x[m];
}
template <int S, bool HI, bool LAT, bool RD, int NWV, int WAIT>
DEV void pstep(Prod& P, const ProdC& C, bool lo, unsigned so, unsigned sl, unsigned wv BARARGS) {
  TSC(0);
  f32x4 d[6][2];
  u32x4 x[6], B[6];
  __builtin_amdgcn_s_setprio(PRIO_A);
  padd<S, 0, WAIT>(P, C, d); padd<S, 1, WAIT>(P, C, d); padd<S, 2, WAIT>(P, C, d);
  padd<S, 3, WAIT>(P, C, d); padd<S, 4, WAIT>(P, C, d); padd<S, 5, WAIT>(P, C, d);
  __builtin_amdgcn_s_setprio(PRIO_P);
  SB();
  TSC(1);
  pcvt<0>(d, x); pcvt<1>(d, x); pcvt<2>(d, x); pcvt<3>(d, x); pcvt<4>(d, x); pcvt<5>(d, x);
  TSC(2);
  if (lo) dma24(C.rlo, C.vd[0], C.vd[1], sl, C.lds0 + ((S + 1) & 3) * 6144);
  if (HI) ld_hi(P.ring[S], C, so);
#pragma unroll
  for (int n = 0; n < NWV; ++n) dma16(C.rwv, C.vw, wv + n * 4096, C.ldsv + wv + n * 4096);
  SB();
  TSC(3);
  const f32x4 z4 = {0.f, 0.f, 0.f, 0.f};
  const u32x2 ones = {0x3F803F80u, 0x3F803F80u};
  Stat Z;
#pragma unroll
  for (int n = 0; n < 6; ++n) {
    x_write<S>(C, x, n);
    SB();
#pragma unroll
    for (int ab = 0; ab < 2; ++ab) {
      const u32x2 v = {x[(n >> 1) * 2 + ab][(n & 1) * 2], x[(n >> 1) * 2 + ab][(n & 1) * 2 + 1]};
      Z.q1[ab] = mfma4(ones, v, n == 0 ? z4 : Z.q1[ab]);
      Z.q2[ab] = mfma4(v, v, n == 0 ? z4 : Z.q2[ab]);
      OPQ(Z.q1[ab]); OPQ(Z.q2[ab]);
      SB();
    }
    if (LAT && (n & 1)) { B[n - 1] = lo_read(C, (S + 2) & 3, n - 1); B[n] = lo_read(C, (S + 2) & 3, n); SB(); }
  }
  TSC(4);
  __builtin_amdgcn_s_setprio(PRIO_G);
  f32x4 G[3];
#pragma unroll
  for (int n = 0; n < 18; ++n) {
    if (LAT) { G[n % 3] = mfma32(P.W[(n % 3) * 6 + n / 3], B[n / 3], n < 3 ? z4 : G[n % 3]); OPQ(G[n % 3]); SB(); }
    if (RD && n % 2 == 0 && n < 12) P.lat[n >> 2][(n >> 1) & 1] = lat_read(C, ((S + 1) & 1) * 12288, n >> 1);
    stat_op(Z, C, n, S);
    SB();
  }
  TSC(5);
  if (LAT) lat_write(G, C, (S & 1) * 12288);
  TSC(6);
}

struct Cons {
  f32x16 U[6];
  u32x4 Ab[12];
  float mrun, lsum;
};
struct ConsC { lds_char* smem; unsigned xr[4], xt[2][2], ra; };
template <int SL> DEV u32x4 row_read(const ConsC& C, int s) { return *(lds_u32x4*)(C.smem + C.xr[s & 3] + (s >> 2) * 8192 + SL * 24576); }
template <int SL> DEV u32x4 col_read(const ConsC& C, int o) {
  const int ks = o / 6, ct = o % 6;
  const u32x2 a0 = trread(C.smem + C.xt[ct & 1][0] + (ct >> 1) * 8192 + ks * 2048 + SL * 24576);
  const u32x2 a1 = trread(C.smem + C.xt[ct & 1][1] + (ct >> 1) * 8192 + ks * 2048 + SL * 24576);
  return u32x4{a0[0], a0[1], a1[0], a1[1]};
}
template <int SL> DEV void cstep(Cons& K, const ConsC& C, bool first BARARGS) {
  TSC(0);
  u32x4 Xr[12], X[12];
  f32x4 rr[4];
#pragma unroll
  for (int s = 0; s < 4; ++s) { Xr[s] = row_read<SL>(C, s); SB(); }
  f32x16 S = {0.f, 0.f, 0.f, 0.f, 0.f, 0.f, 0.f, 0.f, 0.f, 0.f, 0.f, 0.f, 0.f, 0.f, 0.f, 0.f};
#pragma unroll
  for (int s = 0; s < 12; ++s) {
    S = mfma3232(Xr[s], K.Ab[s], S);
    OPQ(S);
    SB();
    if (s == 0) TSC(1);
    if (s == 11) TSC(2);
    if (s < 8) Xr[s + 4] = row_read<SL>(C, s + 4);
    else rr[s - 8] = *(lds_f32x4*)(C.smem + C.ra + (s - 8) * 32 + SL * 256);
    SB();
    if (s >= 6) X[s - 6] = col_read<SL>(C, s - 6);
    SB();
  }
  float V[16];
  float vmax = -3e38f;
#pragma unroll
  for (int e = 0; e < 16; ++e) { V[e] = cut(S[e] * rr[e >> 2][e & 3] - K.mrun); vmax = __builtin_fmaxf(vmax, V[e]); }
  if (first || __builtin_amdgcn_ballot_w64(!(vmax <= THR)) != 0) {
    auto t = __builtin_amdgcn_permlane32_swap(f2u(vmax), f2u(vmax), false, false);
    float dl = __builtin_fmaxf(u2f(t[0]), u2f(t[1]));
    if (!first) dl = __builtin_fmaxf(dl, 0.f);
    const float al = __builtin_amdgcn_exp2f(__builtin_fminf(-dl, 0.f));
    K.mrun += dl; K.lsum *= al;
#pragma unroll
    for (int c = 0; c < 6; ++c) K.U[c] *= al;
#pragma unroll
    for (int e = 0; e < 16; ++e) V[e] -= dl;
  }
  TSC(3);
  u32x4 Pb[2];
  float pe[16];
#pragma unroll
  for (int e = 0; e < 8; ++e) pe[e] = __builtin_amdgcn_exp2f(V[e]);
#pragma unroll
  for (int e = 0; e < 8; e += 2) Pb[0][e >> 1] = cvtpk(cut(pe[e] * rr[e >> 2][e & 3]), cut(pe[e + 1] * rr[e >> 2][(e & 3) + 1]));
  SB();
  TSC(4);
#pragma unroll
  for (int ct = 0; ct < 6; ++ct) {
    K.U[ct] = mfma3232(X[ct], Pb[0], K.U[ct]);
    OPQ(K.U[ct]);
    SB();
    X[6 + ct] = col_read<SL>(C, 6 + ct);
    SB();
    if (ct < 4) {
      const int e = 8 + 2 * ct;
      pe[e] = __builtin_amdgcn_exp2f(V[e]); pe[e + 1] = __builtin_amdgcn_exp2f(V[e + 1]);
      Pb[1][ct] = cvtpk(cut(pe[e] * rr[e >> 2][e & 3]), cut(pe[e + 1] * rr[e >> 2][(e & 3) + 1]));
    } else {
      const int e = 4 * (ct - 4);
      K.lsum += cut(pe[e] + pe[e + 1]) + cut(pe[e + 2] + pe[e + 3]);
    }
    SB();
  }
  TSC(5);
#pragma unroll
  for (int ct = 0; ct < 6; ++ct) {
    K.U[ct] = mfma3232(X[6 + ct], Pb[1], K.U[ct]);
    OPQ(K.U[ct]);
    SB();
    if (ct < 2) { const int e = 8 + 4 * ct; K.lsum += cut(pe[e] + pe[e + 1]) + cut(pe[e + 2] + pe[e + 3]); }
    SB();
  }
  TSC(6);
}
DEV void cfinish(Cons& K, lds_char* smem, int h, int b, rsrc_t rws, unsigned ek BARARGS) {
  const int lane = laneid(), i = lane & 15, g = lane >> 4;
  const int xo = (h >> 1) ? LDS_LAT : LDS_X, PH = h & 1;
  f32x4 Uo[12];
  lds_char* ex = smem + xo + PH * 12288 + lane * 16;
#pragma unroll
  for (int ct = 0; ct < 6; ++ct)
#pragma unroll
    for (int a = 0; a < 2; ++a) {
      f32x4 k0, k1;
#pragma unroll
      for (int r = 0; r < 4; ++r) {
        auto sw = __builtin_amdgcn_permlane16_swap(f2u(K.U[ct][8 * a + r]), f2u(K.U[ct][8 * a + 4 + r]), false, false);
        k0[r] = u2f(sw[0]); k1[r] = u2f(sw[1]);
      }
      Uo[2 * ct + a] = k0;
      *(lds_f32x4*)(ex + (2 * ct + a) * 1024) = k1;
      SB();
    }
  TSX(51);
  float mo, lo_;
  {
    auto t = __builtin_amdgcn_permlane32_swap(f2u(K.lsum), f2u(K.lsum), false, false);
    const float l = u2f(t[0]) + u2f(t[1]);
    auto sl = __builtin_amdgcn_permlane16_swap(f2u(l), f2u(l), false, false);
    auto sm = __builtin_amdgcn_permlane16_swap(f2u(K.mrun), f2u(K.mrun), false, false);
    mo = u2f(sm[0]); lo_ = u2f(sl[0]);
    *(__attribute__((address_space(3))) f32x2*)(smem + LDS_ML + h * 512 + lane * 8) = f32x2{u2f(sm[1]), u2f(sl[1])};
  }
  BAR();
  const f32x2 ml = *(__attribute__((address_space(3))) f32x2*)(smem + LDS_ML + (h ^ 1) * 512 + lane * 8);
  lds_char* ey = smem + xo + (1 - PH) * 12288 + lane * 16;
  f32x4 o[12];
#pragma unroll
  for (int c = 0; c < 12; ++c) o[c] = *(lds_f32x4*)(ey + c * 1024);
  lds_char* wv = smem + LDS_WV + h * 18432 + (8 * (g & 1) + 4 * (g >> 1) + (i >> 2)) * 96 + (i & 3) * 8;
  u32x4 Wv[18];
#pragma unroll
  for (int n = 0; n < 18; ++n) {
    const u32x2 a0 = trread(wv + (n / 3) * 3072 + (n % 3) * 32), a1 = trread(wv + (n / 3) * 3072 + 1536 + (n % 3) * 32);
    Wv[n] = u32x4{a0[0], a0[1], a1[0], a1[1]};
  }
  SB();
  const float mn = __builtin_fmaxf(mo, ml[0]);
  const float fa = __builtin_amdgcn_exp2f(mo - mn), fb = __builtin_amdgcn_exp2f(ml[0] - mn);
  const float l = lo_ * fa + ml[1] * fb, il = __builtin_amdgcn_rcpf(l);
  const unsigned lse = (unsigned)__builtin_amdgcn_ds_bpermute((4 * g + (i & 3)) * 4, (int)f2u(mn + __builtin_amdgcn_logf(l)));
  const f32x2 fa2 = {fa * il, fa * il}, fb2 = {fb * il, fb * il};
  f32x2 u2[24], us[2];
#pragma unroll
  for (int c = 0; c < 24; ++c) {
    const f32x2 own = {Uo[c >> 1][(c & 1) * 2], Uo[c >> 1][(c & 1) * 2 + 1]}, oth = {o[c >> 1][(c & 1) * 2], o[c >> 1][(c & 1) * 2 + 1]};
    u2[c] = pkfma(oth, fb2, pkmul(own, fa2));
    us[c & 1] = c < 2 ? u2[c] : pkadd(us[c & 1], u2[c]);
  }
  TSX(52);
  const float um = xg_sum((us[0][0] + us[0][1]) + (us[1][0] + us[1][1])) * (-1.0f / 192.0f);
  const f32x2 um2 = {um, um};
  const f32x4 z4 = {0.f, 0.f, 0.f, 0.f};
  f32x4 cx[3] = {z4, z4, z4};
#pragma unroll
  for (int s = 0; s < 6; ++s) {
    u32x4 ub;
#pragma unroll
    for (int r = 0; r < 4; ++r) { const f32x2 v = pkadd(u2[4 * s + r], um2); ub[r] = cvtpk(v[0], v[1]); }
#pragma unroll
    for (int dt = 0; dt < 3; ++dt) cx[dt] = mfma32(ub, Wv[3 * s + dt], cx[dt]);
  }
  TSX(53);
  const u32x4 ra = cvt8(cx[0], cx[1]);
  u32x4 rb = {cvtpk(cx[2][0], cx[2][1]), cvtpk(cx[2][2], cx[2][3]), lse, 0u};
  rb[3] = (ra[0] + ra[1] + ra[2]) + (ra[3] + rb[0] + rb[1]) + lse + ek;
  const unsigned vo = (lane >> 2) * 32768 + (lane & 3) * 16, so = WS_REC + h * 524288 + b * 64;
  bst4<SC1>(ra, rws, vo, so);
  bst4<SC1>(rb, rws, vo, so + 16384);
}

struct AIn { u32x4 q[6]; };
DEV void a_issue(AIn& I, unsigned lbase, int h, int cs, const void* q_, const void* wq_, const void* wk_, const void* wm_) {
  const int lane = laneid();
  const rsrc_t rq = mkrsrc(q_, 6144u), rwq = mkrsrc(wq_, 73728u), rwk = mkrsrc(wk_, 73728u), rwm = mkrsrc(wm_, 73728u);
  const unsigned vl = lane * 16;
#pragma unroll
  for (int n = 0; n < 18; ++n) dma16(rwq, vl, h * 18432 + n * 1024, lbase + LDS_AWQ + n * 1024);
  unsigned vo[3];
#pragma unroll
  for (int m = 0; m < 3; ++m) { const unsigned o = m * 1024 + vl; vo[m] = (o / 96) * 384 + o % 96; }
#pragma unroll
  for (int n = 0; n < 18; ++n) dma16(rwm, vo[n % 3], (n / 3) * 12288 + cs * 96, lbase + LDS_AWM + n * 1024);
#pragma unroll
  for (int n = 0; n < 5; ++n) dma16(rwk, vl, h * 18432 + cs * 4608 + n * 1024, lbase + LDS_AWK + n * 1024);
  const unsigned vq = (lane & 15) * 384 + (lane >> 4) * 16;
  ldpl<0>(I.q[0], rq, vq); ldpl<64>(I.q[1], rq, vq); ldpl<128>(I.q[2], rq, vq);
  ldpl<192>(I.q[3], rq, vq); ldpl<256>(I.q[4], rq, vq); ldpl<320>(I.q[5], rq, vq);
}
DEV void a_compute(AIn& I, lds_char* smem, int h, int cs, rsrc_t rws, unsigned ek) {
  const int lane = laneid(), i = lane & 15, g = lane >> 4;
  vmwait<0>();
#pragma unroll
  for (int s = 0; s < 6; ++s) OPQ(I.q[s]);
  SB();
  lds_char* a = smem + (8 * g + (i >> 2)) * 96 + (i & 3) * 8;
  const f32x4 z4 = {0.f, 0.f, 0.f, 0.f};
  f32x4 Q[3], M[3], T[3];
  u32x4 W[18], V[18];
  u32x2 Kf[9];
#pragma unroll
  for (int n = 0; n < 18; ++n) {
    const u32x2 a0 = trread(a + LDS_AWQ + (n / 3) * 3072 + (n % 3) * 32), a1 = trread(a + LDS_AWQ + (n / 3) * 3072 + 384 + (n % 3) * 32);
    W[n] = u32x4{a0[0], a0[1], a1[0], a1[1]};
  }
#pragma unroll
  for (int n = 0; n < 18; ++n) {
    const u32x2 a0 = trread(a + LDS_AWM + (n / 3) * 3072 + (n % 3) * 32), a1 = trread(a + LDS_AWM + (n / 3) * 3072 + 384 + (n % 3) * 32);
    V[n] = u32x4{a0[0], a0[1], a1[0], a1[1]};
  }
#pragma unroll
  for (int n = 0; n < 9; ++n) Kf[n] = *(__attribute__((address_space(3))) u32x2*)(smem + LDS_AWK + i * 96 + g * 8 + (n / 3) * 1536 + (n % 3) * 32);
  SB();
#pragma unroll
  for (int n = 0; n < 18; ++n) Q[n % 3] = mfma32(W[n], I.q[n / 3], n < 3 ? z4 : Q[n % 3]);
#pragma unroll
  for (int n = 0; n < 18; ++n) M[n % 3] = mfma32(V[n], I.q[n / 3], n < 3 ? z4 : M[n % 3]);
#pragma unroll
  for (int dt = 0; dt < 3; ++dt) {
    const u32x2 qb = {cvtpk(Q[dt][0], Q[dt][1]), cvtpk(Q[dt][2], Q[dt][3])};
#pragma unroll
    for (int ct = 0; ct < 3; ++ct) T[ct] = mfma16(Kf[3 * ct + dt], qb, dt == 0 ? z4 : T[ct]);
  }
  const float s1 = 1.4426950408889634f * 0.14433756729740643f;
  unsigned P[6];
#pragma unroll
  for (int n = 0; n < 6; ++n)
    P[n] = cvtpk((T[n >> 1][(n & 1) * 2] + 0.5f * M[n >> 1][(n & 1) * 2]) * s1, (T[n >> 1][(n & 1) * 2 + 1] + 0.5f * M[n >> 1][(n & 1) * 2 + 1]) * s1);
  const unsigned so = WS_A + h * 8192 + cs * 2048;
  bst4<SC1>(u32x4{P[0], P[1], P[2], P[0] + P[1] + P[2] + ek}, rws, lane * 16, so);
  bst4<SC1>(u32x4{P[4], P[5], P[3], P[4] + P[5] + P[3] + ek}, rws, lane * 16 + 1024, so);
}
template <int OFF> DEV void ldsc(u32x4& d, rsrc_t r, unsigned voff, unsigned soff) {
  asm volatile("buffer_load_dwordx4 %0, %1, %2, %3 offen offset:%4 sc1" : "=v"(d) : "v"(voff), "s"(r), "s"(soff), "n"(OFF) : "memory");
}
DEV void a_fetch(u32x4 (&G)[8], int h, rsrc_t rws) {
  const unsigned vo = laneid() * 16, s0 = WS_A + h * 8192, s1 = s0 + 4096;
  ldsc<0>(G[0], rws, vo, s0); ldsc<1024>(G[1], rws, vo, s0); ldsc<2048>(G[2], rws, vo, s0); ldsc<3072>(G[3], rws, vo, s0);
  ldsc<0>(G[4], rws, vo, s1); ldsc<1024>(G[5], rws, vo, s1); ldsc<2048>(G[6], rws, vo, s1); ldsc<3072>(G[7], rws, vo, s1);
}
DEV void a_poll(u32x4 (&G)[8], int h, rsrc_t rws, unsigned ek BARARGS) {
  for (int spin = 0;; ++spin) {
    VALX(46, spin);
    if (spin) a_fetch(G, h, rws);
    vmwait<0>();
#pragma unroll
    for (int n = 0; n < 8; ++n) OPQ(G[n]);
    bool bad = false;
#pragma unroll
    for (int n = 0; n < 8; ++n) bad |= G[n][0] + G[n][1] + G[n][2] + ek != G[n][3];
    if (__builtin_amdgcn_ballot_w64(bad) == 0 || spin >= SPIN_MAX) break;
  }
  RTX(45);
}
DEV void a_centre(const u32x4 (&G)[8], lds_char* smem, int h) {
  const int lane = laneid(), i = lane & 15, g = lane >> 4;
  float sm[2] = {0.f, 0.f};
#pragma unroll
  for (int n = 0; n < 24; ++n) sm[n & 1] = dot2(G[n / 3][n % 3], 0x3F803F80u, sm[n & 1]);
  const float mu = xg_sum(sm[0] + sm[1]) * (-1.0f / 192.0f);
  const f32x4 mu4 = {mu, mu, mu, mu};
  const int d = lane & 3;
  const u32x2 I4 = {d == 0 ? 0x00003F80u : (d == 1 ? 0x3F800000u : 0u), d == 2 ? 0x00003F80u : (d == 3 ? 0x3F800000u : 0u)};
  lds_char* dst = smem + LDS_AF + (h >> 1) * 12288 + (g >> 1) * 512 + ((h & 1) * 16 + i) * 16 + (g & 1) * 8;
#pragma unroll
  for (int s = 0; s < 12; ++s) {
    const int cs = s / 3, ct = s % 3;
    const u32x2 v = ct == 0 ? u32x2{G[2 * cs][0], G[2 * cs][1]} : (ct == 1 ? u32x2{G[2 * cs][2], G[2 * cs + 1][2]} : u32x2{G[2 * cs + 1][0], G[2 * cs + 1][1]});
    const f32x4 c = mfma4(I4, v, mu4);
    *(__attribute__((address_space(3))) u32x2*)(dst + s * 1024) = u32x2{cvtpk(c[0], c[1]), cvtpk(c[2], c[3])};
  }
}
DEV float quads_sum(float x) { x += dppmov<0x124>(x); return x + dppmov<0x128>(x); }
DEV float quads_max(float x) { x = __builtin_fmaxf(x, dppmov<0x124>(x)); return __builtin_fmaxf(x, dppmov<0x128>(x)); }
DEV void reduce1(lds_char* smem, int r, int p, rsrc_t rws, unsigned ek BARARGS) {
  const int lane = laneid(), i = lane & 15, g = lane >> 4, d = lane & 3;
  const unsigned m0 = d == 0 ? 0x0000FFFFu : (d == 1 ? 0xFFFF0000u : 0u), m1 = d == 2 ? 0x0000FFFFu : (d == 3 ? 0xFFFF0000u : 0u);
  u32x4 RA[4], RB[4];
  const unsigned so = WS_REC + r * 32768 + p * 4096;
  RTX(44);
  for (int spin = 0;; ++spin) {
    VALX(46, spin);
    asm volatile("" ::: "memory");
#pragma unroll
    for (int j = 0; j < 4; ++j) { RB[j] = bld4<SC1>(rws, lane * 16 + j * 1024, so + 16384); RA[j] = bld4<SC1>(rws, lane * 16 + j * 1024, so); }
    bool bad = false;
#pragma unroll
    for (int j = 0; j < 4; ++j) bad |= (RA[j][0] + RA[j][1] + RA[j][2]) + (RA[j][3] + RB[j][0] + RB[j][1]) + RB[j][2] + ek != RB[j][3];
    if (__builtin_amdgcn_ballot_w64(bad) == 0 || spin >= SPIN_MAX) break;
  }
  RTX(45);
  const float M = xg_max(quads_max(__builtin_fmaxf(__builtin_fmaxf(u2f(RB[0][2]), u2f(RB[1][2])), __builtin_fmaxf(u2f(RB[2][2]), u2f(RB[3][2])))));
  const f32x4 z4 = {0.f, 0.f, 0.f, 0.f};
  f32x4 acc[3] = {z4, z4, z4};
  float L = 0.f;
#pragma unroll
  for (int j = 0; j < 4; ++j) {
    const float wf = __builtin_amdgcn_exp2f(u2f(RB[j][2]) - M);
    const unsigned wb = cvtpk(wf, wf);
    L += u2f(wb & 0xFFFF0000u);
    const u32x2 dg = {wb & m0, wb & m1};
    acc[0] = mfma4(dg, u32x2{RA[j][0], RA[j][1]}, acc[0]);
    acc[1] = mfma4(dg, u32x2{RA[j][2], RA[j][3]}, acc[1]);
    acc[2] = mfma4(dg, u32x2{RB[j][0], RB[j][1]}, acc[2]);
  }
  L = xg_sum(quads_sum(L));
  float u[6];
  f32x4 v;
#pragma unroll
  for (int k = 0; k < 6; ++k) {
    auto t = __builtin_amdgcn_permlane32_swap(f2u(acc[k % 3][k / 3]), f2u(acc[k % 3][2 + k / 3]), false, false);
    u[k] = u2f(t[0]) + u2f(t[1]);
  }
#pragma unroll
  for (int k = 0; k < 3; ++k) {
    auto t = __builtin_amdgcn_permlane16_swap(f2u(u[k]), f2u(u[k + 3]), false, false);
    v[k] = quads_sum(u2f(t[0]) + u2f(t[1]));
  }
  v[3] = 0.f;
  if (i < 4) *(lds_f32x4*)(smem + LDS_RED + p * 256 + g * 64 + d * 16) = v;
  if (lane < 4) *(__attribute__((address_space(3))) f32x2*)(smem + LDS_RED + 1024 + p * 32 + d * 8) = f32x2{M, L};
  bar();
  if (p == 0) {
    const f32x4 x = *(lds_f32x4*)(smem + LDS_RED + (i >> 2) * 256 + g * 64 + d * 16);
    const f32x2 ml = *(__attribute__((address_space(3))) f32x2*)(smem + LDS_RED + 1024 + (i >> 2) * 32 + g * 8);
    const float f = __builtin_amdgcn_exp2f(ml[0] - quads_max(ml[0]));
    const float il = __builtin_amdgcn_rcpf(quads_sum(ml[1] * f));
    u32x4 o;
#pragma unroll
    for (int k = 0; k < 3; ++k) { const float c = quads_sum(x[k] * f) * il; o[k] = cvtpk(c, dppmov<0xB1>(c)); }
    o[3] = o[0] + o[1] + o[2] + ek;
    if (i == 0 || i == 2) bst4<SC1>(o, rws, (d >> 1) * 64 + g * 16, WS_CTX + r * 128);
  }
}
DEV void reduce2_issue(u32x2& qr, unsigned lbase, int nt, const void* q_, const void* wo_) {
  const int lane = laneid();
  const rsrc_t rq = mkrsrc(q_, 6144u), rwo = mkrsrc(wo_, 73728u);
  const unsigned vo = (lane >> 1) * 384 + (lane & 1) * 16, vq = (lane & 15) * 384 + (lane >> 4) * 8;
#pragma unroll
  for (int n = 0; n < 6; ++n) dma16(rwo, vo, n * 12288 + nt * 32, lbase + LDS_WO + n * 1024);
  asm volatile("buffer_load_dwordx2 %0, %1, %2, %3 offen" : "=v"(qr) : "v"(vq), "s"(rq), "s"(nt * 32) : "memory");
}
DEV void reduce2(u32x2& qr, lds_char* smem, int nt, rsrc_t rws, unsigned ek, void* out_ BARARGS) {
  const int lane = laneid(), i = lane & 15, g = lane >> 4, rho = i >> 2;
  vmwait<0>();
  OPQ(qr);
  SB();
  lds_char* a0 = smem + LDS_WO + (16 * (rho >> 1) + 4 * g + (rho & 1)) * 32 + (i & 3) * 8;
  lds_char* a1 = smem + LDS_WO + (32 + 4 * g + (rho & 1)) * 32 + (i & 3) * 8;
  u32x4 W[8], G[8];
#pragma unroll
  for (int m = 0; m < 8; ++m) {
    const u32x2 x0 = trread(a0 + ((m >> 1) * 48 + 2 * (m & 1)) * 32), x1 = trread(a1 + ((m >> 1) * 48 + 2 * (m & 1)) * 32);
    W[m] = u32x4{x0[0], x0[1], x1[0], x1[1]};
    OPQ(W[m]);
  }
  const unsigned vo = (i >> 2) * 512 + g * 128 + (i & 3) * 16;
  RTX(44);
  for (int spin = 0;; ++spin) {
    VALX(46, spin);
    asm volatile("" ::: "memory");
#pragma unroll
    for (int m = 0; m < 8; ++m) G[m] = bld4<SC1>(rws, vo + (m & 1) * 64 + ((m >> 1) & 1) * 2048, WS_CTX + (m >> 2) * 4096);
    bool bad = false;
#pragma unroll
    for (int m = 0; m < 8; ++m) bad |= G[m][0] + G[m][1] + G[m][2] + ek != G[m][3];
    if (__builtin_amdgcn_ballot_w64(bad) == 0 || spin >= SPIN_MAX) break;
  }
  RTX(45);
  f32x4 c[2] = {{u2f(qr[0] << 16), u2f(qr[0] & 0xFFFF0000u), u2f(qr[1] << 16), u2f(qr[1] & 0xFFFF0000u)}, {0.f, 0.f, 0.f, 0.f}};
#pragma unroll
  for (int m = 0; m < 8; ++m) { G[m][3] = 0u; c[m & 1] = mfma32(W[m], G[m], c[m & 1]); }
  const f32x4 o = c[0] + c[1];
  __builtin_amdgcn_raw_buffer_store_b64(u32x2{cvtpk(o[0], o[1]), cvtpk(o[2], o[3])}, mkrsrc(out_, 6144u), i * 384 + g * 8, nt * 32, 0);
  if (nt == 0 && lane == 0) __builtin_amdgcn_raw_buffer_store_b32(ek - KCHK + 1u, rws, 0, 0, 0);
}

__global__ void __launch_bounds__(512, 2)
m2f_main(const void* __restrict__ fhi_, const void* __restrict__ flo_, const void* __restrict__ wlat_,
         const void* __restrict__ wv_, const void* __restrict__ q_, const void* __restrict__ wq_, const void* __restrict__ wk_,
         const void* __restrict__ wm_, const void* __restrict__ wo_, void* __restrict__ ws_, void* __restrict__ out_) {
  __shared__ __attribute__((aligned(16))) char smem_[LDS_TOTAL];
  lds_char* smem = (lds_char*)smem_;
  const int w = __builtin_amdgcn_readfirstlane(__builtin_amdgcn_workitem_id_x() >> 6);
  const int b = __builtin_amdgcn_workgroup_id_x();
  TSINIT();
  PRB0();

  if (w < 4) {
    const int p = w, lane = laneid(), i = lane & 15, g = lane >> 4;
    __builtin_amdgcn_s_setprio(PRIO_P);
    const rsrc_t rwl = mkrsrc(wlat_, 73728u);
    ProdC C;
    C.smem = smem;
    C.rhi = mkrsrc(fhi_, 100663296u);
    C.rlo = mkrsrc(flo_, 25165824u);
    C.rwv = mkrsrc(wv_, 73728u);
    const unsigned j = lane >> 3, c7 = lane & 7;
    const unsigned lbase = (unsigned)(uintptr_t)smem;
    C.voH = j * 384 + c7 * 16;
    C.vw = lane * 16;
    C.lds0 = lbase + LDS_LO + p * 1536;
    C.ldsv = lbase + LDS_WV;
#pragma unroll
    for (int k = 0; k < 2; ++k) {
      const unsigned o = k * 1024 + (lane & (k ? 31 : 63)) * 16, px = o / 384, pc = (o % 384) / 16;
      C.vd[k] = px * 384 + ((pc ^ (2 * p + (px >> 1))) * 16);
    }
    const unsigned soL = (unsigned)(b * 256 + p * 4) * 384u;
    const unsigned soH = (unsigned)(b * 1024 + p * 8) * 384u;
    Prod P;
    dma24(C.rlo, C.vd[0], C.vd[1], soL, C.lds0);
    dma24(C.rlo, C.vd[0], C.vd[1], soL + 6144, C.lds0 + 6144);
    SB();
    if (b < 16) bar();
    {
      unsigned vo[6];
#pragma unroll
      for (int m = 0; m < 6; ++m) {
        const unsigned o = m * 1024 + lane * 16, k = o / 384, pc = (o % 384) / 16;
        vo[m] = k * 384 + ((pc ^ (2 * (((k >> 1) & 1) | ((k >> 3) << 1)))) * 16);
      }
      const unsigned so1 = soH + 196608, wo = p * 18432, wl = lbase + LDS_WL + p * 18432;
#define WGRP(M) ld_hi1<M>(P.ring[0][M], C, soH, so1); if (M < NH1) ld_hi1<M>(P.ring[1][M], C, soH + 12288, so1 + 12288); \
      for (int n = 3 * M; n < 3 * M + 3; ++n) dma16(rwl, vo[n % 6], wo + (n / 6) * 6144, wl + n * 1024);
      WGRP(0) WGRP(1) WGRP(2) WGRP(3) WGRP(4) WGRP(5)
#undef WGRP
#define HREST(M) if (M >= NH1) ld_hi1<M>(P.ring[1][M], C, soH + 12288, so1 + 12288);
      HREST(0) HREST(1) HREST(2) HREST(3) HREST(4) HREST(5)
#undef HREST
    }
    SB();
    dma24(C.rlo, C.vd[0], C.vd[1], soL + 2 * 6144, C.lds0 + 2 * 6144);
    dma24(C.rlo, C.vd[0], C.vd[1], soL + 3 * 6144, C.lds0 + 3 * 6144);
    ld_hi(P.ring[2], C, soH + 2 * 12288);
    SB();
    {
      const int d = lane & 3;
      C.I4[0] = d == 0 ? 0x00003F80u : (d == 1 ? 0x3F800000u : 0u);
      C.I4[1] = d == 2 ? 0x00003F80u : (d == 3 ? 0x3F800000u : 0u);
#pragma unroll
      for (int ab = 0; ab < 2; ++ab) {
        C.xw[ab] = LDS_X + (16 * p + 8 * ab + j) * 128 + ((c7 ^ (((j >> 1) & 1) * 4 + 2 * ab + (j >> 2))) * 16);
        C.br[ab] = LDS_LO + i * 384 + (((4 * ab + g) ^ (i >> 1)) * 16);
        C.lw[ab] = LDS_LAT + i * 768 + (((12 * p + 4 * ab + g) ^ (i & 7)) * 16);
        C.lr[ab] = LDS_LAT + (4 * p + (j >> 1)) * 768 + (((2 * c7 + ab) ^ ((4 * p + (j >> 1)) & 7)) * 16);
      }
      C.odd = lane & 1;
      C.rw = c7 < 2 ? LDS_R + (16 * p + 8 * (lane & 1) + j) * 4 : LDS_ML + lane * 4;
#pragma unroll
      for (int ab = 0; ab < 2; ++ab) { OPQ(C.xw[ab]); OPQ(C.br[ab]); OPQ(C.lw[ab]); OPQ(C.lr[ab]); OPQ(C.vd[ab]); }
      OPQ(C.rw); OPQ(C.vw);
    }
    SB();
    unsigned ek = epoch(ws_);
    SB();
    vmwait<16 - NH1>();
    asm volatile("" : "+s"(ek));
    BAR();
    PRB(1);
    {
      const unsigned rp = i >> 2, cp = i & 3, s = (rp >> 1) | ((g & 1) << 1);
      u32x4 B[12];
#pragma unroll
      for (int n = 0; n < 12; ++n) B[n] = lo_read(C, n / 6, n % 6);
#pragma unroll
      for (int ks = 0; ks < 6; ++ks)
#pragma unroll
        for (int tt = 0; tt < 3; ++tt) {
          const unsigned ct = 3 * p + tt;
          lds_char* a = smem + LDS_WL + (8 * g + rp) * 384 + (((ct & 3) ^ s) * 32) + cp * 8 + (ct >> 2) * 128;
          const u32x2 a0 = trread(a + ks * 12288), a1 = trread(a + ks * 12288 + 1536);
          P.W[tt * 6 + ks] = u32x4{a0[0], a0[1], a1[0], a1[1]};
        }
      SB();
      const f32x4 z4 = {0.f, 0.f, 0.f, 0.f};
      f32x4 G0[3], G1[3];
#pragma unroll
      for (int n = 0; n < 18; ++n) {
        G0[n % 3] = mfma32(P.W[(n % 3) * 6 + n / 3], B[n / 3], n < 3 ? z4 : G0[n % 3]);
        G1[n % 3] = mfma32(P.W[(n % 3) * 6 + n / 3], B[6 + n / 3], n < 3 ? z4 : G1[n % 3]);
        OPQ(G0[n % 3]); OPQ(G1[n % 3]);
        SB();
      }
      lat_write(G0, C, LDS_LT0 - LDS_LAT);
      lat_write(G1, C, 12288);
    }
    vmwait<8>();
    BAR();
    PRB(2);
#pragma unroll
    for (int k = 0; k < 6; ++k) P.lat[k >> 1][k & 1] = lat_read(C, LDS_LT0 - LDS_LAT, k);
    dma24(C.rlo, C.vd[0], C.vd[1], soL + 4 * 6144, C.lds0);
    ld_hi(P.ring[3], C, soH + 3 * 12288);
    SB();
#pragma unroll 1
    for (int u = 0; u < 3; ++u) {
      const unsigned so = soH + (u + 1) * 49152, sl = soL + 6144 + (u + 1) * 24576, wv = p * 1024 + u * 20480;
      pstep<0, true, true, true, 1, -1>(P, C, true, so, sl, wv BARPASS); vmwait<23>(); BAR(); if (u == 0) PRB(3);
      pstep<1, true, true, true, 1, -1>(P, C, true, so + 12288, sl + 6144, wv + 4096 BARPASS); vmwait<24>(); BAR(); if (u == 0) PRB(4);
      pstep<2, true, true, true, 1, -1>(P, C, true, so + 2 * 12288, sl + 2 * 6144, wv + 2 * 4096 BARPASS); vmwait<25>(); BAR();
      pstep<3, true, true, true, 2, -1>(P, C, u < 2, so + 3 * 12288, sl + 3 * 6144, wv + 3 * 4096 BARPASS); vmwait<24>(); BAR();
    }
    pstep<0, false, true, true, 3, -1>(P, C, false, 0, 0, p * 1024 + 61440 BARPASS); vmwait<18>(); BAR();
    pstep<1, false, true, true, 0, -1>(P, C, false, 0, 0, 0 BARPASS); BAR();
    pstep<2, false, false, true, 0, 17>(P, C, false, 0, 0, 0 BARPASS); BAR();
    pstep<3, false, false, false, 0, 10>(P, C, false, 0, 0, 0 BARPASS); vmwait<0>(); BAR();
    PRB(6);
    const bool l2 = p == 0 && b >= L2_B0 && b < L2_B0 + 12;
    u32x2 qr;
    if (l2) reduce2_issue(qr, lbase, b - L2_B0, q_, wo_);
    BAR();
    PRB(7);
    if (b >= L1_B0 && b < L1_B0 + 64) reduce1(smem, b - L1_B0, p, mkrsrc(ws_, WS_BYTES), ek BARPASS);
    if (l2) reduce2(qr, smem, b - L2_B0, mkrsrc(ws_, WS_BYTES), ek, out_ BARPASS);
    PRB(9);
    PRBEND();
  } else {
    const int h = w - 4, hp = h >> 1, ph = h & 1, lane = laneid(), i = lane & 15, g = lane >> 4;
    const rsrc_t rws = mkrsrc(ws_, WS_BYTES);
    Cons K;
#define PRIO_C 2
    __builtin_amdgcn_s_setprio(PRIO_C);
    unsigned ek, pf;
    {
      const unsigned n = ((b >> 3) - 2) * 4 + h, h0 = (b & 7) >> 2, cs = b & 3;
      const void* src = n < 10 ? wq_ : (n < 16 ? wm_ : (n < 20 ? wk_ : q_));
      const unsigned so = n < 5 ? h0 * 18432 + n * 4096 : (n < 10 ? h0 * 18432 + 36864 + (n - 5) * 4096 : (n < 16 ? (n - 10) * 12288 + ((cs * 96) >> 6) * 64
                        : (n < 20 ? h0 * 18432 + ((n >> 1) & 1) * 36864 + cs * 4608 + (n & 1) * 4096 : (n & 1) * 4096)));
      const unsigned vo = n >= 10 && n < 16 ? (lane >> 1) * 384 + (lane & 1) * 64 : lane * 64;
      asm volatile("buffer_load_dword %0, %1, %2, %3 offen" : "=v"(pf) : "v"(vo), "s"(mkrsrc(src, n < 20 ? 73728u : (n < 22 ? 6144u : 0u))), "s"(so) : "memory");
    }
    if (b < 16 && h == 0) {
      AIn I;
      a_issue(I, (unsigned)(uintptr_t)smem, b >> 2, b & 3, q_, wq_, wk_, wm_);
      ek = epoch(ws_);
      asm volatile("s_barrier" ::: "memory");
      RTX(47);
      a_compute(I, smem, b >> 2, b & 3, rws, ek);
      RTX(48);
    } else {
      if (b < 16) bar();
      ek = epoch(ws_);
    }
    asm volatile("" : "+s"(ek));
    u32x4 G[8];
    BAR();
    RTX(44);
    a_fetch(G, h, rws);
    BAR();
    a_poll(G, h, rws, ek BARPASS);
    OPQ(pf);
    a_centre(G, smem, h);
    ConsC C;
    C.smem = smem;
    const unsigned hl = lane >> 5;
    {
      const unsigned m = lane & 31, row = 32 * ph + m, f = ((m >> 1) & 1) * 4 + ((m >> 2) & 3);
#pragma unroll
      for (int s = 0; s < 4; ++s) C.xr[s] = LDS_X + row * 128 + (((2 * s + hl) ^ f) * 16);
      const unsigned cb = g & 1, rp = i >> 2, cp = i & 3;
#pragma unroll
      for (int c = 0; c < 2; ++c)
#pragma unroll
        for (int sec = 0; sec < 2; ++sec)
          C.xt[c][sec] = LDS_X + (32 * ph + 8 * sec + 4 * hl + rp) * 128 + (((4 * c + 2 * cb + (cp >> 1)) ^ ((rp >> 1) * 4 + hl + 2 * sec)) * 16) + (cp & 1) * 8;
      C.ra = LDS_R + (32 * ph + 4 * hl) * 4;
#pragma unroll
      for (int s = 0; s < 4; ++s) { OPQ(C.xr[s]); OPQ(C.xt[s >> 1][s & 1]); }
      OPQ(C.ra);
    }
#pragma unroll
    for (int c = 0; c < 6; ++c)
#pragma unroll
      for (int e = 0; e < 16; ++e) K.U[c][e] = 0.f;
    K.mrun = 0.f; K.lsum = 0.f;
#pragma unroll 1
    for (int u = 0; u < 8; ++u) {
      BAR();
      if (u == 0) {
        lds_char* af = smem + LDS_AF + hp * 12288 + (laneid() ^ (16 * ph)) * 16;
#pragma unroll
        for (int s = 0; s < 12; ++s) K.Ab[s] = *(lds_u32x4*)(af + s * 1024);
        SB();
      }
      cstep<0>(K, C, u == 0 BARPASS);
      BAR();
      cstep<1>(K, C, false BARPASS);
    }
    TSX(50);
    cfinish(K, smem, h, b, rws, ek BARPASS);
  }
  TSFIN();
}

extern "C" void kernel_launch(void* const* d_in, const int* in_sizes, int n_in, void* d_out, int out_size, void* d_ws,
                              size_t ws_size, hipStream_t stream) {
  m2f_main<<<256, 512, 0, stream>>>(d_in[1], d_in[2], d_in[3], d_in[6], d_in[0], d_in[4], d_in[5], d_in[8], d_in[7], d_ws, d_out);
}
```

```cpp
#include <hip/hip_runtime.h>
#include <stdint.h>

typedef __attribute__((ext_vector_type(8))) __bf16 bf16x8;
typedef __attribute__((ext_vector_type(2))) __bf16 bf16x2;
typedef __attribute__((ext_vector_type(4))) short s16x4;
typedef __attribute__((ext_vector_type(4))) float f32x4;
typedef __attribute__((ext_vector_type(2))) float f32x2;
typedef __attribute__((ext_vector_type(4))) unsigned u32x4;
typedef __attribute__((ext_vector_type(2))) unsigned u32x2;
typedef __attribute__((address_space(3))) char lds_char;
typedef __attribute__((address_space(3))) s16x4 lds_s16x4;
typedef __attribute__((address_space(3))) u32x4 lds_u32x4;
typedef __attribute__((address_space(3))) f32x4 lds_f32x4;
typedef __attribute__((address_space(3))) float lds_f32;

typedef __attribute__((ext_vector_type(16))) float f32x16;
#define DEV __device__ __forceinline__
#define SB() __builtin_amdgcn_sched_barrier(0)
#define OPQ(x) asm volatile("" : "+v"(x))

constexpr int LDS_X = 0;
constexpr int LDS_WL = 24576;
constexpr int LDS_WV = 49152;
constexpr int LDS_LO = 110592;
constexpr int LDS_LAT = 135168;
constexpr int LDS_R = 159744;
constexpr int LDS_ML = 160256;
constexpr int LDS_TOTAL = 162304;
constexpr int LDS_AF = 73728;
constexpr int LDS_LT0 = 98304;
constexpr int LDS_AWQ = 0, LDS_AWM = LDS_LAT, LDS_AWK = LDS_LAT + 18432;
constexpr int LDS_WO = LDS_LO + 2 * 6144, LDS_RED = LDS_LO + 3 * 6144;
constexpr float THR = 16.0f;
constexpr unsigned WS_A = 4096, WS_REC = 65536, WS_CTX = WS_REC + 64 * 32768, WS_BYTES = WS_CTX + 8192;
constexpr unsigned KCHK = 0x9E3779B9u;
constexpr int SC1 = 16, SPIN_MAX = 1 << 15;
constexpr int L1_B0 = 64, L2_B0 = 128;
#define PRIO_P 0
#define PRIO_A 3
#define PRIO_G 3
#define NH1 6

DEV float cut(float x) { asm("" : "+v"(x)); return x; }
DEV unsigned cvtpk(float lo, float hi) {
  f32x2 v = {lo, hi};
  return __builtin_bit_cast(unsigned, __builtin_convertvector(v, bf16x2));
}
DEV u32x4 cvt8(f32x4 a, f32x4 b) { return u32x4{cvtpk(a[0], a[1]), cvtpk(a[2], a[3]), cvtpk(b[0], b[1]), cvtpk(b[2], b[3])}; }
DEV f32x4 mfma32(u32x4 a, u32x4 b, f32x4 c) {
  return __builtin_amdgcn_mfma_f32_16x16x32_bf16(__builtin_bit_cast(bf16x8, a), __builtin_bit_cast(bf16x8, b), c, 0, 0, 0);
}
DEV f32x16 mfma3232(u32x4 a, u32x4 b, f32x16 c) {
  return __builtin_amdgcn_mfma_f32_32x32x16_bf16(__builtin_bit_cast(bf16x8, a), __builtin_bit_cast(bf16x8, b), c, 0, 0, 0);
}
DEV unsigned f2u(float x) { return __builtin_bit_cast(unsigned, x); }
DEV float u2f(unsigned x) { return __builtin_bit_cast(float, x); }
DEV f32x4 mfma16(u32x2 a, u32x2 b, f32x4 c) {
  return __builtin_amdgcn_mfma_f32_16x16x16bf16_1k(__builtin_bit_cast(s16x4, a), __builtin_bit_cast(s16x4, b), c, 0, 0, 0);
}
DEV f32x4 mfma4(u32x2 a, u32x2 b, f32x4 c) {
  return __builtin_amdgcn_mfma_f32_4x4x4bf16_1k(__builtin_bit_cast(s16x4, a), __builtin_bit_cast(s16x4, b), c, 0, 0, 0);
}
DEV u32x2 trread(lds_char* p) {
  return __builtin_bit_cast(u32x2, __builtin_amdgcn_ds_read_tr16_b64_v4i16((lds_s16x4*)p));
}
template <int CTRL, int BANK> DEV unsigned dppu(unsigned old, unsigned src) {
  return (unsigned)__builtin_amdgcn_update_dpp((int)old, (int)src, CTRL, 0xf, BANK, false);
}
template <int CTRL, int BANK> DEV float dppf(float old, float src) {
  return __builtin_bit_cast(float, dppu<CTRL, BANK>(__builtin_bit_cast(unsigned, old), __builtin_bit_cast(unsigned, src)));
}
constexpr int DPP_ID = 0xE4, DPP_ROR8 = 0x128;
DEV float xg_sum(float x) {
  auto s = __builtin_amdgcn_permlane16_swap(f2u(x), f2u(x), false, false);
  x = u2f(s[0]) + u2f(s[1]);
  auto t = __builtin_amdgcn_permlane32_swap(f2u(x), f2u(x), false, false);
  return u2f(t[0]) + u2f(t[1]);
}
DEV float xg_max(float x) {
  auto s = __builtin_amdgcn_permlane16_swap(f2u(x), f2u(x), false, false);
  x = __builtin_fmaxf(u2f(s[0]), u2f(s[1]));
  auto t = __builtin_amdgcn_permlane32_swap(f2u(x), f2u(x), false, false);
  return __builtin_fmaxf(u2f(t[0]), u2f(t[1]));
}
DEV int laneid() {
  int l;
  asm volatile("v_mbcnt_lo_u32_b32 %0, -1, 0\n\tv_mbcnt_hi_u32_b32 %0, -1, %0" : "=v"(l));
  __builtin_assume(l >= 0 && l < 64);
  return l;
}
DEV void bar() { SB(); __builtin_amdgcn_s_waitcnt(0xC07F); asm volatile("s_barrier" ::: "memory"); SB(); }
typedef __amdgpu_buffer_rsrc_t rsrc_t;
DEV rsrc_t mkrsrc(const void* p, unsigned bytes) { return __builtin_amdgcn_make_buffer_rsrc((void*)p, (short)0, (int)bytes, 0x00020000); }
template <int AUX> DEV u32x4 bld4(rsrc_t r, unsigned voff, unsigned soff) { return __builtin_amdgcn_raw_buffer_load_b128(r, voff, soff, AUX); }
template <int AUX> DEV u32x2 bld2(rsrc_t r, unsigned voff, unsigned soff) { return __builtin_amdgcn_raw_buffer_load_b64(r, voff, soff, AUX); }
constexpr int NT = 2;

DEV void dma16(rsrc_t r, unsigned voff, unsigned soff, unsigned ldsdst) {
  asm volatile("s_mov_b32 m0, %0\n\ts_nop 0\n\tbuffer_load_dwordx4 %1, %2, %3 offen lds" :: "s"(ldsdst), "v"(voff), "s"(r), "s"(soff) : "memory");
}
DEV void dma24(rsrc_t r, unsigned v0, unsigned v1, unsigned soff, unsigned ldsdst) {
  asm volatile("s_mov_b32 m0, %0\n\ts_nop 0\n\tbuffer_load_dwordx4 %1, %3, %4 offen nt lds\n\t"
               "s_mov_b32 exec_hi, 0\n\ts_add_u32 m0, m0, 0x400\n\ts_nop 0\n\tbuffer_load_dwordx4 %2, %3, %4 offen nt lds\n\ts_mov_b32 exec_hi, -1"
               :: "s"(ldsdst), "v"(v0), "v"(v1), "s"(r), "s"(soff) : "memory", "scc");
}
template <int OFF> DEV void ldnt(u32x4& d, rsrc_t r, unsigned voff, unsigned soff) {
  asm volatile("buffer_load_dwordx4 %0, %1, %2, %3 offen offset:%4 nt" : "=v"(d) : "v"(voff), "s"(r), "s"(soff), "n"(OFF) : "memory");
}
template <int OFF> DEV void ldpl(u32x4& d, rsrc_t r, unsigned voff) {
  asm volatile("buffer_load_dwordx4 %0, %1, %2, 0 offen offset:%3" : "=v"(d) : "v"(voff), "s"(r), "n"(OFF) : "memory");
}
template <int AUX> DEV void bst4(u32x4 v, rsrc_t r, unsigned voff, unsigned soff) { __builtin_amdgcn_raw_buffer_store_b128(v, r, voff, soff, AUX); }
template <int N> DEV void vmwait() { asm volatile("s_waitcnt vmcnt(%0)" :: "n"(N) : "memory"); }
DEV unsigned epoch(const void* ws) {
  asm volatile("" : "+s"(ws));
  return *(const __attribute__((address_space(4))) unsigned*)ws + KCHK;
}
DEV f32x2 pkmul(f32x2 a, f32x2 b) { return a * b; }
DEV f32x2 pkadd(f32x2 a, f32x2 b) { return a + b; }
DEV f32x2 pkfma(f32x2 a, f32x2 b, f32x2 c) { return __builtin_elementwise_fma(a, b, c); }
DEV float dot2(unsigned a, unsigned b, float c) {
  return __builtin_amdgcn_fdot2_f32_bf16(__builtin_bit_cast(bf16x2, a), __builtin_bit_cast(bf16x2, b), c, false);
}
template <int CTRL> DEV float dppadd(float x) {
  return cut(x + __builtin_bit_cast(float, __builtin_amdgcn_update_dpp(0, __builtin_bit_cast(int, x), CTRL, 0xf, 0xf, true)));
}

struct Prod {
  u32x4 W[18];
  u32x4 ring[4][6];
  f32x4 lat[3][2];
};
struct ProdC {
  lds_char* smem;
  unsigned xw[2], rw, br[2], lw[2], lr[2], vd[2], voH, vw, lds0, ldsv;
  u32x2 I4;
  bool odd;
  rsrc_t rhi, rlo, rwv;
};
DEV u32x4 lo_read(const ProdC& C, int slot, int ks) { return *(lds_u32x4*)(C.smem + C.br[ks & 1] + slot * 6144 + (ks >> 1) * 128); }
DEV void lat_write(const f32x4 (&T)[3], const ProdC& C, int off) {
#pragma unroll
  for (int tt = 0; tt < 3; ++tt) *(lds_f32x4*)(C.smem + C.lw[tt & 1] + off + (tt >> 1) * 128) = T[tt];
}
DEV f32x4 lat_read(const ProdC& C, int off, int k) { return *(lds_f32x4*)(C.smem + C.lr[k & 1] + off + (k >> 1) * 256); }
template <int M> DEV void ld_hi1(u32x4& d, const ProdC& C, unsigned so0, unsigned so1) { ldnt<(M >> 1) * 128>(d, C.rhi, C.voH, (M & 1) ? so1 : so0); }
DEV void ld_hi(u32x4 (&dst)[6], const ProdC& C, unsigned so) {
  const unsigned so1 = so + 196608;
  ld_hi1<0>(dst[0], C, so, so1); ld_hi1<1>(dst[1], C, so, so1); ld_hi1<2>(dst[2], C, so, so1);
  ld_hi1<3>(dst[3], C, so, so1); ld_hi1<4>(dst[4], C, so, so1); ld_hi1<5>(dst[5], C, so, so1);
}
template <int CTRL> DEV float dppmov(float x) { return __builtin_bit_cast(float, __builtin_amdgcn_update_dpp(0, __builtin_bit_cast(int, x), CTRL, 0xf, 0xf, true)); }
struct Stat { f32x4 q2[2], q1[2]; float a0, a1, v, w, mu; };
DEV void stat_op(Stat& Z, const ProdC& C, int n, int slot) {
  if (n == 0) { Z.v = cut(C.odd ? Z.q1[1][0] : Z.q1[0][0]); Z.w = cut(C.odd ? Z.q1[0][0] : Z.q1[1][0]); }
  if (n == 1) Z.a0 = cut(Z.q2[0][0] + dppmov<0x55>(Z.q2[0][1]));
  if (n == 2) Z.a1 = cut(Z.q2[1][1] + dppmov<0x00>(Z.q2[1][0]));
  if (n == 3) Z.v = cut(Z.v + dppmov<0xB1>(Z.w));
  if (n == 4) Z.a0 = cut(Z.a0 + dppmov<0xAA>(Z.q2[0][2]));
  if (n == 5) Z.a1 = cut(Z.a1 + dppmov<0xAA>(Z.q2[1][2]));
  if (n == 6) Z.v = cut(Z.v + dppmov<0x4E>(Z.v));
  if (n == 7) Z.a0 = cut(Z.a0 + dppmov<0xFF>(Z.q2[0][3]));
  if (n == 8) Z.a1 = cut(Z.a1 + dppmov<0xFF>(Z.q2[1][3]));
  if (n == 9) Z.v = cut(Z.v + dppmov<0x104>(Z.v));
  if (n == 10) Z.a0 = cut(C.odd ? Z.a1 : Z.a0);
  if (n == 11) Z.mu = cut(Z.v * (1.0f / 192.0f));
  if (n == 12) Z.a0 = cut(Z.a0 + dppmov<0x104>(Z.a0));
  if (n == 13) Z.w = cut(Z.mu * Z.mu);
  if (n == 14) Z.a0 = cut(__builtin_fmaxf(Z.a0 * (1.0f / 192.0f) - Z.w, 0.0f) + 1e-5f);
  if (n == 15) Z.a0 = cut(__builtin_amdgcn_rsqf(Z.a0));
  if (n == 17) *(lds_f32*)(C.smem + C.rw + (slot & 1) * 256) = Z.a0;
}
template <int S, int M, int WAIT> DEV void padd(Prod& P, const ProdC& C, f32x4 (&d)[6][2]) {
  if (WAIT >= 0) vmwait<(WAIT >= 0 ? WAIT - M : 0)>();
  OPQ(P.ring[S][M]);
  const u32x4 F = P.ring[S][M];
  d[M][0] = mfma4(C.I4, u32x2{F[0], F[1]}, P.lat[M >> 1][0]);
  d[M][1] = mfma4(C.I4, u32x2{F[2], F[3]}, P.lat[M >> 1][1]);
  OPQ(d[M][0]); OPQ(d[M][1]);
  SB();
}
template <int M> DEV void pcvt(const f32x4 (&d)[6][2], u32x4 (&x)[6]) { x[M] = cvt8(d[M][0], d[M][1]); OPQ(x[M]); SB(); }
template <int S> DEV void x_write(const ProdC& C, const u32x4 (&x)[6], int m) {
  *(lds_u32x4*)(C.smem + C.xw[m & 1] + (S & 1) * 24576 + (m >> 1) * 8192) = x[m];
}
template <int S, bool HI, bool LAT, bool RD, int NWV, int WAIT>
DEV void pstep(Prod& P, const ProdC& C, bool lo, unsigned so, unsigned sl, unsigned wv) {
  f32x4 d[6][2];
  u32x4 x[6], B[6];
  __builtin_amdgcn_s_setprio(PRIO_A);
  padd<S, 0, WAIT>(P, C, d); padd<S, 1, WAIT>(P, C, d); padd<S, 2, WAIT>(P, C, d);
  padd<S, 3, WAIT>(P, C, d); padd<S, 4, WAIT>(P, C, d); padd<S, 5, WAIT>(P, C, d);
  __builtin_amdgcn_s_setprio(PRIO_P);
  SB();
  pcvt<0>(d, x); pcvt<1>(d, x); pcvt<2>(d, x); pcvt<3>(d, x); pcvt<4>(d, x); pcvt<5>(d, x);
  if (lo) dma24(C.rlo, C.vd[0], C.vd[1], sl, C.lds0 + ((S + 1) & 3) * 6144);
  if (HI) ld_hi(P.ring[S], C, so);
#pragma unroll
  for (int n = 0; n < NWV; ++n) dma16(C.rwv, C.vw, wv + n * 4096, C.ldsv + wv + n * 4096);
  SB();
  const f32x4 z4 = {0.f, 0.f, 0.f, 0.f};
  const u32x2 ones = {0x3F803F80u, 0x3F803F80u};
  Stat Z;
#pragma unroll
  for (int n = 0; n < 6; ++n) {
    x_write<S>(C, x, n);
    SB();
#pragma unroll
    for (int ab = 0; ab < 2; ++ab) {
      const u32x2 v = {x[(n >> 1) * 2 + ab][(n & 1) * 2], x[(n >> 1) * 2 + ab][(n & 1) * 2 + 1]};
      Z.q1[ab] = mfma4(ones, v, n == 0 ? z4 : Z.q1[ab]);
      Z.q2[ab] = mfma4(v, v, n == 0 ? z4 : Z.q2[ab]);
      OPQ(Z.q1[ab]); OPQ(Z.q2[ab]);
      SB();
    }
    if (LAT && (n & 1)) { B[n - 1] = lo_read(C, (S + 2) & 3, n - 1); B[n] = lo_read(C, (S + 2) & 3, n); SB(); }
  }
  __builtin_amdgcn_s_setprio(PRIO_G);
  f32x4 G[3];
#pragma unroll
  for (int n = 0; n < 18; ++n) {
    if (LAT) { G[n % 3] = mfma32(P.W[(n % 3) * 6 + n / 3], B[n / 3], n < 3 ? z4 : G[n % 3]); OPQ(G[n % 3]); SB(); }
    if (RD && n % 2 == 0 && n < 12) P.lat[n >> 2][(n >> 1) & 1] = lat_read(C, ((S + 1) & 1) * 12288, n >> 1);
    stat_op(Z, C, n, S);
    SB();
  }
  if (LAT) lat_write(G, C, (S & 1) * 12288);
}

struct Cons {
  f32x16 U[6];
  u32x4 Ab[12];
  float mrun, lsum;
};
struct ConsC { lds_char* smem; unsigned xr[4], xt[2][2], ra; };
template <int SL> DEV u32x4 row_read(const ConsC& C, int s) { return *(lds_u32x4*)(C.smem + C.xr[s & 3] + (s >> 2) * 8192 + SL * 24576); }
template <int SL> DEV u32x4 col_read(const ConsC& C, int o) {
  const int ks = o / 6, ct = o % 6;
  const u32x2 a0 = trread(C.smem + C.xt[ct & 1][0] + (ct >> 1) * 8192 + ks * 2048 + SL * 24576);
  const u32x2 a1 = trread(C.smem + C.xt[ct & 1][1] + (ct >> 1) * 8192 + ks * 2048 + SL * 24576);
  return u32x4{a0[0], a0[1], a1[0], a1[1]};
}
template <int SL> DEV void cstep(Cons& K, const ConsC& C, bool first) {
  u32x4 Xr[12], X[12];
  f32x4 rr[4];
#pragma unroll
  for (int s = 0; s < 4; ++s) { Xr[s] = row_read<SL>(C, s); SB(); }
  f32x16 S = {0.f, 0.f, 0.f, 0.f, 0.f, 0.f, 0.f, 0.f, 0.f, 0.f, 0.f, 0.f, 0.f, 0.f, 0.f, 0.f};
#pragma unroll
  for (int s = 0; s < 12; ++s) {
    S = mfma3232(Xr[s], K.Ab[s], S);
    OPQ(S);
    SB();

    if (s < 8) Xr[s + 4] = row_read<SL>(C, s + 4);
    else rr[s - 8] = *(lds_f32x4*)(C.smem + C.ra + (s - 8) * 32 + SL * 256);
    SB();
    if (s >= 6) X[s - 6] = col_read<SL>(C, s - 6);
    SB();
  }
  float V[16];
  float vmax = -3e38f;
#pragma unroll
  for (int e = 0; e < 16; ++e) { V[e] = cut(S[e] * rr[e >> 2][e & 3] - K.mrun); vmax = __builtin_fmaxf(vmax, V[e]); }
  if (first || __builtin_amdgcn_ballot_w64(!(vmax <= THR)) != 0) {
    auto t = __builtin_amdgcn_permlane32_swap(f2u(vmax), f2u(vmax), false, false);
    float dl = __builtin_fmaxf(u2f(t[0]), u2f(t[1]));
    if (!first) dl = __builtin_fmaxf(dl, 0.f);
    const float al = __builtin_amdgcn_exp2f(__builtin_fminf(-dl, 0.f));
    K.mrun += dl; K.lsum *= al;
#pragma unroll
    for (int c = 0; c < 6; ++c) K.U[c] *= al;
#pragma unroll
    for (int e = 0; e < 16; ++e) V[e] -= dl;
  }
  u32x4 Pb[2];
  float pe[16];
#pragma unroll
  for (int e = 0; e < 8; ++e) pe[e] = __builtin_amdgcn_exp2f(V[e]);
#pragma unroll
  for (int e = 0; e < 8; e += 2) Pb[0][e >> 1] = cvtpk(cut(pe[e] * rr[e >> 2][e & 3]), cut(pe[e + 1] * rr[e >> 2][(e & 3) + 1]));
  SB();
#pragma unroll
  for (int ct = 0; ct < 6; ++ct) {
    K.U[ct] = mfma3232(X[ct], Pb[0], K.U[ct]);
    OPQ(K.U[ct]);
    SB();
    X[6 + ct] = col_read<SL>(C, 6 + ct);
    SB();
    if (ct < 4) {
      const int e = 8 + 2 * ct;
      pe[e] = __builtin_amdgcn_exp2f(V[e]); pe[e + 1] = __builtin_amdgcn_exp2f(V[e + 1]);
      Pb[1][ct] = cvtpk(cut(pe[e] * rr[e >> 2][e & 3]), cut(pe[e + 1] * rr[e >> 2][(e & 3) + 1]));
    } else {
      const int e = 4 * (ct - 4);
      K.lsum += cut(pe[e] + pe[e + 1]) + cut(pe[e + 2] + pe[e + 3]);
    }
    SB();
  }
#pragma unroll
  for (int ct = 0; ct < 6; ++ct) {
    K.U[ct] = mfma3232(X[6 + ct], Pb[1], K.U[ct]);
    OPQ(K.U[ct]);
    SB();
    if (ct < 2) { const int e = 8 + 4 * ct; K.lsum += cut(pe[e] + pe[e + 1]) + cut(pe[e + 2] + pe[e + 3]); }
    SB();
  }
}
DEV void cfinish(Cons& K, lds_char* smem, int h, int b, rsrc_t rws, unsigned ek) {
  const int lane = laneid(), i = lane & 15, g = lane >> 4;
  const int xo = (h >> 1) ? LDS_LAT : LDS_X, PH = h & 1;
  f32x4 Uo[12];
  lds_char* ex = smem + xo + PH * 12288 + lane * 16;
#pragma unroll
  for (int ct = 0; ct < 6; ++ct)
#pragma unroll
    for (int a = 0; a < 2; ++a) {
      f32x4 k0, k1;
#pragma unroll
      for (int r = 0; r < 4; ++r) {
        auto sw = __builtin_amdgcn_permlane16_swap(f2u(K.U[ct][8 * a + r]), f2u(K.U[ct][8 * a + 4 + r]), false, false);
        k0[r] = u2f(sw[0]); k1[r] = u2f(sw[1]);
      }
      Uo[2 * ct + a] = k0;
      *(lds_f32x4*)(ex + (2 * ct + a) * 1024) = k1;
      SB();
    }
  float mo, lo_;
  {
    auto t = __builtin_amdgcn_permlane32_swap(f2u(K.lsum), f2u(K.lsum), false, false);
    const float l = u2f(t[0]) + u2f(t[1]);
    auto sl = __builtin_amdgcn_permlane16_swap(f2u(l), f2u(l), false, false);
    auto sm = __builtin_amdgcn_permlane16_swap(f2u(K.mrun), f2u(K.mrun), false, false);
    mo = u2f(sm[0]); lo_ = u2f(sl[0]);
    *(__attribute__((address_space(3))) f32x2*)(smem + LDS_ML + h * 512 + lane * 8) = f32x2{u2f(sm[1]), u2f(sl[1])};
  }
  bar();
  const f32x2 ml = *(__attribute__((address_space(3))) f32x2*)(smem + LDS_ML + (h ^ 1) * 512 + lane * 8);
  lds_char* ey = smem + xo + (1 - PH) * 12288 + lane * 16;
  f32x4 o[12];
#pragma unroll
  for (int c = 0; c < 12; ++c) o[c] = *(lds_f32x4*)(ey + c * 1024);
  lds_char* wv = smem + LDS_WV + h * 18432 + (8 * (g & 1) + 4 * (g >> 1) + (i >> 2)) * 96 + (i & 3) * 8;
  u32x4 Wv[18];
#pragma unroll
  for (int n = 0; n < 18; ++n) {
    const u32x2 a0 = trread(wv + (n / 3) * 3072 + (n % 3) * 32), a1 = trread(wv + (n / 3) * 3072 + 1536 + (n % 3) * 32);
    Wv[n] = u32x4{a0[0], a0[1], a1[0], a1[1]};
  }
  SB();
  const float mn = __builtin_fmaxf(mo, ml[0]);
  const float fa = __builtin_amdgcn_exp2f(mo - mn), fb = __builtin_amdgcn_exp2f(ml[0] - mn);
  const float l = lo_ * fa + ml[1] * fb, il = __builtin_amdgcn_rcpf(l);
  const unsigned lse = (unsigned)__builtin_amdgcn_ds_bpermute((4 * g + (i & 3)) * 4, (int)f2u(mn + __builtin_amdgcn_logf(l)));
  const f32x2 fa2 = {fa * il, fa * il}, fb2 = {fb * il, fb * il};
  f32x2 u2[24], us[2];
#pragma unroll
  for (int c = 0; c < 24; ++c) {
    const f32x2 own = {Uo[c >> 1][(c & 1) * 2], Uo[c >> 1][(c & 1) * 2 + 1]}, oth = {o[c >> 1][(c & 1) * 2], o[c >> 1][(c & 1) * 2 + 1]};
    u2[c] = pkfma(oth, fb2, pkmul(own, fa2));
    us[c & 1] = c < 2 ? u2[c] : pkadd(us[c & 1], u2[c]);
  }
  const float um = xg_sum((us[0][0] + us[0][1]) + (us[1][0] + us[1][1])) * (-1.0f / 192.0f);
  const f32x2 um2 = {um, um};
  const f32x4 z4 = {0.f, 0.f, 0.f, 0.f};
  f32x4 cx[3] = {z4, z4, z4};
#pragma unroll
  for (int s = 0; s < 6; ++s) {
    u32x4 ub;
#pragma unroll
    for (int r = 0; r < 4; ++r) { const f32x2 v = pkadd(u2[4 * s + r], um2); ub[r] = cvtpk(v[0], v[1]); }
#pragma unroll
    for (int dt = 0; dt < 3; ++dt) cx[dt] = mfma32(ub, Wv[3 * s + dt], cx[dt]);
  }
  const u32x4 ra = cvt8(cx[0], cx[1]);
  u32x4 rb = {cvtpk(cx[2][0], cx[2][1]), cvtpk(cx[2][2], cx[2][3]), lse, 0u};
  rb[3] = (ra[0] + ra[1] + ra[2]) + (ra[3] + rb[0] + rb[1]) + lse + ek;
  const unsigned vo = (lane >> 2) * 32768 + (lane & 3) * 16, so = WS_REC + h * 524288 + b * 64;
  bst4<SC1>(ra, rws, vo, so);
  bst4<SC1>(rb, rws, vo, so + 16384);
}

struct AIn { u32x4 q[6]; };
DEV void a_issue(AIn& I, unsigned lbase, int h, int cs, const void* q_, const void* wq_, const void* wk_, const void* wm_) {
  const int lane = laneid();
  const rsrc_t rq = mkrsrc(q_, 6144u), rwq = mkrsrc(wq_, 73728u), rwk = mkrsrc(wk_, 73728u), rwm = mkrsrc(wm_, 73728u);
  const unsigned vl = lane * 16;
#pragma unroll
  for (int n = 0; n < 18; ++n) dma16(rwq, vl, h * 18432 + n * 1024, lbase + LDS_AWQ + n * 1024);
  unsigned vo[3];
#pragma unroll
  for (int m = 0; m < 3; ++m) { const unsigned o = m * 1024 + vl; vo[m] = (o / 96) * 384 + o % 96; }
#pragma unroll
  for (int n = 0; n < 18; ++n) dma16(rwm, vo[n % 3], (n / 3) * 12288 + cs * 96, lbase + LDS_AWM + n * 1024);
#pragma unroll
  for (int n = 0; n < 5; ++n) dma16(rwk, vl, h * 18432 + cs * 4608 + n * 1024, lbase + LDS_AWK + n * 1024);
  const unsigned vq = (lane & 15) * 384 + (lane >> 4) * 16;
  ldpl<0>(I.q[0], rq, vq); ldpl<64>(I.q[1], rq, vq); ldpl<128>(I.q[2], rq, vq);
  ldpl<192>(I.q[3], rq, vq); ldpl<256>(I.q[4], rq, vq); ldpl<320>(I.q[5], rq, vq);
}
DEV void a_compute(AIn& I, lds_char* smem, int h, int cs, rsrc_t rws, unsigned ek) {
  const int lane = laneid(), i = lane & 15, g = lane >> 4;
  vmwait<0>();
#pragma unroll
  for (int s = 0; s < 6; ++s) OPQ(I.q[s]);
  SB();
  lds_char* a = smem + (8 * g + (i >> 2)) * 96 + (i & 3) * 8;
  const f32x4 z4 = {0.f, 0.f, 0.f, 0.f};
  f32x4 Q[3], M[3], T[3];
  u32x4 W[18], V[18];
  u32x2 Kf[9];
#pragma unroll
  for (int n = 0; n < 18; ++n) {
    const u32x2 a0 = trread(a + LDS_AWQ + (n / 3) * 3072 + (n % 3) * 32), a1 = trread(a + LDS_AWQ + (n / 3) * 3072 + 384 + (n % 3) * 32);
    W[n] = u32x4{a0[0], a0[1], a1[0], a1[1]};
  }
#pragma unroll
  for (int n = 0; n < 18; ++n) {
    const u32x2 a0 = trread(a + LDS_AWM + (n / 3) * 3072 + (n % 3) * 32), a1 = trread(a + LDS_AWM + (n / 3) * 3072 + 384 + (n % 3) * 32);
    V[n] = u32x4{a0[0], a0[1], a1[0], a1[1]};
  }
#pragma unroll
  for (int n = 0; n < 9; ++n) Kf[n] = *(__attribute__((address_space(3))) u32x2*)(smem + LDS_AWK + i * 96 + g * 8 + (n / 3) * 1536 + (n % 3) * 32);
  SB();
#pragma unroll
  for (int n = 0; n < 18; ++n) Q[n % 3] = mfma32(W[n], I.q[n / 3], n < 3 ? z4 : Q[n % 3]);
#pragma unroll
  for (int n = 0; n < 18; ++n) M[n % 3] = mfma32(V[n], I.q[n / 3], n < 3 ? z4 : M[n % 3]);
#pragma unroll
  for (int dt = 0; dt < 3; ++dt) {
    const u32x2 qb = {cvtpk(Q[dt][0], Q[dt][1]), cvtpk(Q[dt][2], Q[dt][3])};
#pragma unroll
    for (int ct = 0; ct < 3; ++ct) T[ct] = mfma16(Kf[3 * ct + dt], qb, dt == 0 ? z4 : T[ct]);
  }
  const float s1 = 1.4426950408889634f * 0.14433756729740643f;
  unsigned P[6];
#pragma unroll
  for (int n = 0; n < 6; ++n)
    P[n] = cvtpk((T[n >> 1][(n & 1) * 2] + 0.5f * M[n >> 1][(n & 1) * 2]) * s1, (T[n >> 1][(n & 1) * 2 + 1] + 0.5f * M[n >> 1][(n & 1) * 2 + 1]) * s1);
  const unsigned so = WS_A + h * 8192 + cs * 2048;
  bst4<SC1>(u32x4{P[0], P[1], P[2], P[0] + P[1] + P[2] + ek}, rws, lane * 16, so);
  bst4<SC1>(u32x4{P[4], P[5], P[3], P[4] + P[5] + P[3] + ek}, rws, lane * 16 + 1024, so);
}
template <int OFF> DEV void ldsc(u32x4& d, rsrc_t r, unsigned voff, unsigned soff) {
  asm volatile("buffer_load_dwordx4 %0, %1, %2, %3 offen offset:%4 sc1" : "=v"(d) : "v"(voff), "s"(r), "s"(soff), "n"(OFF) : "memory");
}
DEV void a_fetch(u32x4 (&G)[8], int h, rsrc_t rws) {
  const unsigned vo = laneid() * 16, s0 = WS_A + h * 8192, s1 = s0 + 4096;
  ldsc<0>(G[0], rws, vo, s0); ldsc<1024>(G[1], rws, vo, s0); ldsc<2048>(G[2], rws, vo, s0); ldsc<3072>(G[3], rws, vo, s0);
  ldsc<0>(G[4], rws, vo, s1); ldsc<1024>(G[5], rws, vo, s1); ldsc<2048>(G[6], rws, vo, s1); ldsc<3072>(G[7], rws, vo, s1);
}
DEV void a_poll(u32x4 (&G)[8], int h, rsrc_t rws, unsigned ek) {
  for (int spin = 0;; ++spin) {
    if (spin) a_fetch(G, h, rws);
    vmwait<0>();
#pragma unroll
    for (int n = 0; n < 8; ++n) OPQ(G[n]);
    bool bad = false;
#pragma unroll
    for (int n = 0; n < 8; ++n) bad |= G[n][0] + G[n][1] + G[n][2] + ek != G[n][3];
    if (__builtin_amdgcn_ballot_w64(bad) == 0 || spin >= SPIN_MAX) break;
  }
}
DEV void a_centre(const u32x4 (&G)[8], lds_char* smem, int h) {
  const int lane = laneid(), i = lane & 15, g = lane >> 4;
  float sm[2] = {0.f, 0.f};
#pragma unroll
  for (int n = 0; n < 24; ++n) sm[n & 1] = dot2(G[n / 3][n % 3], 0x3F803F80u, sm[n & 1]);
  const float mu = xg_sum(sm[0] + sm[1]) * (-1.0f / 192.0f);
  const f32x4 mu4 = {mu, mu, mu, mu};
  const int d = lane & 3;
  const u32x2 I4 = {d == 0 ? 0x00003F80u : (d == 1 ? 0x3F800000u : 0u), d == 2 ? 0x00003F80u : (d == 3 ? 0x3F800000u : 0u)};
  lds_char* dst = smem + LDS_AF + (h >> 1) * 12288 + (g >> 1) * 512 + ((h & 1) * 16 + i) * 16 + (g & 1) * 8;
#pragma unroll
  for (int s = 0; s < 12; ++s) {
    const int cs = s / 3, ct = s % 3;
    const u32x2 v = ct == 0 ? u32x2{G[2 * cs][0], G[2 * cs][1]} : (ct == 1 ? u32x2{G[2 * cs][2], G[2 * cs + 1][2]} : u32x2{G[2 * cs + 1][0], G[2 * cs + 1][1]});
    const f32x4 c = mfma4(I4, v, mu4);
    *(__attribute__((address_space(3))) u32x2*)(dst + s * 1024) = u32x2{cvtpk(c[0], c[1]), cvtpk(c[2], c[3])};
  }
}
DEV float quads_sum(float x) { x += dppmov<0x124>(x); return x + dppmov<0x128>(x); }
DEV float quads_max(float x) { x = __builtin_fmaxf(x, dppmov<0x124>(x)); return __builtin_fmaxf(x, dppmov<0x128>(x)); }
DEV void reduce1(lds_char* smem, int r, int p, rsrc_t rws, unsigned ek) {
  const int lane = laneid(), i = lane & 15, g = lane >> 4, d = lane & 3;
  const unsigned m0 = d == 0 ? 0x0000FFFFu : (d == 1 ? 0xFFFF0000u : 0u), m1 = d == 2 ? 0x0000FFFFu : (d == 3 ? 0xFFFF0000u : 0u);
  u32x4 RA[4], RB[4];
  const unsigned so = WS_REC + r * 32768 + p * 4096;
  for (int spin = 0;; ++spin) {
    asm volatile("" ::: "memory");
#pragma unroll
    for (int j = 0; j < 4; ++j) { RB[j] = bld4<SC1>(rws, lane * 16 + j * 1024, so + 16384); RA[j] = bld4<SC1>(rws, lane * 16 + j * 1024, so); }
    bool bad = false;
#pragma unroll
    for (int j = 0; j < 4; ++j) bad |= (RA[j][0] + RA[j][1] + RA[j][2]) + (RA[j][3] + RB[j][0] + RB[j][1]) + RB[j][2] + ek != RB[j][3];
    if (__builtin_amdgcn_ballot_w64(bad) == 0 || spin >= SPIN_MAX) break;
  }
  const float M = xg_max(quads_max(__builtin_fmaxf(__builtin_fmaxf(u2f(RB[0][2]), u2f(RB[1][2])), __builtin_fmaxf(u2f(RB[2][2]), u2f(RB[3][2])))));
  const f32x4 z4 = {0.f, 0.f, 0.f, 0.f};
  f32x4 acc[3] = {z4, z4, z4};
  float L = 0.f;
#pragma unroll
  for (int j = 0; j < 4; ++j) {
    const float wf = __builtin_amdgcn_exp2f(u2f(RB[j][2]) - M);
    const unsigned wb = cvtpk(wf, wf);
    L += u2f(wb & 0xFFFF0000u);
    const u32x2 dg = {wb & m0, wb & m1};
    acc[0] = mfma4(dg, u32x2{RA[j][0], RA[j][1]}, acc[0]);
    acc[1] = mfma4(dg, u32x2{RA[j][2], RA[j][3]}, acc[1]);
    acc[2] = mfma4(dg, u32x2{RB[j][0], RB[j][1]}, acc[2]);
  }
  L = xg_sum(quads_sum(L));
  float u[6];
  f32x4 v;
#pragma unroll
  for (int k = 0; k < 6; ++k) {
    auto t = __builtin_amdgcn_permlane32_swap(f2u(acc[k % 3][k / 3]), f2u(acc[k % 3][2 + k / 3]), false, false);
    u[k] = u2f(t[0]) + u2f(t[1]);
  }
#pragma unroll
  for (int k = 0; k < 3; ++k) {
    auto t = __builtin_amdgcn_permlane16_swap(f2u(u[k]), f2u(u[k + 3]), false, false);
    v[k] = quads_sum(u2f(t[0]) + u2f(t[1]));
  }
  v[3] = 0.f;
  if (i < 4) *(lds_f32x4*)(smem + LDS_RED + p * 256 + g * 64 + d * 16) = v;
  if (lane < 4) *(__attribute__((address_space(3))) f32x2*)(smem + LDS_RED + 1024 + p * 32 + d * 8) = f32x2{M, L};
  bar();
  if (p == 0) {
    const f32x4 x = *(lds_f32x4*)(smem + LDS_RED + (i >> 2) * 256 + g * 64 + d * 16);
    const f32x2 ml = *(__attribute__((address_space(3))) f32x2*)(smem + LDS_RED + 1024 + (i >> 2) * 32 + g * 8);
    const float f = __builtin_amdgcn_exp2f(ml[0] - quads_max(ml[0]));
    const float il = __builtin_amdgcn_rcpf(quads_sum(ml[1] * f));
    u32x4 o;
#pragma unroll
    for (int k = 0; k < 3; ++k) { const float c = quads_sum(x[k] * f) * il; o[k] = cvtpk(c, dppmov<0xB1>(c)); }
    o[3] = o[0] + o[1] + o[2] + ek;
    if (i == 0 || i == 2) bst4<SC1>(o, rws, (d >> 1) * 64 + g * 16, WS_CTX + r * 128);
  }
}
DEV void reduce2_issue(u32x2& qr, unsigned lbase, int nt, const void* q_, const void* wo_) {
  const int lane = laneid();
  const rsrc_t rq = mkrsrc(q_, 6144u), rwo = mkrsrc(wo_, 73728u);
  const unsigned vo = (lane >> 1) * 384 + (lane & 1) * 16, vq = (lane & 15) * 384 + (lane >> 4) * 8;
#pragma unroll
  for (int n = 0; n < 6; ++n) dma16(rwo, vo, n * 12288 + nt * 32, lbase + LDS_WO + n * 1024);
  asm volatile("buffer_load_dwordx2 %0, %1, %2, %3 offen" : "=v"(qr) : "v"(vq), "s"(rq), "s"(nt * 32) : "memory");
}
DEV void reduce2(u32x2& qr, lds_char* smem, int nt, rsrc_t rws, unsigned ek, void* out_) {
  const int lane = laneid(), i = lane & 15, g = lane >> 4, rho = i >> 2;
  vmwait<0>();
  OPQ(qr);
  SB();
  lds_char* a0 = smem + LDS_WO + (16 * (rho >> 1) + 4 * g + (rho & 1)) * 32 + (i & 3) * 8;
  lds_char* a1 = smem + LDS_WO + (32 + 4 * g + (rho & 1)) * 32 + (i & 3) * 8;
  u32x4 W[8], G[8];
#pragma unroll
  for (int m = 0; m < 8; ++m) {
    const u32x2 x0 = trread(a0 + ((m >> 1) * 48 + 2 * (m & 1)) * 32), x1 = trread(a1 + ((m >> 1) * 48 + 2 * (m & 1)) * 32);
    W[m] = u32x4{x0[0], x0[1], x1[0], x1[1]};
    OPQ(W[m]);
  }
  const unsigned vo = (i >> 2) * 512 + g * 128 + (i & 3) * 16;
  for (int spin = 0;; ++spin) {
    asm volatile("" ::: "memory");
#pragma unroll
    for (int m = 0; m < 8; ++m) G[m] = bld4<SC1>(rws, vo + (m & 1) * 64 + ((m >> 1) & 1) * 2048, WS_CTX + (m >> 2) * 4096);
    bool bad = false;
#pragma unroll
    for (int m = 0; m < 8; ++m) bad |= G[m][0] + G[m][1] + G[m][2] + ek != G[m][3];
    if (__builtin_amdgcn_ballot_w64(bad) == 0 || spin >= SPIN_MAX) break;
  }
  f32x4 c[2] = {{u2f(qr[0] << 16), u2f(qr[0] & 0xFFFF0000u), u2f(qr[1] << 16), u2f(qr[1] & 0xFFFF0000u)}, {0.f, 0.f, 0.f, 0.f}};
#pragma unroll
  for (int m = 0; m < 8; ++m) { G[m][3] = 0u; c[m & 1] = mfma32(W[m], G[m], c[m & 1]); }
  const f32x4 o = c[0] + c[1];
  __builtin_amdgcn_raw_buffer_store_b64(u32x2{cvtpk(o[0], o[1]), cvtpk(o[2], o[3])}, mkrsrc(out_, 6144u), i * 384 + g * 8, nt * 32, 0);
  if (nt == 0 && lane == 0) __builtin_amdgcn_raw_buffer_store_b32(ek - KCHK + 1u, rws, 0, 0, 0);
}

__global__ void __launch_bounds__(512, 2)
m2f_main(const void* __restrict__ fhi_, const void* __restrict__ flo_, const void* __restrict__ wlat_,
         const void* __restrict__ wv_, const void* __restrict__ q_, const void* __restrict__ wq_, const void* __restrict__ wk_,
         const void* __restrict__ wm_, const void* __restrict__ wo_, void* __restrict__ ws_, void* __restrict__ out_) {
  __shared__ __attribute__((aligned(16))) char smem_[LDS_TOTAL];
  lds_char* smem = (lds_char*)smem_;
  const int w = __builtin_amdgcn_readfirstlane(__builtin_amdgcn_workitem_id_x() >> 6);
  const int b = __builtin_amdgcn_workgroup_id_x();

  if (w < 4) {
    const int p = w, lane = laneid(), i = lane & 15, g = lane >> 4;
    __builtin_amdgcn_s_setprio(PRIO_P);
    const rsrc_t rwl = mkrsrc(wlat_, 73728u);
    ProdC C;
    C.smem = smem;
    C.rhi = mkrsrc(fhi_, 100663296u);
    C.rlo = mkrsrc(flo_, 25165824u);
    C.rwv = mkrsrc(wv_, 73728u);
    const unsigned j = lane >> 3, c7 = lane & 7;
    const unsigned lbase = (unsigned)(uintptr_t)smem;
    C.voH = j * 384 + c7 * 16;
    C.vw = lane * 16;
    C.lds0 = lbase + LDS_LO + p * 1536;
    C.ldsv = lbase + LDS_WV;
#pragma unroll
    for (int k = 0; k < 2; ++k) {
      const unsigned o = k * 1024 + (lane & (k ? 31 : 63)) * 16, px = o / 384, pc = (o % 384) / 16;
      C.vd[k] = px * 384 + ((pc ^ (2 * p + (px >> 1))) * 16);
    }
    const unsigned soL = (unsigned)(b * 256 + p * 4) * 384u;
    const unsigned soH = (unsigned)(b * 1024 + p * 8) * 384u;
    Prod P;
    dma24(C.rlo, C.vd[0], C.vd[1], soL, C.lds0);
    dma24(C.rlo, C.vd[0], C.vd[1], soL + 6144, C.lds0 + 6144);
    SB();
    if (b < 16) bar();
    {
      unsigned vo[6];
#pragma unroll
      for (int m = 0; m < 6; ++m) {
        const unsigned o = m * 1024 + lane * 16, k = o / 384, pc = (o % 384) / 16;
        vo[m] = k * 384 + ((pc ^ (2 * (((k >> 1) & 1) | ((k >> 3) << 1)))) * 16);
      }
      const unsigned so1 = soH + 196608, wo = p * 18432, wl = lbase + LDS_WL + p * 18432;
#define WGRP(M) ld_hi1<M>(P.ring[0][M], C, soH, so1); if (M < NH1) ld_hi1<M>(P.ring[1][M], C, soH + 12288, so1 + 12288); \
      for (int n = 3 * M; n < 3 * M + 3; ++n) dma16(rwl, vo[n % 6], wo + (n / 6) * 6144, wl + n * 1024);
      WGRP(0) WGRP(1) WGRP(2) WGRP(3) WGRP(4) WGRP(5)
#undef WGRP
#define HREST(M) if (M >= NH1) ld_hi1<M>(P.ring[1][M], C, soH + 12288, so1 + 12288);
      HREST(0) HREST(1) HREST(2) HREST(3) HREST(4) HREST(5)
#undef HREST
    }
    SB();
    dma24(C.rlo, C.vd[0], C.vd[1], soL + 2 * 6144, C.lds0 + 2 * 6144);
    dma24(C.rlo, C.vd[0], C.vd[1], soL + 3 * 6144, C.lds0 + 3 * 6144);
    ld_hi(P.ring[2], C, soH + 2 * 12288);
    SB();
    {
      const int d = lane & 3;
      C.I4[0] = d == 0 ? 0x00003F80u : (d == 1 ? 0x3F800000u : 0u);
      C.I4[1] = d == 2 ? 0x00003F80u : (d == 3 ? 0x3F800000u : 0u);
#pragma unroll
      for (int ab = 0; ab < 2; ++ab) {
        C.xw[ab] = LDS_X + (16 * p + 8 * ab + j) * 128 + ((c7 ^ (((j >> 1) & 1) * 4 + 2 * ab + (j >> 2))) * 16);
        C.br[ab] = LDS_LO + i * 384 + (((4 * ab + g) ^ (i >> 1)) * 16);
        C.lw[ab] = LDS_LAT + i * 768 + (((12 * p + 4 * ab + g) ^ (i & 7)) * 16);
        C.lr[ab] = LDS_LAT + (4 * p + (j >> 1)) * 768 + (((2 * c7 + ab) ^ ((4 * p + (j >> 1)) & 7)) * 16);
      }
      C.odd = lane & 1;
      C.rw = c7 < 2 ? LDS_R + (16 * p + 8 * (lane & 1) + j) * 4 : LDS_ML + lane * 4;
#pragma unroll
      for (int ab = 0; ab < 2; ++ab) { OPQ(C.xw[ab]); OPQ(C.br[ab]); OPQ(C.lw[ab]); OPQ(C.lr[ab]); OPQ(C.vd[ab]); }
      OPQ(C.rw); OPQ(C.vw);
    }
    SB();
    unsigned ek = epoch(ws_);
    SB();
    vmwait<16 - NH1>();
    asm volatile("" : "+s"(ek));
    bar();
    {
      const unsigned rp = i >> 2, cp = i & 3, s = (rp >> 1) | ((g & 1) << 1);
      u32x4 B[12];
#pragma unroll
      for (int n = 0; n < 12; ++n) B[n] = lo_read(C, n / 6, n % 6);
#pragma unroll
      for (int ks = 0; ks < 6; ++ks)
#pragma unroll
        for (int tt = 0; tt < 3; ++tt) {
          const unsigned ct = 3 * p + tt;
          lds_char* a = smem + LDS_WL + (8 * g + rp) * 384 + (((ct & 3) ^ s) * 32) + cp * 8 + (ct >> 2) * 128;
          const u32x2 a0 = trread(a + ks * 12288), a1 = trread(a + ks * 12288 + 1536);
          P.W[tt * 6 + ks] = u32x4{a0[0], a0[1], a1[0], a1[1]};
        }
      SB();
      const f32x4 z4 = {0.f, 0.f, 0.f, 0.f};
      f32x4 G0[3], G1[3];
#pragma unroll
      for (int n = 0; n < 18; ++n) {
        G0[n % 3] = mfma32(P.W[(n % 3) * 6 + n / 3], B[n / 3], n < 3 ? z4 : G0[n % 3]);
        G1[n % 3] = mfma32(P.W[(n % 3) * 6 + n / 3], B[6 + n / 3], n < 3 ? z4 : G1[n % 3]);
        OPQ(G0[n % 3]); OPQ(G1[n % 3]);
        SB();
      }
      lat_write(G0, C, LDS_LT0 - LDS_LAT);
      lat_write(G1, C, 12288);
    }
    vmwait<8>();
    bar();
#pragma unroll
    for (int k = 0; k < 6; ++k) P.lat[k >> 1][k & 1] = lat_read(C, LDS_LT0 - LDS_LAT, k);
    dma24(C.rlo, C.vd[0], C.vd[1], soL + 4 * 6144, C.lds0);
    ld_hi(P.ring[3], C, soH + 3 * 12288);
    SB();
#pragma unroll 1
    for (int u = 0; u < 3; ++u) {
      const unsigned so = soH + (u + 1) * 49152, sl = soL + 6144 + (u + 1) * 24576, wv = p * 1024 + u * 20480;
      pstep<0, true, true, true, 1, -1>(P, C, true, so, sl, wv); vmwait<23>(); bar();
      pstep<1, true, true, true, 1, -1>(P, C, true, so + 12288, sl + 6144, wv + 4096); vmwait<24>(); bar();
      pstep<2, true, true, true, 1, -1>(P, C, true, so + 2 * 12288, sl + 2 * 6144, wv + 2 * 4096); vmwait<25>(); bar();
      pstep<3, true, true, true, 2, -1>(P, C, u < 2, so + 3 * 12288, sl + 3 * 6144, wv + 3 * 4096); vmwait<24>(); bar();
    }
    pstep<0, false, true, true, 3, -1>(P, C, false, 0, 0, p * 1024 + 61440); vmwait<18>(); bar();
    pstep<1, false, true, true, 0, -1>(P, C, false, 0, 0, 0); bar();
    pstep<2, false, false, true, 0, 17>(P, C, false, 0, 0, 0); bar();
    pstep<3, false, false, false, 0, 10>(P, C, false, 0, 0, 0); vmwait<0>(); bar();
    const bool l2 = p == 0 && b >= L2_B0 && b < L2_B0 + 12;
    u32x2 qr;
    if (l2) reduce2_issue(qr, lbase, b - L2_B0, q_, wo_);
    bar();
    if (b >= L1_B0 && b < L1_B0 + 64) reduce1(smem, b - L1_B0, p, mkrsrc(ws_, WS_BYTES), ek);
    if (l2) reduce2(qr, smem, b - L2_B0, mkrsrc(ws_, WS_BYTES), ek, out_);
  } else {
    const int h = w - 4, hp = h >> 1, ph = h & 1, lane = laneid(), i = lane & 15, g = lane >> 4;
    const rsrc_t rws = mkrsrc(ws_, WS_BYTES);
    Cons K;
#define PRIO_C 2
    __builtin_amdgcn_s_setprio(PRIO_C);
    unsigned ek, pf;
    {
      const unsigned n = ((b >> 3) - 2) * 4 + h, h0 = (b & 7) >> 2, cs = b & 3;
      const void* src = n < 10 ? wq_ : (n < 16 ? wm_ : (n < 20 ? wk_ : q_));
      const unsigned so = n < 5 ? h0 * 18432 + n * 4096 : (n < 10 ? h0 * 18432 + 36864 + (n - 5) * 4096 : (n < 16 ? (n - 10) * 12288 + ((cs * 96) >> 6) * 64
                        : (n < 20 ? h0 * 18432 + ((n >> 1) & 1) * 36864 + cs * 4608 + (n & 1) * 4096 : (n & 1) * 4096)));
      const unsigned vo = n >= 10 && n < 16 ? (lane >> 1) * 384 + (lane & 1) * 64 : lane * 64;
      asm volatile("buffer_load_dword %0, %1, %2, %3 offen" : "=v"(pf) : "v"(vo), "s"(mkrsrc(src, n < 20 ? 73728u : (n < 22 ? 6144u : 0u))), "s"(so) : "memory");
    }
    if (b < 16 && h == 0) {
      AIn I;
      a_issue(I, (unsigned)(uintptr_t)smem, b >> 2, b & 3, q_, wq_, wk_, wm_);
      ek = epoch(ws_);
      asm volatile("s_barrier" ::: "memory");
      a_compute(I, smem, b >> 2, b & 3, rws, ek);
    } else {
      if (b < 16) bar();
      ek = epoch(ws_);
    }
    asm volatile("" : "+s"(ek));
    u32x4 G[8];
    bar();
    a_fetch(G, h, rws);
    bar();
    a_poll(G, h, rws, ek);
    OPQ(pf);
    a_centre(G, smem, h);
    ConsC C;
    C.smem = smem;
    const unsigned hl = lane >> 5;
    {
      const unsigned m = lane & 31, row = 32 * ph + m, f = ((m >> 1) & 1) * 4 + ((m >> 2) & 3);
#pragma unroll
      for (int s = 0; s < 4; ++s) C.xr[s] = LDS_X + row * 128 + (((2 * s + hl) ^ f) * 16);
      const unsigned cb = g & 1, rp = i >> 2, cp = i & 3;
#pragma unroll
      for (int c = 0; c < 2; ++c)
#pragma unroll
        for (int sec = 0; sec < 2; ++sec)
          C.xt[c][sec] = LDS_X + (32 * ph + 8 * sec + 4 * hl + rp) * 128 + (((4 * c + 2 * cb + (cp >> 1)) ^ ((rp >> 1) * 4 + hl + 2 * sec)) * 16) + (cp & 1) * 8;
      C.ra = LDS_R + (32 * ph + 4 * hl) * 4;
#pragma unroll
      for (int s = 0; s < 4; ++s) { OPQ(C.xr[s]); OPQ(C.xt[s >> 1][s & 1]); }
      OPQ(C.ra);
    }
#pragma unroll
    for (int c = 0; c < 6; ++c)
#pragma unroll
      for (int e = 0; e < 16; ++e) K.U[c][e] = 0.f;
    K.mrun = 0.f; K.lsum = 0.f;
#pragma unroll 1
    for (int u = 0; u < 8; ++u) {
      bar();
      if (u == 0) {
        lds_char* af = smem + LDS_AF + hp * 12288 + (laneid() ^ (16 * ph)) * 16;
#pragma unroll
        for (int s = 0; s < 12; ++s) K.Ab[s] = *(lds_u32x4*)(af + s * 1024);
        SB();
      }
      cstep<0>(K, C, u == 0);
      bar();
      cstep<1>(K, C, false);
    }
    cfinish(K, smem, h, b, rws, ek);
  }
}

extern "C" void kernel_launch(void* const* d_in, const int* in_sizes, int n_in, void* d_out, int out_size, void* d_ws,
                              size_t ws_size, hipStream_t stream) {
  m2f_main<<<256, 512, 0, stream>>>(d_in[1], d_in[2], d_in[3], d_in[6], d_in[0], d_in[4], d_in[5], d_in[8], d_in[7], d_ws, d_out);
}
```

```cpp
#include <hip/hip_runtime.h>
#include <stdint.h>

typedef __attribute__((ext_vector_type(8))) __bf16 bf16x8;
typedef __attribute__((ext_vector_type(2))) __bf16 bf16x2;
typedef __attribute__((ext_vector_type(4))) short s16x4;
typedef __attribute__((ext_vector_type(4))) float f32x4;
typedef __attribute__((ext_vector_type(2))) float f32x2;
typedef __attribute__((ext_vector_type(4))) unsigned u32x4;
typedef __attribute__((ext_vector_type(2))) unsigned u32x2;
typedef __attribute__((address_space(3))) char lds_char;
typedef __attribute__((address_space(3))) s16x4 lds_s16x4;
typedef __attribute__((address_space(3))) u32x4 lds_u32x4;
typedef __attribute__((address_space(3))) f32x4 lds_f32x4;
typedef __attribute__((address_space(3))) float lds_f32;

#define DEV __device__ __forceinline__
#define SB() __builtin_amdgcn_sched_barrier(0)
#define OPQ(x) asm volatile("" : "+v"(x))

constexpr int LDS_X = 0;
constexpr int LDS_WL = 24576;
constexpr int LDS_WV = 49152;
constexpr int LDS_LO = 110592;
constexpr int LDS_LAT = 135168;
constexpr int LDS_R = 159744;
constexpr int LDS_ML = 160256;
constexpr int LDS_TOTAL = 162304;
constexpr int LDS_AF = 73728;
constexpr int LDS_LT0 = 98304;
constexpr int LDS_AWQ = 0, LDS_AWM = LDS_LAT, LDS_AWK = LDS_LAT + 18432;
constexpr int LDS_WO = LDS_LO + 2 * 6144, LDS_RED = LDS_LO + 3 * 6144;
constexpr float THR = 1099511627776.0f, LOW = 1.0f / THR;
constexpr unsigned WS_A = 4096, WS_REC = 65536, WS_CTX = WS_REC + 64 * 32768, WS_BYTES = WS_CTX + 8192;
constexpr unsigned KCHK = 0x9E3779B9u;
constexpr int SC1 = 16, SPIN_MAX = 1 << 15;
constexpr int L1_B0 = 64, L2_B0 = 128;
constexpr unsigned HS = 786432u, LS = 196608u;
constexpr int NPF = 8, PRIO_P = 0, PRIO_A = 3, PRIO_G = 2, PRIO_C = 2;

DEV float cut(float x) { asm("" : "+v"(x)); return x; }
DEV unsigned cvtpk(float lo, float hi) {
  f32x2 v = {lo, hi};
  return __builtin_bit_cast(unsigned, __builtin_convertvector(v, bf16x2));
}
DEV u32x4 cvt8(f32x4 a, f32x4 b) { return u32x4{cvtpk(a[0], a[1]), cvtpk(a[2], a[3]), cvtpk(b[0], b[1]), cvtpk(b[2], b[3])}; }
DEV f32x4 mfma32(u32x4 a, u32x4 b, f32x4 c) {
  return __builtin_amdgcn_mfma_f32_16x16x32_bf16(__builtin_bit_cast(bf16x8, a), __builtin_bit_cast(bf16x8, b), c, 0, 0, 0);
}
DEV unsigned f2u(float x) { return __builtin_bit_cast(unsigned, x); }
DEV float u2f(unsigned x) { return __builtin_bit_cast(float, x); }
DEV f32x4 mfma16(u32x2 a, u32x2 b, f32x4 c) {
  return __builtin_amdgcn_mfma_f32_16x16x16bf16_1k(__builtin_bit_cast(s16x4, a), __builtin_bit_cast(s16x4, b), c, 0, 0, 0);
}
DEV f32x4 mfma4(u32x2 a, u32x2 b, f32x4 c) {
  return __builtin_amdgcn_mfma_f32_4x4x4bf16_1k(__builtin_bit_cast(s16x4, a), __builtin_bit_cast(s16x4, b), c, 0, 0, 0);
}
DEV u32x2 trread(lds_char* p) {
  return __builtin_bit_cast(u32x2, __builtin_amdgcn_ds_read_tr16_b64_v4i16((lds_s16x4*)p));
}
DEV float xg_sum(float x) {
  auto s = __builtin_amdgcn_permlane16_swap(f2u(x), f2u(x), false, false);
  x = u2f(s[0]) + u2f(s[1]);
  auto t = __builtin_amdgcn_permlane32_swap(f2u(x), f2u(x), false, false);
  return u2f(t[0]) + u2f(t[1]);
}
DEV float xg_max(float x) {
  auto s = __builtin_amdgcn_permlane16_swap(f2u(x), f2u(x), false, false);
  x = __builtin_fmaxf(u2f(s[0]), u2f(s[1]));
  auto t = __builtin_amdgcn_permlane32_swap(f2u(x), f2u(x), false, false);
  return __builtin_fmaxf(u2f(t[0]), u2f(t[1]));
}
DEV int laneid() {
  int l;
  asm volatile("v_mbcnt_lo_u32_b32 %0, -1, 0\n\tv_mbcnt_hi_u32_b32 %0, -1, %0" : "=v"(l));
  __builtin_assume(l >= 0 && l < 64);
  return l;
}
DEV void bar() { SB(); __builtin_amdgcn_s_waitcnt(0xC07F); asm volatile("s_barrier" ::: "memory"); SB(); }
typedef __amdgpu_buffer_rsrc_t rsrc_t;
DEV rsrc_t mkrsrc(const void* p, unsigned bytes) { return __builtin_amdgcn_make_buffer_rsrc((void*)p, (short)0, (int)bytes, 0x00020000); }
template <int AUX> DEV u32x4 bld4(rsrc_t r, unsigned voff, unsigned soff) { return __builtin_amdgcn_raw_buffer_load_b128(r, voff, soff, AUX); }

DEV void dma16(rsrc_t r, unsigned voff, unsigned soff, unsigned ldsdst) {
  asm volatile("s_mov_b32 m0, %0\n\ts_nop 0\n\tbuffer_load_dwordx4 %1, %2, %3 offen lds" :: "s"(ldsdst), "v"(voff), "s"(r), "s"(soff) : "memory");
}
DEV void dma24(rsrc_t r, unsigned v0, unsigned v1, unsigned soff, unsigned ldsdst) {
  asm volatile("s_mov_b32 m0, %0\n\ts_nop 0\n\tbuffer_load_dwordx4 %1, %3, %4 offen nt lds\n\t"
               "s_mov_b32 exec_hi, 0\n\ts_add_u32 m0, m0, 0x400\n\ts_nop 0\n\tbuffer_load_dwordx4 %2, %3, %4 offen nt lds\n\ts_mov_b32 exec_hi, -1"
               :: "s"(ldsdst), "v"(v0), "v"(v1), "s"(r), "s"(soff) : "memory", "scc");
}
template <int OFF> DEV void ldnt(u32x4& d, rsrc_t r, unsigned voff, unsigned soff) {
  asm volatile("buffer_load_dwordx4 %0, %1, %2, %3 offen offset:%4 nt" : "=v"(d) : "v"(voff), "s"(r), "s"(soff), "n"(OFF) : "memory");
}
template <int OFF> DEV void ldpl(u32x4& d, rsrc_t r, unsigned voff) {
  asm volatile("buffer_load_dwordx4 %0, %1, %2, 0 offen offset:%3" : "=v"(d) : "v"(voff), "s"(r), "n"(OFF) : "memory");
}
template <int AUX> DEV void bst4(u32x4 v, rsrc_t r, unsigned voff, unsigned soff) { __builtin_amdgcn_raw_buffer_store_b128(v, r, voff, soff, AUX); }
template <int N> DEV void vmwait() { asm volatile("s_waitcnt vmcnt(%0)" :: "n"(N) : "memory"); }
DEV unsigned epoch(const void* ws) {
  asm volatile("" : "+s"(ws));
  return *(const __attribute__((address_space(4))) unsigned*)ws + KCHK;
}
DEV float dot2(unsigned a, unsigned b, float c) {
  return __builtin_amdgcn_fdot2_f32_bf16(__builtin_bit_cast(bf16x2, a), __builtin_bit_cast(bf16x2, b), c, false);
}
struct Prod {
  u32x4 W[18];
  u32x4 ring[4][6];
  f32x4 lat[3][2];
};
struct ProdC {
  lds_char* smem;
  unsigned xw[2], rw, br[2], lw[2], lr[2], vd[2], voH, vw, lds0, ldsv;
  u32x2 I4;
  bool odd;
  rsrc_t rhi, rlo, rwv;
};
DEV u32x4 lo_read(const ProdC& C, int slot, int ks) { return *(lds_u32x4*)(C.smem + C.br[ks & 1] + slot * 6144 + (ks >> 1) * 128); }
DEV void lat_write(const f32x4 (&T)[3], const ProdC& C, int off) {
#pragma unroll
  for (int tt = 0; tt < 3; ++tt) *(lds_f32x4*)(C.smem + C.lw[tt & 1] + off + (tt >> 1) * 128) = T[tt];
}
DEV f32x4 lat_read(const ProdC& C, int off, int k) { return *(lds_f32x4*)(C.smem + C.lr[k & 1] + off + (k >> 1) * 256); }
template <int M> DEV void ld_hi1(u32x4& d, const ProdC& C, unsigned so0, unsigned so1) { ldnt<(M >> 1) * 128>(d, C.rhi, C.voH, (M & 1) ? so1 : so0); }
template <int CTRL> DEV float dppmov(float x) { return __builtin_bit_cast(float, __builtin_amdgcn_update_dpp(0, __builtin_bit_cast(int, x), CTRL, 0xf, 0xf, true)); }
struct Stat { f32x4 q2[2], q1[2]; float a0, a1, v, w, mu; };
DEV void stat_op(Stat& Z, const ProdC& C, int n, int slot) {
  if (n == 0) { Z.v = cut(C.odd ? Z.q1[1][0] : Z.q1[0][0]); Z.w = cut(C.odd ? Z.q1[0][0] : Z.q1[1][0]); }
  if (n == 1) Z.a0 = cut(Z.q2[0][0] + dppmov<0x55>(Z.q2[0][1]));
  if (n == 2) Z.a1 = cut(Z.q2[1][1] + dppmov<0x00>(Z.q2[1][0]));
  if (n == 3) Z.v = cut(Z.v + dppmov<0xB1>(Z.w));
  if (n == 4) Z.a0 = cut(Z.a0 + dppmov<0xAA>(Z.q2[0][2]));
  if (n == 5) Z.a1 = cut(Z.a1 + dppmov<0xAA>(Z.q2[1][2]));
  if (n == 6) Z.v = cut(Z.v + dppmov<0x4E>(Z.v));
  if (n == 7) Z.a0 = cut(Z.a0 + dppmov<0xFF>(Z.q2[0][3]));
  if (n == 8) Z.a1 = cut(Z.a1 + dppmov<0xFF>(Z.q2[1][3]));
  if (n == 9) Z.v = cut(Z.v + dppmov<0x104>(Z.v));
  if (n == 10) Z.a0 = cut(C.odd ? Z.a1 : Z.a0);
  if (n == 11) Z.mu = cut(Z.v * (1.0f / 192.0f));
  if (n == 12) Z.a0 = cut(Z.a0 + dppmov<0x104>(Z.a0));
  if (n == 13) Z.w = cut(Z.mu * Z.mu);
  if (n == 14) Z.a0 = cut(__builtin_fmaxf(Z.a0 * (1.0f / 192.0f) - Z.w, 0.0f) + 1e-5f);
  if (n == 15) Z.a0 = cut(__builtin_amdgcn_rsqf(Z.a0));
  if (n == 17) *(lds_f32*)(C.smem + C.rw + (slot & 1) * 256) = Z.a0;
}
template <int S, int M, int WAIT> DEV void padd(Prod& P, const ProdC& C, f32x4 (&d)[6][2]) {
  if (WAIT >= 0) vmwait<(WAIT >= 0 ? WAIT - M : 0)>();
  OPQ(P.ring[S][M]);
  const u32x4 F = P.ring[S][M];
  d[M][0] = mfma4(C.I4, u32x2{F[0], F[1]}, P.lat[M >> 1][0]);
  d[M][1] = mfma4(C.I4, u32x2{F[2], F[3]}, P.lat[M >> 1][1]);
  OPQ(d[M][0]); OPQ(d[M][1]);
  SB();
}
template <int M> DEV void pcvt(const f32x4 (&d)[6][2], u32x4 (&x)[6]) { x[M] = cvt8(d[M][0], d[M][1]); OPQ(x[M]); SB(); }
template <int S> DEV void x_write(const ProdC& C, const u32x4 (&x)[6], int m) {
  *(lds_u32x4*)(C.smem + C.xw[m & 1] + (S & 1) * 24576 + (m >> 1) * 8192) = x[m];
}
template <int S, bool HI, bool LAT, bool RD, int NWV, int WAIT, bool RAMP = false, int D = 3>
DEV void pstep(Prod& P, const ProdC& C, bool lo, unsigned so, unsigned sl, unsigned wv) {
  f32x4 d[6][2];
  u32x4 x[6], B[6];
  if (NWV < 0) for (int n = 0; n < -NWV; ++n) dma16(C.rwv, C.vw, wv + n * 4096, C.ldsv + wv + n * 4096);
  __builtin_amdgcn_s_setprio(PRIO_A);
  padd<S, 0, WAIT>(P, C, d); padd<S, 1, WAIT>(P, C, d); padd<S, 2, WAIT>(P, C, d);
  padd<S, 3, WAIT>(P, C, d); padd<S, 4, WAIT>(P, C, d); padd<S, 5, WAIT>(P, C, d);
  __builtin_amdgcn_s_setprio(PRIO_P);
  SB();
  pcvt<0>(d, x); pcvt<1>(d, x); pcvt<2>(d, x); pcvt<3>(d, x); pcvt<4>(d, x); pcvt<5>(d, x);
  const f32x4 z4 = {0.f, 0.f, 0.f, 0.f};
  const u32x2 ones = {0x3F803F80u, 0x3F803F80u};
  Stat Z;
#pragma unroll
  for (int n = 0; n < 6; ++n) {
    x_write<S>(C, x, n);
    SB();
#pragma unroll
    for (int ab = 0; ab < 2; ++ab) {
      const u32x2 v = {x[(n >> 1) * 2 + ab][(n & 1) * 2], x[(n >> 1) * 2 + ab][(n & 1) * 2 + 1]};
      Z.q1[ab] = mfma4(ones, v, n == 0 ? z4 : Z.q1[ab]);
      Z.q2[ab] = mfma4(v, v, n == 0 ? z4 : Z.q2[ab]);
      OPQ(Z.q1[ab]); OPQ(Z.q2[ab]);
      SB();
    }
    if (LAT && (n & 1)) { B[n - 1] = lo_read(C, (S + 2) & 3, n - 1); B[n] = lo_read(C, (S + 2) & 3, n); SB(); }
    if (RAMP) {
      const unsigned sa = so - HS, sb = sa + 196608;
      if (n == 0) { dma24(C.rlo, C.vd[0], C.vd[1], sl - LS, C.lds0 + (S & 3) * 6144); ld_hi1<0>(P.ring[(S + 2) & 3][0], C, sa, sb); }
      if (n == 1) ld_hi1<1>(P.ring[(S + 2) & 3][1], C, sa, sb);
      if (n == 2) ld_hi1<2>(P.ring[(S + 2) & 3][2], C, sa, sb);
      if (n == 3) ld_hi1<3>(P.ring[(S + 2) & 3][3], C, sa, sb);
      if (n == 4) ld_hi1<4>(P.ring[(S + 2) & 3][4], C, sa, sb);
      if (n == 5) ld_hi1<5>(P.ring[(S + 2) & 3][5], C, sa, sb);
      SB();
    }
  }
  __builtin_amdgcn_s_setprio(PRIO_G);
  f32x4 G[3];
  const unsigned so1 = so + 196608;
#pragma unroll
  for (int n = 0; n < 18; ++n) {
    if (LAT) { G[n % 3] = mfma32(P.W[(n % 3) * 6 + n / 3], B[n / 3], n < 3 ? z4 : G[n % 3]); OPQ(G[n % 3]); SB(); }
    if (RD && n % 2 == 0 && n < 12) P.lat[n >> 2][(n >> 1) & 1] = lat_read(C, ((S + 1) & 1) * 12288, n >> 1);
    stat_op(Z, C, n, S);
    SB();
    if (n == 0 && lo) dma24(C.rlo, C.vd[0], C.vd[1], sl, C.lds0 + ((S + D + 2) & 3) * 6144);
    if (HI && n == 3) ld_hi1<0>(P.ring[(S + D) & 3][0], C, so, so1);
    if (HI && n == 5) ld_hi1<1>(P.ring[(S + D) & 3][1], C, so, so1);
    if (HI && n == 7) ld_hi1<2>(P.ring[(S + D) & 3][2], C, so, so1);
    if (HI && n == 9) ld_hi1<3>(P.ring[(S + D) & 3][3], C, so, so1);
    if (HI && n == 11) ld_hi1<4>(P.ring[(S + D) & 3][4], C, so, so1);
    if (HI && n == 13) ld_hi1<5>(P.ring[(S + D) & 3][5], C, so, so1);
    if (n % 2 == 1 && n / 2 < NWV) dma16(C.rwv, C.vw, wv + (n / 2) * 4096, C.ldsv + wv + (n / 2) * 4096);
    SB();
  }
  if (LAT) lat_write(G, C, (S & 1) * 12288);
}

struct Cons {
  f32x4 U[2][12];
  u32x4 Ab[2][6];
  float m[2], l[2];
};
struct ConsC { lds_char* smem; unsigned xr[2], xt[4], ra, wv, ex, ml; };
template <int SL> DEV u32x4 row_read(const ConsC& C, int n) {
  const int a = n / 6, s = n % 6;
  return *(lds_u32x4*)(C.smem + C.xr[s & 1] + (s >> 1) * 8192 + a * 2048 + SL * 24576);
}
template <int SL> DEV u32x4 col_read(const ConsC& C, int ct) {
  lds_char* p = C.smem + C.xt[ct & 3] + (ct >> 2) * 8192 + SL * 24576;
  const u32x2 a0 = trread(p), a1 = trread(p + 2048);
  return u32x4{a0[0], a0[1], a1[0], a1[1]};
}
struct Soft { f32x4 V[2], E[2]; float t[2], ls; u32x4 Pb; };
DEV void soft_op(Soft& Z, const f32x4& S, const f32x4& rr, float m, int a, int n) {
  if (n < 4) Z.V[a][n] = cut(S[n] * rr[n] - m);
  else if (n < 8) Z.E[a][n - 4] = cut(__builtin_amdgcn_exp2f(Z.V[a][n - 4]));
  else if (n == 8) Z.t[a] = cut(Z.E[a][0] + Z.E[a][1]);
  else if (n == 9) Z.ls = cut(a == 0 ? Z.E[a][2] + Z.E[a][3] : Z.ls + Z.E[a][2]);
  else if (n == 10) Z.t[a] = a == 0 ? Z.t[a] : cut(Z.t[a] + Z.E[a][3]);
  else if (n == 11) Z.ls = cut(Z.ls + Z.t[a]);
  else if (n < 16) Z.E[a][n - 12] = cut(Z.E[a][n - 12] * rr[n - 12]);
  else { Z.Pb[2 * a + n - 16] = cvtpk(Z.E[a][2 * (n - 16)], Z.E[a][2 * (n - 16) + 1]); OPQ(Z.Pb[2 * a + n - 16]); }
}
DEV void soft_check(Soft& Z, f32x4 (&U)[12], const f32x4 (&rr)[2], float& m, float& l, bool first) {
  if (__builtin_expect((__builtin_amdgcn_ballot_w64(!(Z.ls <= THR)) | __builtin_amdgcn_ballot_w64(Z.ls < (first ? LOW : 0.0f))) != 0, 0)) {
    float dl = __builtin_fmaxf(__builtin_fmaxf(__builtin_fmaxf(Z.V[0][0], Z.V[0][1]), __builtin_fmaxf(Z.V[0][2], Z.V[0][3])),
                               __builtin_fmaxf(__builtin_fmaxf(Z.V[1][0], Z.V[1][1]), __builtin_fmaxf(Z.V[1][2], Z.V[1][3])));
    dl = xg_max(dl);
    dl = __builtin_fmaxf(dl, first ? -3e38f : 0.f);
    const float al = __builtin_amdgcn_exp2f(__builtin_fminf(-dl, 0.f));
    m += dl; l *= al;
#pragma unroll
    for (int c = 0; c < 12; ++c) U[c] *= al;
#pragma unroll
    for (int e = 0; e < 8; ++e) Z.E[e >> 2][e & 3] = __builtin_amdgcn_exp2f(Z.V[e >> 2][e & 3] - dl);
    Z.ls = ((Z.E[0][0] + Z.E[0][1]) + (Z.E[0][2] + Z.E[0][3])) + ((Z.E[1][0] + Z.E[1][1]) + (Z.E[1][2] + Z.E[1][3]));
    Z.Pb[0] = cvtpk(Z.E[0][0] * rr[0][0], Z.E[0][1] * rr[0][1]);
    Z.Pb[1] = cvtpk(Z.E[0][2] * rr[0][2], Z.E[0][3] * rr[0][3]);
  }
  l = cut(l + Z.ls);
}
struct Slot { int hh, a, lo, hi; };
template <int I> struct Ic { static constexpr int v = I; };
template <int I, int N, class F> DEV void sfor(F&& f) { if constexpr (I < N) { f(Ic<I>{}); sfor<I + 1, N>(f); } }
constexpr Slot vslot(int n, int k) {
  switch (n * 2 + k) {
    case 24: return {0, 0, 0, 4};
    case 26: return {0, 0, 4, 6};
    case 28: return {0, 0, 6, 8};
    case 30: return {0, 0, 8, 13};
    case 32: return {0, 0, 13, 17};
    case 34: return {0, 0, 17, 18};  case 35: return {1, 0, 0, 3};
    case 36: return {1, 0, 3, 5};
    case 38: return {0, 1, 0, 4};
    case 40: return {0, 1, 4, 6};
    case 42: return {0, 1, 6, 8};    case 43: return {1, 0, 5, 6};
    case 44: return {0, 1, 8, 12};
    case 46: return {0, 1, 12, 18};
    case 48: return {1, 0, 6, 8};
    case 50: return {1, 1, 0, 4};
    case 52: return {1, 1, 4, 6};
    case 54: return {1, 1, 6, 8};
    case 56: return {1, 0, 8, 13};
    case 58: return {1, 0, 13, 18};
    case 60: return {1, 1, 8, 12};
    case 62: return {1, 1, 12, 18};
    default: return {0, 0, 0, 0};
  }
}
template <int SL> DEV void cstep(Cons& K, const ConsC& C, bool first) {
  u32x4 Xr[12], X[12];
  f32x4 rr[2], S[2][2];
  Soft Z[2];
  const f32x4 z4 = {0.f, 0.f, 0.f, 0.f};
#pragma unroll
  for (int n = 0; n < 4; ++n) { Xr[n] = row_read<SL>(C, n); SB(); }
  sfor<0, 48>([&](auto nc) __attribute__((always_inline)) {
    constexpr int n = decltype(nc)::v;
    if constexpr (n < 24) {
      constexpr int a = n >= 12, hh = n < 12 ? n & 1 : n >= 18, s = n < 12 ? n >> 1 : (n - 12) % 6;
      S[a][hh] = mfma32(Xr[6 * a + s], K.Ab[hh][s], s == 0 ? z4 : S[a][hh]);
      OPQ(S[a][hh]);
    } else {
      constexpr int o = n - 24, hh = o >= 16 || (o >= 8 && !(o & 1)), ct = o < 8 ? o : (o >= 16 ? o - 12 : ((o & 1) ? (o + 7) / 2 : (o - 8) / 2));
      K.U[hh][ct] = mfma32(X[ct], Z[hh].Pb, K.U[hh][ct]);
      OPQ(K.U[hh][ct]);
    }
    SB();


    if constexpr (n < 12 && !(n & 1)) Xr[n / 2 + 4] = row_read<SL>(C, n / 2 + 4);
    if constexpr (n == 9 || n == 11) Xr[n / 2 + 6] = row_read<SL>(C, n / 2 + 6);
    if constexpr (n == 1 || n == 5) rr[n >> 2] = *(lds_f32x4*)(C.smem + C.ra + (n >> 2) * 64 + SL * 256);
    if constexpr (n >= 18 && n < 30) X[n - 18] = col_read<SL>(C, n - 18);
    if (n == 23) soft_check(Z[0], K.U[0], rr, K.m[0], K.l[0], first);
    if (n == 31) soft_check(Z[1], K.U[1], rr, K.m[1], K.l[1], first);
    constexpr Slot v = vslot(n, 0), w = vslot(n, 1);
#pragma unroll
    for (int e = v.lo; e < v.hi; ++e) soft_op(Z[v.hh], S[v.a][v.hh], rr[v.a], K.m[v.hh], v.a, e);
#pragma unroll
    for (int e = w.lo; e < w.hi; ++e) soft_op(Z[w.hh], S[w.a][w.hh], rr[w.a], K.m[w.hh], w.a, e);
    SB();
  });
}
DEV u32x4 ucvt(const f32x4 (&U)[12], int s) {
  u32x4 r = cvt8(U[2 * s], U[2 * s + 1]);
  OPQ(r);
  return r;
}
DEV void cfinish(Cons& K, const ConsC& C, int h, int b, rsrc_t rws, unsigned ek) {
  lds_char* smem = C.smem;
  lds_char* wv = smem + C.wv;
  u32x2 Wh[36];
  const f32x4 z4 = {0.f, 0.f, 0.f, 0.f};
  u32x4 one, uo[6], ub[2];
  f32x4 ca[4], cb[4], fa4, fb4, cs, t[3], M;
  f32x2 ml;
  float mn, fa, fb, l;
  unsigned lse, o[6], ck, vo, fw, fr, ux, mx, cx;
#define WVF(n) u32x4{Wh[2 * (n)][0], Wh[2 * (n)][1], Wh[2 * (n) + 1][0], Wh[2 * (n) + 1][1]}
  auto own = [&](int s, int dt) __attribute__((always_inline)) {
    ca[dt] = mfma32(ub[s & 1], dt < 3 ? WVF(3 * s + (dt % 3)) : one, s == 0 ? z4 : ca[dt]);
    OPQ(ca[dt]); SB();
  };
  *(__attribute__((address_space(3))) f32x2*)(smem + C.ml) = f32x2{K.m[0], K.l[0]};
  SB();
  sfor<0, 6>([&](auto sc) __attribute__((always_inline)) {
    constexpr int s = decltype(sc)::v;
    if (s == 0) for (int c = 0; c < 6; ++c) Wh[c] = trread(wv + (c & 1) * 1536 + (c >> 1) * 32);
    SB();
    const u32x4 x = ucvt(K.U[0], s);
    SB();
    if (s > 0) own(s - 1, 0);
    *(lds_u32x4*)(smem + C.ex + s * 1024) = x;
    SB();
    if (s == 0) { one = u32x4{0x3F803F80u, 0x3F803F80u, 0x3F803F80u, 0x3F803F80u}; OPQ(one); SB(); }
#pragma unroll
    for (int c = 0; c < 6; ++c) {
      if (s < 5) Wh[6 * s + 6 + (c % 6)] = trread(wv + (s + 1) * 3072 + (c & 1) * 1536 + (c >> 1) * 32);
      SB();
      if (s == 5 && c == 0) { ux = C.ex + ((h & 1) ? -6144 : 6144); OPQ(ux); SB(); }
      if (s == 5 && c == 2) { mx = C.ml + ((h & 1) ? -512 : 512); OPQ(mx); SB(); }
      if (s == 5 && c == 4) { cx = C.ex + (LDS_LAT + 1024 - LDS_X - h * 5120); OPQ(cx); SB(); }
      if (s > 0 && (c & 1)) own(s - 1, 1 + (c >> 1));
    }
    ub[s & 1] = ucvt(K.U[1], s);
    SB();
  });
  bar();
  ml = *(__attribute__((address_space(3))) f32x2*)(smem + mx);
  SB();
#pragma unroll
  for (int k = 0; k < 6; ++k) uo[k] = *(lds_u32x4*)(smem + ux + k * 1024);
  cs = *(lds_f32x4*)(smem + cx);
  SB();
  own(5, 0);
  const int lane = laneid(), i = lane & 15, g = lane >> 4;
  fw = LDS_LAT + h * 256 + i * 4; OPQ(fw); SB();
  own(5, 1);
  fr = LDS_LAT + h * 256 + g * 16; OPQ(fr); SB();
  own(5, 2);
  vo = (lane >> 2) * 32768 + (lane & 3) * 16; OPQ(vo); SB();
  own(5, 3);
  sfor<0, 24>([&](auto nc) __attribute__((always_inline)) {
    constexpr int n = decltype(nc)::v, k = (n % 12) >> 1, dt = n < 12 ? ((n & 1) ? 0 : 3) : 1 + (n & 1);
    if (n == 0) { mn = cut(__builtin_fmaxf(K.m[1], ml[0])); SB(); }
    cb[dt] = mfma32(uo[k], dt < 3 ? WVF(3 * k + (dt % 3)) : one, k == 0 ? z4 : cb[dt]);
    OPQ(cb[dt]);
    SB();
    if (n == 0) { fa = cut(K.m[1] - mn); fb = cut(ml[0] - mn); }
    if (n == 1) { fa = cut(__builtin_amdgcn_exp2f(fa)); fb = cut(__builtin_amdgcn_exp2f(fb)); }
    if (n == 2) { l = cut(K.l[1] * fa); }
    if (n == 3) { l = cut(ml[1] * fb + l); }
    if (n == 4) { auto x = __builtin_amdgcn_permlane16_swap(f2u(l), f2u(l), false, false); l = cut(u2f(x[0]) + u2f(x[1])); }
    if (n == 5) { auto x = __builtin_amdgcn_permlane32_swap(f2u(l), f2u(l), false, false); l = cut(u2f(x[0]) + u2f(x[1])); }
    if (n == 6) { M[0] = cut(__builtin_amdgcn_rcpf(l)); l = cut(__builtin_amdgcn_logf(l)); }
    if (n == 7) { fa = cut(fa * M[0]); fb = cut(fb * M[0]); l = cut(mn + l); }
    if (n == 8) { *(lds_f32*)(smem + fw) = fa; *(lds_f32*)(smem + fw + 64) = fb; *(lds_f32*)(smem + fw + 128) = l; }
    if (n == 9) { fa4 = *(lds_f32x4*)(smem + fr); fb4 = *(lds_f32x4*)(smem + fr + 64); lse = f2u(*(lds_f32*)(smem + fr + 128 + (i & 3) * 4)); }
    if (n == 10) OPQ(cs);
    if (n >= 10 && n < 14) for (int r = 0; r < 3; ++r) { constexpr int m = 3 * ((n - 10) & 3); t[(m + r) >> 2][(m + r) & 3] = cut(__builtin_fmaf(ca[3][(m + r) & 3], cs[(m + r) >> 2], ca[(m + r) >> 2][(m + r) & 3])); }
    if (n >= 14 && n < 18) for (int r = 0; r < 3; ++r) { constexpr int m = 3 * ((n - 14) & 3); t[(m + r) >> 2][(m + r) & 3] = cut(t[(m + r) >> 2][(m + r) & 3] * fa4[(m + r) & 3]); }
    if (n == 18) for (int r = 0; r < 4; ++r) cb[0][r] = cut(__builtin_fmaf(cb[3][r], cs[0], cb[0][r]));
    if (n == 19) for (int r = 0; r < 4; ++r) M[r] = cut(cb[3][r] * fb4[r]);
    if (n == 20) for (int r = 0; r < 4; ++r) t[0][r] = cut(__builtin_fmaf(cb[0][r], fb4[r], t[0][r]));
    if (n == 21) { for (int r = 0; r < 4; ++r) t[1][r] = cut(__builtin_fmaf(M[r], cs[1], t[1][r])); }
    if (n == 22) { for (int r = 0; r < 4; ++r) t[2][r] = cut(__builtin_fmaf(M[r], cs[2], t[2][r])); }
    if (n == 23) { o[0] = cvtpk(t[0][0], t[0][1]); o[1] = cvtpk(t[0][2], t[0][3]); ck = lse + ek; OPQ(o[0]); OPQ(o[1]); OPQ(ck); SB(); ck += o[0] + o[1]; OPQ(ck); }
    SB();
  });
#pragma unroll
  for (int dt = 1; dt < 3; ++dt) {
#pragma unroll
    for (int r = 0; r < 4; ++r) t[dt][r] = cut(__builtin_fmaf(cb[dt][r], fb4[r], t[dt][r]));
    SB();
  }
#pragma unroll
  for (int n = 2; n < 6; ++n) o[n] = cvtpk(t[n >> 1][(n & 1) * 2], t[n >> 1][(n & 1) * 2 + 1]);
#undef WVF
  const u32x4 ra = {o[0], o[1], o[2], o[3]};
  const u32x4 rb = {o[4], o[5], lse, (o[2] + o[3] + ck) + o[4] + o[5]};
  const unsigned so = WS_REC + h * 524288 + b * 64;
  bst4<SC1>(ra, rws, vo, so);
  bst4<SC1>(rb, rws, vo, so + 16384);
  if (b >= L1_B0 && b < L2_B0 + 12) asm volatile("s_barrier" ::: "memory");
}
DEV void wv_colsum(lds_char* smem, int h) {
  const int lane = laneid(), i = lane & 15, g = lane >> 4;
  lds_char* wv = smem + LDS_WV + h * 18432 + (4 * g + (i >> 2)) * 96 + (i & 3) * 8;
  const f32x4 z4 = {0.f, 0.f, 0.f, 0.f};
  const u32x4 one = {0x3F803F80u, 0x3F803F80u, 0x3F803F80u, 0x3F803F80u};
  f32x4 c[3];
#pragma unroll
  for (int n = 0; n < 18; ++n) {
    const u32x2 a0 = trread(wv + (n / 3) * 3072 + (n % 3) * 32), a1 = trread(wv + (n / 3) * 3072 + 1536 + (n % 3) * 32);
    c[n % 3] = mfma32(one, u32x4{a0[0], a0[1], a1[0], a1[1]}, n < 3 ? z4 : c[n % 3]);
  }
  *(lds_f32x4*)(smem + LDS_LAT + 1024 + h * 1024 + lane * 16) = f32x4{c[0][0] * (-1.0f / 192.0f), c[1][0] * (-1.0f / 192.0f), c[2][0] * (-1.0f / 192.0f), 0.f};
}

DEV unsigned slice_off(int m, int lane, unsigned stride) {
  const unsigned o = m * 1024 + lane * 16, r = o / 96;
  return (8 * ((r >> 2) & 3) + 4 * (r >> 4) + (r & 3)) * stride + o % 96;
}
DEV u32x4 slice_frag(lds_char* a, int ks, int tt) {
  const u32x2 a0 = trread(a + ks * 3072 + tt * 32), a1 = trread(a + ks * 3072 + 1536 + tt * 32);
  return u32x4{a0[0], a0[1], a1[0], a1[1]};
}
struct AIn { u32x4 q[6]; };
DEV void a_issue(AIn& I, unsigned lbase, int h, int cs, const void* q_, const void* wq_, const void* wk_, const void* wm_) {
  const int lane = laneid();
  const rsrc_t rq = mkrsrc(q_, 6144u), rwq = mkrsrc(wq_, 73728u), rwk = mkrsrc(wk_, 73728u), rwm = mkrsrc(wm_, 73728u);
  const unsigned vl = lane * 16, vq = (lane & 15) * 384 + (lane >> 4) * 16;
  ldpl<0>(I.q[0], rq, vq); ldpl<64>(I.q[1], rq, vq); ldpl<128>(I.q[2], rq, vq);
  ldpl<192>(I.q[3], rq, vq); ldpl<256>(I.q[4], rq, vq); ldpl<320>(I.q[5], rq, vq);
  unsigned v1[3], vo[3];
#pragma unroll
  for (int m = 0; m < 3; ++m) { v1[m] = slice_off(m, lane, 96); vo[m] = slice_off(m, lane, 384); }
#pragma unroll
  for (int n = 0; n < 18; ++n) dma16(rwq, v1[n % 3], h * 18432 + (n / 3) * 3072, lbase + LDS_AWQ + n * 1024);
#pragma unroll
  for (int n = 0; n < 5; ++n) dma16(rwk, vl, h * 18432 + cs * 4608 + n * 1024, lbase + LDS_AWK + n * 1024);
#pragma unroll
  for (int n = 0; n < 18; ++n) dma16(rwm, vo[n % 3], (n / 3) * 12288 + cs * 96, lbase + LDS_AWM + n * 1024);
}
DEV void a_compute(AIn& I, lds_char* smem, int h, int cs, rsrc_t rws, unsigned ek) {
  const int lane = laneid(), i = lane & 15, g = lane >> 4;
  lds_char* a = smem + (4 * g + (i >> 2)) * 96 + (i & 3) * 8;
  const f32x4 z4 = {0.f, 0.f, 0.f, 0.f};
  f32x4 Q[3], M[3], TT[3];
  u32x4 W[18], V[18];
  u32x2 Kf[9];
  vmwait<23>();
#pragma unroll
  for (int s = 0; s < 6; ++s) OPQ(I.q[s]);
  SB();
#pragma unroll
  for (int n = 0; n < 18; ++n) W[n] = slice_frag(a + LDS_AWQ, n / 3, n % 3);
  SB();
  vmwait<18>();
#pragma unroll
  for (int n = 0; n < 9; ++n) Kf[n] = *(__attribute__((address_space(3))) u32x2*)(smem + LDS_AWK + i * 96 + g * 8 + (n / 3) * 1536 + (n % 3) * 32);
  SB();
#pragma unroll
  for (int n = 0; n < 18; ++n) Q[n % 3] = mfma32(W[n], I.q[n / 3], n < 3 ? z4 : Q[n % 3]);
  SB();
  vmwait<0>();
#pragma unroll
  for (int n = 0; n < 18; ++n) V[n] = slice_frag(a + LDS_AWM, n / 3, n % 3);
  SB();
#pragma unroll
  for (int dt = 0; dt < 3; ++dt) {
    const u32x2 qb = {cvtpk(Q[dt][0], Q[dt][1]), cvtpk(Q[dt][2], Q[dt][3])};
#pragma unroll
    for (int ct = 0; ct < 3; ++ct) TT[ct] = mfma16(Kf[3 * ct + dt], qb, dt == 0 ? z4 : TT[ct]);
  }
  SB();
#pragma unroll
  for (int n = 0; n < 18; ++n) M[n % 3] = mfma32(V[n], I.q[n / 3], n < 3 ? z4 : M[n % 3]);
  const float s1 = 1.4426950408889634f * 0.14433756729740643f;
  unsigned P[6];
#pragma unroll
  for (int n = 0; n < 6; ++n)
    P[n] = cvtpk((TT[n >> 1][(n & 1) * 2] + 0.5f * M[n >> 1][(n & 1) * 2]) * s1, (TT[n >> 1][(n & 1) * 2 + 1] + 0.5f * M[n >> 1][(n & 1) * 2 + 1]) * s1);
  const unsigned so = WS_A + h * 8192 + cs * 2048;
  bst4<SC1>(u32x4{P[0], P[1], P[2], P[0] + P[1] + P[2] + ek}, rws, lane * 16, so);
  bst4<SC1>(u32x4{P[4], P[5], P[3], P[4] + P[5] + P[3] + ek}, rws, lane * 16 + 1024, so);
}
template <int OFF> DEV void ldsc(u32x4& d, rsrc_t r, unsigned voff, unsigned soff) {
  asm volatile("buffer_load_dwordx4 %0, %1, %2, %3 offen offset:%4 sc1" : "=v"(d) : "v"(voff), "s"(r), "s"(soff), "n"(OFF) : "memory");
}
DEV void a_fetch(u32x4 (&G)[8], int h, rsrc_t rws) {
  const unsigned vo = laneid() * 16, s0 = WS_A + h * 8192, s1 = s0 + 4096;
  ldsc<0>(G[0], rws, vo, s0); ldsc<1024>(G[1], rws, vo, s0); ldsc<2048>(G[2], rws, vo, s0); ldsc<3072>(G[3], rws, vo, s0);
  ldsc<0>(G[4], rws, vo, s1); ldsc<1024>(G[5], rws, vo, s1); ldsc<2048>(G[6], rws, vo, s1); ldsc<3072>(G[7], rws, vo, s1);
}
DEV void a_poll(u32x4 (&G)[8], int h, rsrc_t rws, unsigned ek) {
  for (int spin = 0;; ++spin) {
    if (spin) a_fetch(G, h, rws);
    vmwait<0>();
#pragma unroll
    for (int n = 0; n < 8; ++n) OPQ(G[n]);
    bool bad = false;
#pragma unroll
    for (int n = 0; n < 8; ++n) bad |= G[n][0] + G[n][1] + G[n][2] + ek != G[n][3];
    if (__builtin_amdgcn_ballot_w64(bad) == 0 || spin >= SPIN_MAX) break;
  }
}
DEV void a_centre(const u32x4 (&G)[8], lds_char* smem, int h) {
  const int lane = laneid(), i = lane & 15, g = lane >> 4;
  float sm[2] = {0.f, 0.f};
#pragma unroll
  for (int n = 0; n < 24; ++n) sm[n & 1] = dot2(G[n / 3][n % 3], 0x3F803F80u, sm[n & 1]);
  const float mu = xg_sum(sm[0] + sm[1]) * (-1.0f / 192.0f);
  const f32x4 mu4 = {mu, mu, mu, mu};
  const int d = lane & 3;
  const u32x2 I4 = {d == 0 ? 0x00003F80u : (d == 1 ? 0x3F800000u : 0u), d == 2 ? 0x00003F80u : (d == 3 ? 0x3F800000u : 0u)};
  lds_char* dst = smem + LDS_AF + h * 6144 + ((g >> 1) * 16 + i) * 16 + (g & 1) * 8;
#pragma unroll
  for (int s = 0; s < 12; ++s) {
    const int cs = s / 3, ct = s % 3;
    const u32x2 v = ct == 0 ? u32x2{G[2 * cs][0], G[2 * cs][1]} : (ct == 1 ? u32x2{G[2 * cs][2], G[2 * cs + 1][2]} : u32x2{G[2 * cs + 1][0], G[2 * cs + 1][1]});
    const f32x4 c = mfma4(I4, v, mu4);
    *(__attribute__((address_space(3))) u32x2*)(dst + s * 512) = u32x2{cvtpk(c[0], c[1]), cvtpk(c[2], c[3])};
  }
}
DEV float quads_sum(float x) { x += dppmov<0x124>(x); return x + dppmov<0x128>(x); }
DEV float quads_max(float x) { x = __builtin_fmaxf(x, dppmov<0x124>(x)); return __builtin_fmaxf(x, dppmov<0x128>(x)); }
DEV void reduce1(lds_char* smem, int r, int p, rsrc_t rws, unsigned ek) {
  const int lane = laneid(), i = lane & 15, g = lane >> 4, d = lane & 3;
  const unsigned m0 = d == 0 ? 0x0000FFFFu : (d == 1 ? 0xFFFF0000u : 0u), m1 = d == 2 ? 0x0000FFFFu : (d == 3 ? 0xFFFF0000u : 0u);
  u32x4 RA[4], RB[4];
  const unsigned so = WS_REC + r * 32768 + p * 4096;
  for (int spin = 0;; ++spin) {
    asm volatile("" ::: "memory");
#pragma unroll
    for (int j = 0; j < 4; ++j) { RB[j] = bld4<SC1>(rws, lane * 16 + j * 1024, so + 16384); RA[j] = bld4<SC1>(rws, lane * 16 + j * 1024, so); }
    bool bad = false;
#pragma unroll
    for (int j = 0; j < 4; ++j) bad |= (RA[j][0] + RA[j][1] + RA[j][2]) + (RA[j][3] + RB[j][0] + RB[j][1]) + RB[j][2] + ek != RB[j][3];
    if (__builtin_amdgcn_ballot_w64(bad) == 0 || spin >= SPIN_MAX) break;
  }
  const float M = xg_max(quads_max(__builtin_fmaxf(__builtin_fmaxf(u2f(RB[0][2]), u2f(RB[1][2])), __builtin_fmaxf(u2f(RB[2][2]), u2f(RB[3][2])))));
  const f32x4 z4 = {0.f, 0.f, 0.f, 0.f};
  f32x4 acc[3] = {z4, z4, z4};
  float L = 0.f;
#pragma unroll
  for (int j = 0; j < 4; ++j) {
    const float wf = __builtin_amdgcn_exp2f(u2f(RB[j][2]) - M);
    const unsigned wb = cvtpk(wf, wf);
    L += u2f(wb & 0xFFFF0000u);
    const u32x2 dg = {wb & m0, wb & m1};
    acc[0] = mfma4(dg, u32x2{RA[j][0], RA[j][1]}, acc[0]);
    acc[1] = mfma4(dg, u32x2{RA[j][2], RA[j][3]}, acc[1]);
    acc[2] = mfma4(dg, u32x2{RB[j][0], RB[j][1]}, acc[2]);
  }
  L = xg_sum(quads_sum(L));
  float u[6];
  f32x4 v;
#pragma unroll
  for (int k = 0; k < 6; ++k) {
    auto t = __builtin_amdgcn_permlane32_swap(f2u(acc[k % 3][k / 3]), f2u(acc[k % 3][2 + k / 3]), false, false);
    u[k] = u2f(t[0]) + u2f(t[1]);
  }
#pragma unroll
  for (int k = 0; k < 3; ++k) {
    auto t = __builtin_amdgcn_permlane16_swap(f2u(u[k]), f2u(u[k + 3]), false, false);
    v[k] = quads_sum(u2f(t[0]) + u2f(t[1]));
  }
  v[3] = 0.f;
  if (i < 4) *(lds_f32x4*)(smem + LDS_RED + p * 256 + g * 64 + d * 16) = v;
  if (lane < 4) *(__attribute__((address_space(3))) f32x2*)(smem + LDS_RED + 1024 + p * 32 + d * 8) = f32x2{M, L};
  bar();
  if (p == 0) {
    const f32x4 x = *(lds_f32x4*)(smem + LDS_RED + (i >> 2) * 256 + g * 64 + d * 16);
    const f32x2 ml = *(__attribute__((address_space(3))) f32x2*)(smem + LDS_RED + 1024 + (i >> 2) * 32 + g * 8);
    const float f = __builtin_amdgcn_exp2f(ml[0] - quads_max(ml[0]));
    const float il = __builtin_amdgcn_rcpf(quads_sum(ml[1] * f));
    u32x4 o;
#pragma unroll
    for (int k = 0; k < 3; ++k) { const float c = quads_sum(x[k] * f) * il; o[k] = cvtpk(c, dppmov<0xB1>(c)); }
    o[3] = o[0] + o[1] + o[2] + ek;
    if (i == 0 || i == 2) bst4<SC1>(o, rws, (d >> 1) * 64 + g * 16, WS_CTX + r * 128);
  }
}
DEV void reduce2_issue(u32x2& qr, unsigned lbase, int nt, const void* q_, const void* wo_) {
  const int lane = laneid();
  const rsrc_t rq = mkrsrc(q_, 6144u), rwo = mkrsrc(wo_, 73728u);
  const unsigned vo = (lane >> 1) * 384 + (lane & 1) * 16, vq = (lane & 15) * 384 + (lane >> 4) * 8;
#pragma unroll
  for (int n = 0; n < 6; ++n) dma16(rwo, vo, n * 12288 + nt * 32, lbase + LDS_WO + n * 1024);
  asm volatile("buffer_load_dwordx2 %0, %1, %2, %3 offen" : "=v"(qr) : "v"(vq), "s"(rq), "s"(nt * 32) : "memory");
}
DEV void reduce2(u32x2& qr, lds_char* smem, int nt, rsrc_t rws, unsigned ek, void* out_) {
  const int lane = laneid(), i = lane & 15, g = lane >> 4, rho = i >> 2;
  vmwait<0>();
  OPQ(qr);
  SB();
  lds_char* a0 = smem + LDS_WO + (16 * (rho >> 1) + 4 * g + (rho & 1)) * 32 + (i & 3) * 8;
  lds_char* a1 = smem + LDS_WO + (32 + 4 * g + (rho & 1)) * 32 + (i & 3) * 8;
  u32x4 W[8], G[8];
#pragma unroll
  for (int m = 0; m < 8; ++m) {
    const u32x2 x0 = trread(a0 + ((m >> 1) * 48 + 2 * (m & 1)) * 32), x1 = trread(a1 + ((m >> 1) * 48 + 2 * (m & 1)) * 32);
    W[m] = u32x4{x0[0], x0[1], x1[0], x1[1]};
    OPQ(W[m]);
  }
  const unsigned vo = (i >> 2) * 512 + g * 128 + (i & 3) * 16;
  for (int spin = 0;; ++spin) {
    asm volatile("" ::: "memory");
#pragma unroll
    for (int m = 0; m < 8; ++m) G[m] = bld4<SC1>(rws, vo + (m & 1) * 64 + ((m >> 1) & 1) * 2048, WS_CTX + (m >> 2) * 4096);
    bool bad = false;
#pragma unroll
    for (int m = 0; m < 8; ++m) bad |= G[m][0] + G[m][1] + G[m][2] + ek != G[m][3];
    if (__builtin_amdgcn_ballot_w64(bad) == 0 || spin >= SPIN_MAX) break;
  }
  f32x4 c[2] = {{u2f(qr[0] << 16), u2f(qr[0] & 0xFFFF0000u), u2f(qr[1] << 16), u2f(qr[1] & 0xFFFF0000u)}, {0.f, 0.f, 0.f, 0.f}};
#pragma unroll
  for (int m = 0; m < 8; ++m) { G[m][3] = 0u; c[m & 1] = mfma32(W[m], G[m], c[m & 1]); }
  const f32x4 o = c[0] + c[1];
  __builtin_amdgcn_raw_buffer_store_b64(u32x2{cvtpk(o[0], o[1]), cvtpk(o[2], o[3])}, mkrsrc(out_, 6144u), i * 384 + g * 8, nt * 32, 0);
  if (nt == 0 && lane == 0) __builtin_amdgcn_raw_buffer_store_b32(ek - KCHK + 1u, rws, 0, 0, 0);
}

__global__ void __launch_bounds__(512, 2)
m2f_main(const void* __restrict__ fhi_, const void* __restrict__ flo_, const void* __restrict__ wlat_,
         const void* __restrict__ wv_, const void* __restrict__ q_, const void* __restrict__ wq_, const void* __restrict__ wk_,
         const void* __restrict__ wm_, const void* __restrict__ wo_, void* __restrict__ ws_, void* __restrict__ out_) {
  __shared__ __attribute__((aligned(16))) char smem_[LDS_TOTAL];
  lds_char* smem = (lds_char*)smem_;
  const int w = __builtin_amdgcn_readfirstlane(__builtin_amdgcn_workitem_id_x() >> 6);
  const int b = __builtin_amdgcn_workgroup_id_x();

  if (w < 4) {
    const int p = w, lane = laneid(), i = lane & 15, g = lane >> 4;
    __builtin_amdgcn_s_setprio(PRIO_P);
    const rsrc_t rwl = mkrsrc(wlat_, 73728u);
    ProdC C;
    C.smem = smem;
    C.rhi = mkrsrc(fhi_, 100663296u);
    C.rlo = mkrsrc(flo_, 25165824u);
    C.rwv = mkrsrc(wv_, 73728u);
    const unsigned j = lane >> 3, c7 = lane & 7;
    const unsigned lbase = (unsigned)(uintptr_t)smem;
    C.voH = j * 384 + c7 * 16;
    C.vw = lane * 16;
    C.lds0 = lbase + LDS_LO + p * 1536;
    C.ldsv = lbase + LDS_WV;
#pragma unroll
    for (int k = 0; k < 2; ++k) {
      const unsigned o = k * 1024 + (lane & (k ? 31 : 63)) * 16, px = o / 384, pc = (o % 384) / 16;
      C.vd[k] = px * 384 + ((pc ^ (2 * p + (px >> 1))) * 16);
    }
    const unsigned mr = (b & 7) * 32 + (b >> 7), mc = (b >> 3) & 15;
    const unsigned soL = mr * 98304u + mc * 6144u + p * 1536u, soH = mr * 393216u + mc * 12288u + p * 3072u;
    Prod P;
    dma24(C.rlo, C.vd[0], C.vd[1], soL, C.lds0);
    dma24(C.rlo, C.vd[0], C.vd[1], soL + LS, C.lds0 + 6144);
    dma24(C.rlo, C.vd[0], C.vd[1], soL + 2 * LS, C.lds0 + 2 * 6144);
    SB();
    if (b < 16) bar();
    {
      unsigned vo[3];
#pragma unroll
      for (int m = 0; m < 3; ++m) vo[m] = slice_off(m, lane, 384);
      const unsigned so1 = soH + 196608;
#define WGRP(M) for (int n = 0; n < 3; ++n) dma16(rwl, vo[n], M * 12288 + p * 96, lbase + LDS_WL + p * 18432 + (3 * M + n) * 1024); \
      ld_hi1<M>(P.ring[0][M], C, soH, so1);
      WGRP(0) WGRP(1) WGRP(2) WGRP(3) WGRP(4) WGRP(5)
#undef WGRP
    }
    SB();
    {
      const int d = lane & 3;
      C.I4[0] = d == 0 ? 0x00003F80u : (d == 1 ? 0x3F800000u : 0u);
      C.I4[1] = d == 2 ? 0x00003F80u : (d == 3 ? 0x3F800000u : 0u);
#pragma unroll
      for (int ab = 0; ab < 2; ++ab) {
        C.xw[ab] = LDS_X + (16 * p + 8 * ab + j) * 128 + ((c7 ^ (((j >> 1) & 1) * 4 + (j >> 2) * 2)) * 16);
        C.br[ab] = LDS_LO + i * 384 + (((4 * ab + g) ^ (i >> 1)) * 16);
        C.lw[ab] = LDS_LAT + i * 768 + (((12 * p + 4 * ab + g) ^ (i & 7)) * 16);
        C.lr[ab] = LDS_LAT + (4 * p + (j >> 1)) * 768 + (((2 * c7 + ab) ^ ((4 * p + (j >> 1)) & 7)) * 16);
      }
      C.odd = lane & 1;
      C.rw = c7 < 2 ? LDS_R + (16 * p + 8 * (lane & 1) + j) * 4 : LDS_ML + lane * 4;
#pragma unroll
      for (int ab = 0; ab < 2; ++ab) { OPQ(C.xw[ab]); OPQ(C.br[ab]); OPQ(C.lw[ab]); OPQ(C.lr[ab]); OPQ(C.vd[ab]); }
      OPQ(C.rw); OPQ(C.vw);
    }
    SB();
    unsigned ek = epoch(ws_);
    SB();
    vmwait<24>();
    asm volatile("" : "+s"(ek));
    bar();
    {
      lds_char* a = smem + LDS_WL + p * 18432 + (4 * g + (i >> 2)) * 96 + (i & 3) * 8;
      const f32x4 z4 = {0.f, 0.f, 0.f, 0.f};
      const unsigned sn0 = soH + HS, sn1 = sn0 + 196608;
      u32x4 B[12];
      f32x4 G0[3], G1[3];
#pragma unroll
      for (int n = 0; n < 12; ++n) B[n] = lo_read(C, n & 1, n >> 1);
      SB();
#define WSL(KS, NW, ISSUE) if (KS < 6) { vmwait<(NW)>(); for (int tt = 0; tt < 3; ++tt) P.W[tt * 6 + (KS < 6 ? KS : 0)] = slice_frag(a, KS, tt); SB(); } ISSUE; SB(); \
      if (KS > 0) for (int tt = 0; tt < 3; ++tt) { constexpr int k_ = KS > 0 ? KS - 1 : 0; \
        G0[tt] = mfma32(P.W[tt * 6 + k_], B[2 * k_], k_ == 0 ? z4 : G0[tt]); G1[tt] = mfma32(P.W[tt * 6 + k_], B[2 * k_ + 1], k_ == 0 ? z4 : G1[tt]); \
        OPQ(G0[tt]); OPQ(G1[tt]); SB(); }
      WSL(0, 21, dma24(C.rlo, C.vd[0], C.vd[1], soL + 3 * LS, C.lds0 + 3 * 6144); ld_hi1<0>(P.ring[1][0], C, sn0, sn1))
      WSL(1, 20, ld_hi1<1>(P.ring[1][1], C, sn0, sn1))
      WSL(2, 17, ld_hi1<2>(P.ring[1][2], C, sn0, sn1))
      WSL(3, 14, ld_hi1<3>(P.ring[1][3], C, sn0, sn1))
      WSL(4, 11, ld_hi1<4>(P.ring[1][4], C, sn0, sn1))
      WSL(5, 8, ld_hi1<5>(P.ring[1][5], C, sn0, sn1))
      WSL(6, 0, )
#undef WSL
      lat_write(G0, C, LDS_LT0 - LDS_LAT);
      lat_write(G1, C, 12288);
      bar();
#pragma unroll
      for (int k = 0; k < 6; ++k) P.lat[k >> 1][k & 1] = lat_read(C, LDS_LT0 - LDS_LAT, k);
      SB();
    }
    pstep<0, true, true, true, 0, 13, false, 2>(P, C, true, soH + 2 * HS, soL + 4 * LS, 0);
    vmwait<14>();
    bar();
    pstep<1, true, true, true, 0, 13, true>(P, C, true, soH + 4 * HS, soL + 6 * LS, 0);
    vmwait<22>(); bar();
#pragma unroll 1
    for (int u = 0; u < 3; ++u) {
      const unsigned so = soH + 5 * HS + u * (4 * HS), sl = soL + 7 * LS + u * (4 * LS);
      pstep<2, true, true, true, 0, 17>(P, C, true, so, sl, 0); vmwait<22>(); bar();
      pstep<3, true, true, true, 0, 19>(P, C, u < 2, so + HS, sl + LS, 0); vmwait<20>(); bar();
      pstep<0, true, true, true, 0, 19>(P, C, u < 2, so + 2 * HS, sl + 2 * LS, 0); vmwait<18>(); bar();
      if (u == 2) C.rhi = mkrsrc(fhi_, 0u);
      pstep<1, true, true, true, 0, 17>(P, C, u < 2, so + 3 * HS, sl + 3 * LS, 0); vmwait<22>(); bar();
    }
    pstep<2, false, false, true, 9, 17>(P, C, false, 0, 0, p * 1024); bar();
    pstep<3, false, false, false, -9, 29>(P, C, false, 0, 0, p * 1024 + 36864); vmwait<0>(); bar();
    __builtin_amdgcn_s_setprio(0);
    wv_colsum(smem, p);
    OPQ(P.ring[0][0]); OPQ(P.ring[0][1]); OPQ(P.ring[0][2]); OPQ(P.ring[0][3]); OPQ(P.ring[0][4]); OPQ(P.ring[0][5]);
    const bool l2 = p == 0 && b >= L2_B0 && b < L2_B0 + 12;
    u32x2 qr;
    if (l2) reduce2_issue(qr, lbase, b - L2_B0, q_, wo_);
    bar();
    if (b >= L1_B0 && b < L2_B0 + 12) asm volatile("s_barrier" ::: "memory");
    if (b >= L1_B0 && b < L1_B0 + 64) reduce1(smem, b - L1_B0, p, mkrsrc(ws_, WS_BYTES), ek);
    if (l2) reduce2(qr, smem, b - L2_B0, mkrsrc(ws_, WS_BYTES), ek, out_);
    asm volatile(".Lm2f_end:");
  } else {
    const int h = w - 4, ph = h & 1, lane = laneid(), i = lane & 15, g = lane >> 4;
    const rsrc_t rws = mkrsrc(ws_, WS_BYTES);
    Cons K;
    __builtin_amdgcn_s_setprio(PRIO_C);
    unsigned ek, pf;
    {
      const unsigned n = ((b >> 3) - 2) * 4 + h, h0 = (b & 7) >> 2, cs = b & 3;
      const void* code;
      unsigned clen;
      asm volatile("s_getpc_b64 %0\n.Lm2f_pc:\n\ts_mov_b32 %1, .Lm2f_end-.Lm2f_pc" : "=s"(code), "=s"(clen));
      const void* src = n < 10 ? wq_ : (n < 16 ? wm_ : (n < 20 ? wk_ : (n < 24 ? q_ : code)));
      const unsigned so = n < 5 ? h0 * 18432 + n * 4096 : (n < 10 ? h0 * 18432 + 36864 + (n - 5) * 4096 : (n < 16 ? (n - 10) * 12288 + ((cs * 96) >> 6) * 64
                        : (n < 20 ? h0 * 18432 + ((n >> 1) & 1) * 36864 + cs * 4608 + (n & 1) * 4096 : (n & (n < 24 ? 1 : 7)) * 4096)));
      const unsigned vo = n >= 10 && n < 16 ? (lane >> 1) * 384 + (lane & 1) * 64 : lane * 64;
      asm volatile("buffer_load_dword %0, %1, %2, %3 offen" : "=v"(pf) : "v"(vo), "s"(mkrsrc(src, n < 20 ? 73728u : (n < 22 ? 6144u : (n - 24 < NPF ? clen : 0u)))), "s"(so) : "memory");
    }
    if (b < 16 && h == 0) {
      AIn I;
      a_issue(I, (unsigned)(uintptr_t)smem, b >> 2, b & 3, q_, wq_, wk_, wm_);
      ek = epoch(ws_);
      asm volatile("s_barrier" ::: "memory");
      a_compute(I, smem, b >> 2, b & 3, rws, ek);
    } else {
      if (b < 16) bar();
      ek = epoch(ws_);
    }
    asm volatile("" : "+s"(ek));
    u32x4 G[8];
    bar();
    ConsC C;
    C.smem = smem;
    {
      const unsigned f = ((i >> 1) & 1) * 4 + ((i >> 2) & 1) * 2, rp = i >> 2, cp = i & 3;
#pragma unroll
      for (int s = 0; s < 2; ++s) C.xr[s] = LDS_X + (32 * ph + i) * 128 + (((4 * s + g) ^ f) * 16);
#pragma unroll
      for (int c = 0; c < 4; ++c)
        C.xt[c] = LDS_X + (32 * ph + 4 * g + rp) * 128 + (((2 * c + (cp >> 1)) ^ ((rp >> 1) * 4 + (g & 1) * 2)) * 16) + (cp & 1) * 8;
      C.ra = LDS_R + (32 * ph + 4 * g) * 4;
#pragma unroll
      for (int s = 0; s < 4; ++s) { OPQ(C.xr[s & 1]); OPQ(C.xt[s]); }
      OPQ(C.ra);
      C.wv = LDS_WV + h * 18432 + (4 * g + (i >> 2)) * 96 + (i & 3) * 8; C.ex = LDS_X + h * 6144 + lane * 16; C.ml = LDS_ML + h * 512 + lane * 8;
      OPQ(C.wv); OPQ(C.ex); OPQ(C.ml);
    }
#pragma unroll
    for (int c = 0; c < 24; ++c) { K.U[c / 12][c % 12] = f32x4{0.f, 0.f, 0.f, 0.f}; OPQ(K.U[c / 12][c % 12]); }
    K.m[0] = K.m[1] = K.l[0] = K.l[1] = 0.f;
    SB();
    bar();
    a_fetch(G, h, rws);
    a_poll(G, h, rws, ek);
    OPQ(pf);
    a_centre(G, smem, h);
#pragma unroll 1
    for (int u = 0; u < 8; ++u) {
      bar();
      if (__builtin_expect(u == 0, 0)) {
        lds_char* af = smem + LDS_AF + laneid() * 16;
#pragma unroll
        for (int s = 0; s < 12; ++s) K.Ab[s & 1][s >> 1] = *(lds_u32x4*)(af + (h ^ (s & 1) ^ 1) * 6144 + (s >> 1) * 1024);
        SB();
      }
      cstep<0>(K, C, u == 0);
      bar();
      cstep<1>(K, C, false);
    }
    cfinish(K, C, h, b, rws, ek);
  }
}

extern "C" void kernel_launch(void* const* d_in, const int* in_sizes, int n_in, void* d_out, int out_size, void* d_ws,
                              size_t ws_size, hipStream_t stream) {
  m2f_main<<<256, 512, 0, stream>>>(d_in[1], d_in[2], d_in[3], d_in[6], d_in[0], d_in[4], d_in[5], d_in[8], d_in[7], d_ws, d_out);
}
```
